```python
import torch

def _softmax(x, axis=-1):
    return torch.softmax(x, dim=axis)
def _gelu(x):
    return torch.nn.functional.gelu(x, approximate="tanh")
def _silu(x):
    return torch.nn.functional.silu(x)
def _sigmoid(x):
    return torch.sigmoid(x)
def _layernorm(x, axis=-1, eps=1e-5):
    m = x.mean(dim=axis, keepdim=True)
    v = x.var(dim=axis, keepdim=True, unbiased=False)
    return (x - m) / torch.sqrt(v + eps)
def _scatter_add(tgt, idx, src, axis=0):
    return tgt.clone().index_add_(axis, idx, src)
def _segment_sum(src, seg, n):
    out = torch.zeros((n,) + src.shape[1:], dtype=src.dtype)
    return out.index_add_(0, seg, src)
def _pad(x, pw):
    flat = tuple(v for pair in reversed(pw) for v in pair)
    return torch.nn.functional.pad(x, flat)

kernel_name = 'sequential_preLN__ssm_conv_s4__moe_arch_mod__prompt'

def setup_inputs():

    _w = (lambda a: (lambda t: t.to(torch.bfloat16) if t.is_floating_point() else t)(torch.as_tensor(a)))
    def _A_setup():
        B, L, D, N = 1, 2048, 640, 192
        A_re = -(lambda _x: torch.exp(torch.as_tensor(_x)))((lambda *s: torch.randn(*s, dtype=torch.bfloat16) * 6.4)(D, N))
        A_im = torch.pi * (lambda *s: torch.randn(*s, dtype=torch.bfloat16) * 0.05)(D, N)
        B_re = (lambda *s: torch.randn(*s, dtype=torch.bfloat16) * 0.05)(D, N)
        B_im = (lambda *s: torch.randn(*s, dtype=torch.bfloat16) * 0.05)(D, N)
        C_re = (lambda *s: torch.randn(*s, dtype=torch.bfloat16) * 0.05)(D, N)
        C_im = (lambda *s: torch.randn(*s, dtype=torch.bfloat16) * 0.05)(D, N)
        log_dt = (lambda *s: torch.randn(*s, dtype=torch.bfloat16) * 0.05)(D) * 0.5 - 4.0
        Dskip = (lambda *s: torch.randn(*s, dtype=torch.bfloat16) * 0.05)(D)
        return {
            "u": _w((lambda *s: torch.randn(*s, dtype=torch.bfloat16) * 0.05)(B, L, D)),
            "A_re": _w(A_re),
            "A_im": _w(A_im),
            "B_re": _w(B_re),
            "B_im": _w(B_im),
            "C_re": _w(C_re),
            "C_im": _w(C_im),
            "log_dt": _w(log_dt),
            "Dskip": _w(Dskip),
        }
    _A_in = _A_setup()

    def _B_setup():
        B, S, D, F, C = 1, 2048, 640, 1536, 1
        return {
            "x": _w((lambda *s: torch.randn(*s, dtype=torch.bfloat16) * 0.05)(B, S, D)),
            "w_r": _w((lambda *s: torch.randn(*s, dtype=torch.bfloat16) * 0.05)(D)),
            "w_gate": _w((lambda *s: torch.randn(*s, dtype=torch.bfloat16) * 0.05)(D, F)),
            "w_up": _w((lambda *s: torch.randn(*s, dtype=torch.bfloat16) * 0.05)(D, F)),
            "w_down": _w((lambda *s: torch.randn(*s, dtype=torch.bfloat16) * 0.05)(F, D)),
        }
    _B_in = _B_setup()

    return {
        "A_u": _A_in["u"],
        "A_A_re": _A_in["A_re"],
        "A_A_im": _A_in["A_im"],
        "A_B_re": _A_in["B_re"],
        "A_B_im": _A_in["B_im"],
        "A_C_re": _A_in["C_re"],
        "A_C_im": _A_in["C_im"],
        "A_log_dt": _A_in["log_dt"],
        "A_Dskip": _A_in["Dskip"],
        "B_x": _B_in["x"],
        "B_w_r": _B_in["w_r"],
        "B_w_gate": _B_in["w_gate"],
        "B_w_up": _B_in["w_up"],
        "B_w_down": _B_in["w_down"],
    }

def reference(A_u, A_A_re, A_A_im, A_B_re, A_B_im, A_C_re, A_C_im, A_log_dt, A_Dskip, B_x, B_w_r, B_w_gate, B_w_up, B_w_down):
    def _A_ref(u, A_re, A_im, B_re, B_im, C_re, C_im, log_dt, Dskip):
        Bsz, L, D = u.shape
        dt = torch.exp(log_dt)[:, None]
        A = A_re + 1j * A_im
        Bc = B_re + 1j * B_im
        Cc = C_re + 1j * C_im
        logdA = dt * A
        dB = dt * Bc
        l = (lambda x, dt: x.to(dt))(torch.arange(L), A_re.dtype)
        P = torch.exp(logdA[:, :, None] * l[None, None, :])
        K = torch.einsum("dn,dnl,dn->dl", Cc, P, dB).real
        L2 = 2 * L
        Kp = _pad(K, ((0, 0), (0, L)))
        up = (lambda x, *d: x.permute(*d))(u, 0, 2, 1)
        up = _pad(up, ((0, 0), (0, 0), (0, L)))
        Kf = (lambda x, axes=(-2,-1): torch.fft.rfft2(x.float(), dim=axes))(Kp[:, None, :], axes=(-2, -1))
        Uf = (lambda x, axes=(-2,-1): torch.fft.rfft2(x.float(), dim=axes))(up[:, :, None, :], axes=(-2, -1))
        Yf = Uf * Kf[None, :, :, :]
        y = (lambda x, s, axes=(-2,-1): torch.fft.irfft2(x, s=s, dim=axes).to(torch.bfloat16))(Yf, (1, L2), axes=(-2, -1))[:, :, 0, :L]
        y = (lambda x, *d: x.permute(*d))(y, 0, 2, 1) + u * Dskip[None, None, :]
        return y

    def _B_ref(x, w_r, w_gate, w_up, w_down):
        B, S, D = x.shape
        C = 1
        r = x @ w_r
        idx = torch.argsort(-r, axis=-1)[:, :C]
        idx_e = (lambda x, n, axis: torch.repeat_interleave(x, n, dim=axis))(idx.reshape(B, C, 1), D, axis=2)
        x_sel = (lambda x, i, axis: torch.take_along_dim(x, i, dim=axis))(x, idx_e, axis=1)
        g = _silu(x_sel @ w_gate)
        u = x_sel @ w_up
        y = (g * u) @ w_down
        r_sel = _sigmoid((lambda x, i, axis: torch.take_along_dim(x, i, dim=axis))(r, idx, axis=1))
        y = y * r_sel.reshape(B, C, 1)
        b_off = (torch.arange(B) * S).reshape(B, 1)
        flat_idx = (idx + b_off).reshape(B * C)
        zero = (x * 0.0).reshape(B * S, D)
        upd = _scatter_add(zero, flat_idx, y.reshape(B * C, D), axis=0)
        return x + upd.reshape(B, S, D)

    x = A_u
    h = x + _A_ref(_layernorm(x, axis=-1), A_A_re, A_A_im, A_B_re, A_B_im, A_C_re, A_C_im, A_log_dt, A_Dskip)
    h2 = h + _B_ref(_layernorm(h, axis=-1), B_w_r, B_w_gate, B_w_up, B_w_down)
    return h2
```

```cpp
#include <hip/hip_runtime.h>
#include <hip/hip_bf16.h>
#include <stdint.h>

#define L_ 2048
#define D_ 640
#define N_ 192
#define F_ 1536

typedef unsigned short u16;
__device__ __forceinline__ float bf2f(u16 v) { return __uint_as_float(((unsigned)v) << 16); }
__device__ __forceinline__ u16 f2bf(float f) {
    unsigned u = __float_as_uint(f);
    if ((u & 0x7fffffffu) > 0x7f800000u) return 0x7fc0;
    return (u16)((u + 0x7fffu + ((u >> 16) & 1)) >> 16);
}
__device__ __forceinline__ float rbf(float f) { return bf2f(f2bf(f)); }

__device__ __forceinline__ float wave_sum(float v) {
    for (int o = 32; o > 0; o >>= 1) v += __shfl_xor(v, o);
    return v;
}

__device__ __forceinline__ void ln_stats(const float (&x)[10], float& m, float& s) {
    float sum = 0.f;
    for (int i = 0; i < 10; i++) sum += x[i];
    sum = wave_sum(sum);
    float mean = sum * (1.0f / D_);
    float ss = 0.f;
    for (int i = 0; i < 10; i++) { float d = x[i] - mean; ss += d * d; }
    ss = wave_sum(ss);
    float var = ss * (1.0f / D_);
    m = rbf(mean);
    float v = rbf(var);
    s = rbf(sqrtf(rbf(v + 1e-5f)));
}

__global__ void k_ln1(const u16* __restrict__ x, u16* __restrict__ u, float* __restrict__ uT) {
    int row = blockIdx.x * 4 + (threadIdx.x >> 6);
    int lane = threadIdx.x & 63;
    float v[10];
    for (int i = 0; i < 10; i++) v[i] = bf2f(x[row * D_ + lane + 64 * i]);
    float m, s;
    ln_stats(v, m, s);
    for (int i = 0; i < 10; i++) {
        float r = rbf(rbf(v[i] - m) / s);
        u[row * D_ + lane + 64 * i] = f2bf(r);
        uT[(lane + 64 * i) * L_ + row] = r;
    }
}

__global__ void k_kgen(const u16* Are, const u16* Aim, const u16* Bre, const u16* Bim, const u16* Cre, const u16* Cim,
                       const u16* logdt, float* __restrict__ K) {
    int d = blockIdx.x;
    __shared__ float ar[N_], ai[N_], wr[N_], wi[N_];
    float dt = rbf(expf(bf2f(logdt[d])));
    for (int n = threadIdx.x; n < N_; n += blockDim.x) {
        ar[n] = dt * bf2f(Are[d * N_ + n]);
        ai[n] = dt * bf2f(Aim[d * N_ + n]);
        float br = dt * bf2f(Bre[d * N_ + n]), bi = dt * bf2f(Bim[d * N_ + n]);
        float cr = bf2f(Cre[d * N_ + n]), ci = bf2f(Cim[d * N_ + n]);
        wr[n] = cr * br - ci * bi;
        wi[n] = cr * bi + ci * br;
    }
    __syncthreads();
    for (int l = threadIdx.x; l < L_; l += blockDim.x) {
        float lf = rbf((float)l);
        float acc = 0.f;
        for (int n = 0; n < N_; n++) {
            float e = expf(ar[n] * lf);
            float sn, cs;
            sincosf(ai[n] * lf, &sn, &cs);
            acc += e * (wr[n] * cs - wi[n] * sn);
        }
        K[d * L_ + l] = acc;
    }
}

__global__ void k_conv(const float* __restrict__ K, const float* __restrict__ uT, float* __restrict__ yT) {
    int d = blockIdx.x;
    __shared__ float sk[L_], su[L_];
    for (int i = threadIdx.x; i < L_; i += blockDim.x) { sk[i] = K[d * L_ + i]; su[i] = uT[d * L_ + i]; }
    __syncthreads();
    for (int t = threadIdx.x; t < L_; t += blockDim.x) {
        float acc = 0.f;
        for (int j = 0; j <= t; j++) acc += sk[j] * su[t - j];
        yT[d * L_ + t] = acc;
    }
}

__global__ void k_ln2(const u16* __restrict__ x, const u16* __restrict__ u, const float* __restrict__ yT,
                      const u16* __restrict__ Dskip, const u16* __restrict__ wr_, u16* __restrict__ hbuf,
                      u16* __restrict__ x2buf, float* __restrict__ rbuf, u16* __restrict__ out) {
    int row = blockIdx.x * 4 + (threadIdx.x >> 6);
    int lane = threadIdx.x & 63;
    float h[10];
    for (int i = 0; i < 10; i++) {
        int c = lane + 64 * i;
        float y = rbf(yT[c * L_ + row]);
        float sk = rbf(bf2f(u[row * D_ + c]) * bf2f(Dskip[c]));
        float a = rbf(y + sk);
        h[i] = rbf(bf2f(x[row * D_ + c]) + a);
    }
    float m, s;
    ln_stats(h, m, s);
    float r = 0.f;
    for (int i = 0; i < 10; i++) {
        int c = lane + 64 * i;
        float x2 = rbf(rbf(h[i] - m) / s);
        r += x2 * bf2f(wr_[c]);
        hbuf[row * D_ + c] = f2bf(h[i]);
        x2buf[row * D_ + c] = f2bf(x2);
        out[row * D_ + c] = f2bf(h[i] + x2);
    }
    r = wave_sum(r);
    if (lane == 0) rbuf[row] = rbf(r);
}

__global__ void k_argmax(const float* __restrict__ rbuf, int* __restrict__ idx) {
    int lane = threadIdx.x;
    float best = -INFINITY; int bi = 0x7fffffff;
    for (int i = lane; i < L_; i += 64) { float v = rbuf[i]; if (v > best) { best = v; bi = i; } }
    for (int o = 32; o > 0; o >>= 1) {
        float ob = __shfl_xor(best, o); int oi = __shfl_xor(bi, o);
        if (ob > best || (ob == best && oi < bi)) { best = ob; bi = oi; }
    }
    if (lane == 0) idx[0] = (bi == 0x7fffffff) ? 0 : bi;
}

__global__ void k_ffn1(const int* idx, const u16* __restrict__ x2buf, const u16* __restrict__ wg, const u16* __restrict__ wu,
                       float* __restrict__ act) {
    int f = blockIdx.x * blockDim.x + threadIdx.x;
    int row = idx[0];
    float g = 0.f, u = 0.f;
    for (int k = 0; k < D_; k++) {
        float xv = bf2f(x2buf[row * D_ + k]);
        g += xv * bf2f(wg[k * F_ + f]);
        u += xv * bf2f(wu[k * F_ + f]);
    }
    g = rbf(g); u = rbf(u);
    float sg = rbf(g / (1.0f + expf(-g)));
    act[f] = rbf(sg * u);
}

__global__ void k_ffn2(const int* idx, const float* __restrict__ act, const u16* __restrict__ wd, const float* rbuf,
                       const u16* hbuf, const u16* x2buf, u16* __restrict__ out) {
    int c = blockIdx.x * blockDim.x + threadIdx.x;
    int row = idx[0];
    float y = 0.f;
    for (int k = 0; k < F_; k++) y += act[k] * bf2f(wd[k * D_ + c]);
    y = rbf(y);
    float rs = rbf(1.0f / (1.0f + expf(-rbuf[row])));
    y = rbf(y * rs);
    float b = rbf(bf2f(x2buf[row * D_ + c]) + y);
    out[row * D_ + c] = f2bf(bf2f(hbuf[row * D_ + c]) + b);
}

extern "C" void kernel_launch(void* const* d_in, const int* in_sizes, int n_in,
                              void* d_out, int out_size, void* d_ws, size_t ws_size,
                              hipStream_t stream) {
    const u16* x = (const u16*)d_in[0];
    char* ws = (char*)d_ws;
    u16* u = (u16*)ws;                       ws += L_ * D_ * 2;
    float* uT = (float*)ws;                  ws += L_ * D_ * 4;
    float* K = (float*)ws;                   ws += L_ * D_ * 4;
    float* yT = (float*)ws;                  ws += L_ * D_ * 4;
    u16* hbuf = (u16*)ws;                    ws += L_ * D_ * 2;
    u16* x2buf = (u16*)ws;                   ws += L_ * D_ * 2;
    float* rbuf = (float*)ws;                ws += L_ * 4;
    float* act = (float*)ws;                 ws += F_ * 4;
    int* idx = (int*)ws;
    k_ln1<<<L_ / 4, 256, 0, stream>>>(x, u, uT);
    k_kgen<<<D_, 256, 0, stream>>>((const u16*)d_in[1], (const u16*)d_in[2], (const u16*)d_in[3], (const u16*)d_in[4],
                                   (const u16*)d_in[5], (const u16*)d_in[6], (const u16*)d_in[7], K);
    k_conv<<<D_, 256, 0, stream>>>(K, uT, yT);
    k_ln2<<<L_ / 4, 256, 0, stream>>>(x, u, yT, (const u16*)d_in[8], (const u16*)d_in[10], hbuf, x2buf, rbuf, (u16*)d_out);
    k_argmax<<<1, 64, 0, stream>>>(rbuf, idx);
    k_ffn1<<<F_ / 256, 256, 0, stream>>>(idx, x2buf, (const u16*)d_in[11], (const u16*)d_in[12], act);
    k_ffn2<<<D_ / 64, 64, 0, stream>>>(idx, act, (const u16*)d_in[13], rbuf, hbuf, x2buf, (u16*)d_out);
}
```

```cpp
#include <hip/hip_runtime.h>
#include <stdint.h>

typedef unsigned short u16;
typedef unsigned int u32;
typedef unsigned long long u64;
typedef _Float16 h8 __attribute__((ext_vector_type(8)));
typedef _Float16 h2 __attribute__((ext_vector_type(2)));
typedef __bf16 b2 __attribute__((ext_vector_type(2)));
typedef float f2 __attribute__((ext_vector_type(2)));
typedef float f4 __attribute__((ext_vector_type(4)));
typedef u32 u32x2 __attribute__((ext_vector_type(2)));
typedef u32 u32x3 __attribute__((ext_vector_type(3)));
typedef u32 u32x4 __attribute__((ext_vector_type(4)));
#define LAS __attribute__((address_space(3)))
#define GAS __attribute__((address_space(1)))
#define RLX_AGENT __ATOMIC_RELAXED, __HIP_MEMORY_SCOPE_AGENT

constexpr int L_ = 2048, D_ = 640, N_ = 192, F_ = 1536;

__device__ __forceinline__ float bflo(u32 p) { return __uint_as_float(p << 16); }
__device__ __forceinline__ float bfhi(u32 p) { return __uint_as_float(p & 0xffff0000u); }
__device__ __forceinline__ float bf2f(u16 v) { return __uint_as_float(((u32)v) << 16); }
__device__ __forceinline__ u32 pk_bf16(float a, float b) {
    f2 v = {a, b};
    b2 r = __builtin_convertvector(v, b2);
    return __builtin_bit_cast(u32, r);
}
__device__ __forceinline__ u32 pk_f16(float a, float b) {
    f2 v = {a, b};
    h2 r = __builtin_convertvector(v, h2);
    return __builtin_bit_cast(u32, r);
}
__device__ __forceinline__ float rbf(float a) { return bflo(pk_bf16(a, a)); }
__device__ __forceinline__ float dot2bf(u32 a, u32 b, float c) {
    return __builtin_amdgcn_fdot2_f32_bf16(__builtin_bit_cast(b2, a), __builtin_bit_cast(b2, b), c, false);
}

template <int CTRL, int RM = 0xf, int BM = 0xf>
__device__ __forceinline__ float dpp_get(float v) {
    return __int_as_float(__builtin_amdgcn_update_dpp(0, __float_as_int(v), CTRL, RM, BM, false));
}
template <int CTRL, int RM = 0xf, int BM = 0xf>
__device__ __forceinline__ float dpp_add(float v) { return v + dpp_get<CTRL, RM, BM>(v); }
__device__ __forceinline__ float rdl(float v, int l) { return __int_as_float(__builtin_amdgcn_readlane(__float_as_int(v), l)); }
__device__ __forceinline__ float wave_sum(float v) {
    v = dpp_add<0xB1>(v);
    v = dpp_add<0x4E>(v);
    v = dpp_add<0x141>(v);
    v = dpp_add<0x140>(v);
    v = dpp_add<0x142, 0xa>(v);
    v = dpp_add<0x143, 0xc>(v);
    return rdl(v, 63);
}
template <int CTRL, int RM = 0xf, int BM = 0xf>
__device__ __forceinline__ u32 dpp_umax(u32 v) {
    u32 t = (u32)__builtin_amdgcn_update_dpp(0, (int)v, CTRL, RM, BM, false);
    return v > t ? v : t;
}
__device__ __forceinline__ u32 wave_umax(u32 v) {
    v = dpp_umax<0xB1>(v);
    v = dpp_umax<0x4E>(v);
    v = dpp_umax<0x141>(v);
    v = dpp_umax<0x140>(v);
    v = dpp_umax<0x142, 0xa>(v);
    v = dpp_umax<0x143, 0xc>(v);
    return (u32)__builtin_amdgcn_readlane((int)v, 63);
}
__device__ __forceinline__ float xrow_sum(float v) {
    u32 u = __float_as_uint(v);
    auto p = __builtin_amdgcn_permlane16_swap(u, u, false, false);
    v = __uint_as_float(p[0]) + __uint_as_float(p[1]);
    u = __float_as_uint(v);
    auto q = __builtin_amdgcn_permlane32_swap(u, u, false, false);
    return __uint_as_float(q[0]) + __uint_as_float(q[1]);
}
__device__ __forceinline__ float reduce16(const float (&v)[16], int lane) {
    const bool b3 = lane & 8, b2_ = lane & 4, b1 = lane & 2, b0 = lane & 1;
    float w[8], z[4], y[2];
#pragma unroll
    for (int k = 0; k < 8; k++) { float keep = b3 ? v[k + 8] : v[k], give = b3 ? v[k] : v[k + 8]; w[k] = keep + dpp_get<0x140>(give); }
#pragma unroll
    for (int k = 0; k < 4; k++) { float keep = b2_ ? w[k + 4] : w[k], give = b2_ ? w[k] : w[k + 4]; z[k] = keep + dpp_get<0x141>(give); }
#pragma unroll
    for (int k = 0; k < 2; k++) { float keep = b1 ? z[k + 2] : z[k], give = b1 ? z[k] : z[k + 2]; y[k] = keep + dpp_get<0x1B>(give); }
    float keep = b0 ? y[1] : y[0], give = b0 ? y[0] : y[1];
    float r = keep + dpp_get<0xB1>(give);
    return xrow_sum(r);
}

__device__ __forceinline__ void ln_stats(const float (&x)[10], float& m, float& s) {
    float sum = 0.f;
#pragma unroll
    for (int i = 0; i < 10; i++) sum += x[i];
    float mean = wave_sum(sum) * (1.0f / D_);
    float ss = 0.f;
#pragma unroll
    for (int i = 0; i < 10; i++) { float d = x[i] - mean; ss = fmaf(d, d, ss); }
    float var = wave_sum(ss) * (1.0f / D_);
    m = rbf(mean);
    s = rbf(__builtin_sqrtf(rbf(rbf(var) + 1e-5f)));
}
__device__ __forceinline__ float div_r(float a, float s, float r) {
    float q = a * r;
    float e = fmaf(-q, s, a);
    return fmaf(e, r, q);
}

struct cf { float r, i; };
__device__ __forceinline__ cf cmul(cf a, cf b) {
    cf o;
    o.r = fmaf(a.r, b.r, -(a.i * b.i));
    o.i = fmaf(a.r, b.i, a.i * b.r);
    return o;
}
__device__ __forceinline__ cf cmulc(cf a, cf b) {
    cf o;
    o.r = fmaf(a.r, b.r, a.i * b.i);
    o.i = fmaf(a.i, b.r, -(a.r * b.i));
    return o;
}
__device__ __forceinline__ cf csq(cf a) {
    cf o;
    o.r = fmaf(a.r, a.r, -(a.i * a.i));
    o.i = 2.0f * a.r * a.i;
    return o;
}
__device__ __forceinline__ cf cexp_dir(float alpha, float bh, float bl, float x) {
    float mag = __builtin_amdgcn_exp2f(alpha * x);
    float ph = __builtin_amdgcn_fractf(bh * x);
    ph = fmaf(bl, x, ph);
    cf o;
    o.r = mag * __builtin_amdgcn_cosf(ph);
    o.i = mag * __builtin_amdgcn_sinf(ph);
    return o;
}
__device__ __forceinline__ f4 mfma16(h8 a, h8 b, f4 c) { return __builtin_amdgcn_mfma_f32_16x16x32_f16(a, b, c, 0, 0, 0); }
__device__ __forceinline__ void wave_fence() {
    __builtin_amdgcn_fence(__ATOMIC_ACQ_REL, "wavefront");
    __builtin_amdgcn_wave_barrier();
}
__device__ __forceinline__ void lds_signal(LAS u32* w, int lane) {
    asm volatile("s_waitcnt lgkmcnt(0)" ::: "memory");
    if (lane == 0) __hip_atomic_fetch_add(w, 1u, __ATOMIC_RELAXED, __HIP_MEMORY_SCOPE_WORKGROUP);
    asm volatile("" ::: "memory");
}
__device__ __forceinline__ void lds_wait(LAS u32* w, u32 want) {
    asm volatile("" ::: "memory");
    while (__builtin_amdgcn_readfirstlane(__hip_atomic_load(w, __ATOMIC_RELAXED, __HIP_MEMORY_SCOPE_WORKGROUP)) < want)
        __builtin_amdgcn_s_sleep(0);
    asm volatile("" ::: "memory");
}

constexpr int NT = 768;
#define YDELAY 16
#define KDELAY 8
#define GDELAY 16

constexpr int UIMG_CH = (256 + L_) * 2;
constexpr int O_UIMG = 0;
constexpr int KCS = 4160;
constexpr int KIMG_CH = 4 * KCS;
constexpr int O_KIMG = O_UIMG + 3 * UIMG_CH;
constexpr int O_KTAB = O_KIMG + 3 * KIMG_CH;
constexpr int KTAB_CH = 656 * 4;
constexpr int O_ACCX = O_KTAB + KTAB_CH;
constexpr int O_USTG = O_ACCX;
constexpr int SCR = 32 * 272;
constexpr int O_SCR = O_ACCX + 2 * 8192;
constexpr int O_FLAG = O_SCR + 8 * SCR;
constexpr int LDS_MAIN = O_FLAG + 64 + 128;
static_assert(O_KIMG % 16 == 0 && O_KTAB % 16 == 0 && O_ACCX % 16 == 0 && O_FLAG % 16 == 0 && O_SCR % 16 == 0, "align");
enum { FL_POWN = 0, FL_PSH, FL_SLOC, FL_KIMG, FL_UIMG, FL_ACC0, FL_ACC1, FL_STAB, FL_SIMG, FL_USTG, FL_XRD, FL_CLR, FL_CONV };
constexpr int O_MISC = O_FLAG + 64;
constexpr int YS = 1280 + 16;
constexpr int O_YS = O_SCR;
constexpr int O_XSEL = O_YS + 8 * YS;
constexpr int O_ACT = O_XSEL + 2560;
constexpr int O_RED = O_ACT + 6144;

struct PosTab { u16 p[512][8]; };
constexpr int rne_lag(int j) {
    if (j < 256) return j;
    int sh = j < 512 ? 1 : (j < 1024 ? 2 : 3);
    int q = j >> sh, rem = j & ((1 << sh) - 1), half = 1 << (sh - 1);
    if (rem > half || (rem == half && (q & 1))) q++;
    return q << sh;
}
constexpr int ktab_pos_c(int j) {
    if (j < 0 || j >= 2048) return 644;
    int l = rne_lag(j);
    if (l < 256) return l;
    if (l < 512) return 256 + ((l - 256) >> 1);
    if (l < 1024) return 384 + ((l - 512) >> 2);
    return 512 + ((l - 1024) >> 3);
}
constexpr PosTab make_postab() {
    PosTab t{};
    for (int c = 0; c < 512; c++) for (int i = 0; i < 8; i++) t.p[c][i] = (u16)(4 * ktab_pos_c(2047 - 4 * c - i));
    return t;
}
__device__ const __attribute__((aligned(16))) PosTab g_postab = make_postab();

#define STAMP(i)

constexpr int XCH_STRIDE = 5376;
constexpr int CAND_STRIDE = 5120;
struct MainArgs {
    const u16 *x, *Are, *Aim, *Bre, *Bim, *Cre, *Cim, *logdt, *Dskip, *wr, *wg, *wu, *wd;
    u16* out;
    u32* epoch;
    char* xch;
    char* ubuf;
    char* ybuf;
    char* clr;
    char* keys;
    char* cand;
    char* gu;
    u64* dbg;
};
constexpr u32 SPIN_MAX = 1u << 16;

__device__ __forceinline__ void lds_w32(LAS char* p, int off, u32 v) { *(LAS u32*)(p + off) = v; }

typedef float f16v __attribute__((ext_vector_type(16)));
__device__ __forceinline__ f2 rot(f2 b) { return f2{-b.y, b.x}; }
__device__ __forceinline__ f2 cm(f2 a, f2 b, f2 rb) { return __builtin_elementwise_fma(a.yy, rb, a.xx * b); }
__device__ __forceinline__ f2 csq2(f2 a) { return f2{fmaf(a.x, a.x, -(a.y * a.y)), 2.0f * a.x * a.y}; }
template <bool CONJ>
__device__ __forceinline__ f2 cexp2(float alpha, float bh, float bl, float x) {
    float mag = __builtin_amdgcn_exp2f(alpha * x);
    float ph = __builtin_amdgcn_fractf(bh * x);
    ph = fmaf(bl, x, ph);
    f2 cs = {__builtin_amdgcn_cosf(ph), __builtin_amdgcn_sinf(CONJ ? -ph : ph)};
    return cs * mag;
}
__device__ __forceinline__ void put(LAS char* sp, int row, f2 v) { lds_w32(sp, row * 272, pk_f16(v.x, v.y)); }

__device__ __forceinline__ h8 ld_b(LAS const char* ub, int t, int mu) { return *(LAS const h8*)(ub + 512 * t + 64 * (7 - mu)); }
__device__ __forceinline__ h8 ld_a(LAS const char* kl, LAS const char* kh, int M, int mu) {
    u32x2 lo = *(LAS const u32x2*)(kl + 512 * (7 - M) + 64 * (7 - mu));
    u32x2 hi = *(LAS const u32x2*)(kh + 512 * (7 - M) + 64 * (7 - mu));
    u32x4 a4 = {lo.x, lo.y, hi.x, hi.y};
    return __builtin_bit_cast(h8, a4);
}
template <int MU0, int MU1>
__device__ __forceinline__ void conv_run(f4 (&acc)[8], LAS const char* ub, LAS const char* kl, LAS const char* kh) {
    h8 A[8], B[8];
#pragma unroll
    for (int i = 0; i < 8; i++) { B[7 - i] = ld_b(ub, 7 - i, MU0); A[i] = ld_a(kl, kh, i, MU0); __builtin_amdgcn_sched_barrier(0); }
#pragma unroll
    for (int mu = MU0; mu < MU1; mu++) {
#pragma unroll
        for (int M = 0; M < 8; M++) {
#pragma unroll
            for (int t = 7 - M; t >= 0; t--) acc[t + M] = mfma16(A[M], B[t], acc[t + M]);
            if (mu + 1 < MU1) { A[M] = ld_a(kl, kh, M, mu + 1); B[7 - M] = ld_b(ub, 7 - M, mu + 1); }
            __builtin_amdgcn_sched_barrier(0);
        }
    }
}
template <int TA, int TB>
__device__ __forceinline__ void conv_pair(f4& a0, f4& a1, LAS const char* ub, LAS const char* kl, LAS const char* kh) {
    h8 A[TB + 1], B[TB + 1];
    f4 a2 = {0, 0, 0, 0};
#pragma unroll
    for (int i = 0; i <= TB; i++) { B[TB - i] = ld_b(ub, TB - i, 0); A[i] = ld_a(kl, kh, i, 0); __builtin_amdgcn_sched_barrier(0); }
#pragma unroll
    for (int mu = 0; mu < 8; mu++) {
#pragma unroll
        for (int M = 0; M <= TB; M++) {
            if (M & 1) a2 = mfma16(A[M], B[TB - M], a2); else a1 = mfma16(A[M], B[TB - M], a1);
            if (M <= TA) a0 = mfma16(A[M], B[TA - M], a0);
            if (mu + 1 < 8) { A[M] = ld_a(kl, kh, M, mu + 1); B[TB - M] = ld_b(ub, TB - M, mu + 1); }
            __builtin_amdgcn_sched_barrier(0);
        }
    }
    a1 += a2;
}

typedef __amdgpu_buffer_rsrc_t rsrc_t;
__device__ __forceinline__ void store_y_tile(rsrc_t rsy, int ch, int T, int lane, f4 a, float sc) {
    u32x2 o;
    o.x = pk_bf16(a.x * sc, a.y * sc);
    o.y = pk_bf16(a.z * sc, a.w * sc);
    const int n = lane & 15, g = lane >> 4;
    __builtin_amdgcn_raw_buffer_store_b64(o, rsy, (((n >> 1) * D_ + ch) * 64 + (n & 1) * 32 + g * 8) + T * (8 * D_ * 64), 0, 16);
}
__device__ __forceinline__ void raw_barrier() {
    asm volatile("s_waitcnt lgkmcnt(0)\n\ts_barrier" ::: "memory");
}
__device__ __forceinline__ u32 sort_key(float r, int row) {
    u32 u = __float_as_uint(r) >> 16;
    u = (u & 0x8000u) ? (~u & 0xffffu) : (u | 0x8000u);
    return (u << 16) | (u32)(2047 - row);
}

__device__ __forceinline__ void build_chunk(LAS char* kimg, LAS const char* tab, u32x4 pp, int c) {
    float v[7];
#pragma unroll
    for (int i = 0; i < 7; i++) v[i] = *(LAS const float*)(tab + ((i & 1) ? (pp[i >> 1] >> 16) : (pp[i >> 1] & 0xffffu)));
    u32 p01 = pk_f16(v[0], v[1]), p23 = pk_f16(v[2], v[3]), p45 = pk_f16(v[4], v[5]);
    u32 p12 = pk_f16(v[1], v[2]), p34 = pk_f16(v[3], v[4]), p56 = pk_f16(v[5], v[6]);
    *(LAS u32x2*)(kimg + 0 * KCS + 32 + 8 * c) = u32x2{p01, p23};
    *(LAS u32x2*)(kimg + 1 * KCS + 32 + 8 * c) = u32x2{p12, p34};
    *(LAS u32x2*)(kimg + 2 * KCS + 32 + 8 * c) = u32x2{p23, p45};
    *(LAS u32x2*)(kimg + 3 * KCS + 32 + 8 * c) = u32x2{p34, p56};
}

__device__ __forceinline__ void build_chunk3(LAS char* kimg, LAS const char* part, const u32 (&po)[7], int c, float sc) {
    float v[7];
#pragma unroll
    for (int i = 0; i < 7; i++)
        v[i] = (*(LAS const float*)(part + po[i]) + *(LAS const float*)(part + po[i] + SCR) + *(LAS const float*)(part + po[i] + 2 * SCR)) * sc;
    u32 p01 = pk_f16(v[0], v[1]), p23 = pk_f16(v[2], v[3]), p45 = pk_f16(v[4], v[5]);
    u32 p12 = pk_f16(v[1], v[2]), p34 = pk_f16(v[3], v[4]), p56 = pk_f16(v[5], v[6]);
    *(LAS u32x2*)(kimg + 0 * KCS + 32 + 8 * c) = u32x2{p01, p23};
    *(LAS u32x2*)(kimg + 1 * KCS + 32 + 8 * c) = u32x2{p12, p34};
    *(LAS u32x2*)(kimg + 2 * KCS + 32 + 8 * c) = u32x2{p23, p45};
    *(LAS u32x2*)(kimg + 3 * KCS + 32 + 8 * c) = u32x2{p34, p56};
}

constexpr u32 CKM = 0x00070007u;
__device__ __forceinline__ u32x4 ck_expect(u32x4 d, u32 e) {
    u32 m0 = d.x & ~CKM, m1 = d.y & ~CKM, m2 = d.z & ~CKM, m3 = d.w & ~CKM;
    u32 s = (m0 + m1) + (m2 + m3) + e;
    s ^= s >> 9;
    u32x4 o;
    o.x = m0 | (s & CKM);
    o.y = m1 | (__builtin_amdgcn_alignbit(s, s, 3) & CKM);
    o.z = m2 | (__builtin_amdgcn_alignbit(s, s, 6) & CKM);
    o.w = m3 | (__builtin_amdgcn_alignbit(s, s, 9) & CKM);
    return o;
}

__global__ void __launch_bounds__(NT, 3) k_main(MainArgs a) {
    extern __shared__ __attribute__((aligned(16))) char smem_[];
    LAS char* smem = (LAS char*)smem_;
    const int tid = threadIdx.x, lane = tid & 63;
    const int wave = __builtin_amdgcn_readfirstlane(tid >> 6);
    const int b = blockIdx.x;
    const int pairi = (b & 7) * 16 + (b >> 4), side = (b >> 3) & 1;
    const int cbase = 5 * pairi + 2 * side;
    const int ch0 = side ? cbase + 1 : cbase, ch1 = ch0 + 1, chs = 5 * pairi + 2;
    const int chunk = (b & 7) * 32 + (b >> 3);
    LAS u32* flags = (LAS u32*)(smem + O_FLAG);
    STAMP(0);
    const u32 ep = __hip_atomic_load((GAS u32*)a.epoch, RLX_AGENT);
    const rsrc_t rsy = __builtin_amdgcn_make_buffer_rsrc((void*)a.ybuf, 0, 64 * D_ * 64, 0x00020000);
    const int jl = b >> 3;
    const bool is_f1 = jl < 12, is_f2 = jl >= 12 && jl < 17;
    const int f1u = (b & 7) * 12 + jl;
    const int f2u = (b & 7) * 5 + (jl - 12);
    u32x4 wpf[12];
#pragma unroll
    for (int i = 0; i < 12; i++) wpf[i] = u32x4{0, 0, 0, 0};

    if (wave < 8) {
        const int slot_g = wave < 3 ? 0 : (wave < 6 ? 1 : 2);
        const int ch_g = slot_g == 0 ? ch0 : (slot_g == 1 ? ch1 : chs);
        int mode; bool act = true;
        if (slot_g < 2) mode = (wave - 3 * slot_g) * 64 + lane;
        else { mode = 96 * side + (wave - 6) * 64 + lane; act = (wave == 6) || (lane < 32); if (!act) mode = 0; }
        const int gi = ch_g * N_ + mode;
        u16 pdt = a.logdt[ch_g];
        u16 pAre = a.Are[gi], pAim = a.Aim[gi], pBre = a.Bre[gi], pBim = a.Bim[gi], pCre = a.Cre[gi], pCim = a.Cim[gi];
        u32x4 pp = *(const GAS u32x4*)(&g_postab.p[tid][0]);
        if (tid < 16) flags[tid] = 0;
        if (tid >= 64 && tid < 160) { int t = tid - 64; *(LAS u32x4*)(smem + O_UIMG + (t >> 5) * UIMG_CH + (t & 31) * 16) = u32x4{0, 0, 0, 0}; }
        if (tid >= 192 && tid < 216) { int t = tid - 192; *(LAS u32x4*)(smem + O_KIMG + (t >> 1) * KCS + 4128 + (t & 1) * 16) = u32x4{0, 0, 0, 0}; }
        asm volatile("s_waitcnt lgkmcnt(0)" ::: "memory");
        __builtin_amdgcn_s_barrier();
        float Are = bf2f(pAre), Aim = bf2f(pAim), Bre = bf2f(pBre), Bim = bf2f(pBim), Cre = bf2f(pCre), Cim = bf2f(pCim);
        float dt = rbf(expf(bf2f(pdt)));
        float are = dt * Are, aim = dt * Aim, br = dt * Bre, bi = dt * Bim;
        f2 wc = {fmaf(Cre, br, -(Cim * bi)), -fmaf(Cre, bi, Cim * br)};
        if (!act) wc = f2{0.f, 0.f};
        STAMP(1);
        float alpha = are * 1.44269504088896341f;
        const float CH = 0.15915494f, CL = 6.4206382e-09f;
        float p_ = aim * CH;
        float pe = fmaf(aim, CL, fmaf(aim, CH, -p_));
        float pf = p_ - __builtin_rintf(p_);
        float bh = __uint_as_float(__float_as_uint(pf + pe) & 0xfffff800u);
        float bl = (pf - bh) + pe;
        float mw = fabsf(wc.x) + fabsf(wc.y);
        float v0r = 16384.f;
        if (__any(alpha > 0.f)) {
            float ap = fmaxf(alpha, 0.f);
            float g1 = __builtin_amdgcn_exp2f(ap * 1024.f);
            wc = (wc * g1) * g1;
            mw = (fabsf(wc.x) + fabsf(wc.y)) * __builtin_amdgcn_exp2f(ap * 248.f);
            v0r = __builtin_amdgcn_exp2f(fmaf(ap, -2048.f, 14.f));
        }
        u32 mwu = wave_umax(__float_as_uint(mw));
        int e = (int)(mwu >> 23) - 126;
        if (e < -100) e = -100;
        if (e > 127) e = 127;
        LAS char* scr = smem + O_SCR + wave * SCR;
        LAS char* sp = scr + lane * 4;
        LAS const char* fp = scr + (lane & 31) * 272 + (lane >> 5) * 16;
        {
            f2 E8 = cexp2<true>(alpha, bh, bl, 8.f);
            f2 E16 = csq2(E8), E32 = csq2(E16);
            f2 r8 = rot(E8), r32 = rot(E32);
            f2 Y0 = wc * __builtin_amdgcn_ldexpf(1.0f, 14 - e);
            f2 Y2 = cm(Y0, E16, rot(E16));
            f2 Y1 = cm(Y0, E8, r8), Y3 = cm(Y2, E8, r8);
#pragma unroll
            for (int q = 0; q < 32; q += 4) {
                put(sp, q, Y0); put(sp, q + 1, Y1); put(sp, q + 2, Y2); put(sp, q + 3, Y3);
                if (q < 28) { Y0 = cm(Y0, E32, r32); Y1 = cm(Y1, E32, r32); Y2 = cm(Y2, E32, r32); Y3 = cm(Y3, E32, r32); }
            }
        }
        wave_fence();
        h8 By[8];
#pragma unroll
        for (int k = 0; k < 8; k++) By[k] = *(LAS const h8*)(fp + 32 * k);
        wave_fence();
        {
            f2 E1 = cexp2<false>(alpha, bh, bl, 1.f), E256 = cexp2<false>(alpha, bh, bl, 256.f), E1024 = cexp2<false>(alpha, bh, bl, 1024.f);
            f2 E2 = csq2(E1), E4 = csq2(E2), E512 = csq2(E256), E2048 = csq2(E1024);
            f2 r1 = rot(E1), r256 = rot(E256);
            f2 E768 = cm(E512, E256, r256);
            f2 V2 = E2 * v0r, V4 = E4 * v0r;
            f2 rV4 = rot(V4);
            f2 V6 = cm(E2, V4, rV4);
            put(sp, 0, f2{v0r, 0.f}); put(sp, 1, E1 * v0r); put(sp, 2, V2); put(sp, 3, cm(V2, E1, r1));
            put(sp, 4, V4); put(sp, 5, cm(V4, E1, r1)); put(sp, 6, V6); put(sp, 7, cm(V6, E1, r1));
            put(sp, 8, E256 * v0r); put(sp, 9, cm(V2, E256, r256)); put(sp, 10, cm(V4, E256, r256)); put(sp, 11, cm(V6, E256, r256));
            put(sp, 12, E512 * v0r); put(sp, 13, cm(E512, V4, rV4)); put(sp, 14, E768 * v0r); put(sp, 15, cm(E768, V4, rV4));
            f2 H0 = E1024 * v0r;
            f2 H1 = cm(H0, E256, r256), H2 = cm(H1, E256, r256), H3 = cm(H2, E256, r256);
            put(sp, 16, H0); put(sp, 17, H1); put(sp, 18, H2); put(sp, 19, H3); put(sp, 20, E2048 * v0r);
        }
        wave_fence();
        f16v dd = {0, 0, 0, 0, 0, 0, 0, 0, 0, 0, 0, 0, 0, 0, 0, 0};
#pragma unroll
        for (int k = 0; k < 8; k++) dd = __builtin_amdgcn_mfma_f32_32x32x16_f16(*(LAS const h8*)(fp + 32 * k), By[k], dd, 0, 0, 0);
        wave_fence();
        {
            const float ts = __builtin_amdgcn_ldexpf(1.0f, e - 28);
            const int q = lane & 31, h = lane >> 5;
            *(LAS f4*)(scr + (8 * q + 4 * h) * 4) = f4{dd[0], dd[1], dd[2], dd[3]} * ts;
            if (h == 0) {
                *(LAS f4*)(scr + (256 + 4 * q) * 4) = f4{dd[4], dd[5], dd[6], dd[7]} * ts;
                *(LAS float*)(scr + (512 + q) * 4) = dd[8] * ts;
                *(LAS float*)(scr + (544 + q) * 4) = dd[9] * ts;
                *(LAS float*)(scr + (576 + q) * 4) = dd[10] * ts;
                *(LAS float*)(scr + (608 + q) * 4) = dd[11] * ts;
            } else {
                *(LAS f2*)(scr + (384 + 2 * q) * 4) = f2{dd[4], dd[5]} * ts;
                *(LAS f2*)(scr + (448 + 2 * q) * 4) = f2{dd[6], dd[7]} * ts;
                if (q == 0) {
                    *(LAS f4*)(scr + 640 * 4) = f4{dd[8] * ts, 0.f, 0.f, 0.f};
                    *(LAS u32x2*)(scr + 644 * 4) = u32x2{0u, (u32)e};
                }
            }
        }
        STAMP(2);
        lds_signal(flags + (slot_g < 2 ? FL_POWN : FL_PSH), lane);
        u32 po[7];
#pragma unroll
        for (int i = 0; i < 7; i++) po[i] = (i & 1) ? (pp[i >> 1] >> 16) : (pp[i >> 1] & 0xffffu);
        const u32 tag = ep * 0x9E3779B1u + 0x7F4A7C15u;
        asm volatile("" :: "v"(tag));
        lds_wait(flags + FL_POWN, 6);
        STAMP(3);
        float ysc;
        {
            LAS const char* p0 = smem + O_SCR;
            int em0 = max(max(*(LAS const int*)(p0 + 645 * 4), *(LAS const int*)(p0 + SCR + 645 * 4)), *(LAS const int*)(p0 + 2 * SCR + 645 * 4));
            int em1 = max(max(*(LAS const int*)(p0 + 3 * SCR + 645 * 4), *(LAS const int*)(p0 + 4 * SCR + 645 * 4)), *(LAS const int*)(p0 + 5 * SCR + 645 * 4));
            build_chunk3(smem + O_KIMG, p0, po, tid, __builtin_amdgcn_ldexpf(1.0f, 7 - em0));
            build_chunk3(smem + O_KIMG + KIMG_CH, p0 + 3 * SCR, po, tid, __builtin_amdgcn_ldexpf(1.0f, 7 - em1));
            ysc = __builtin_amdgcn_ldexpf(1.0f, (wave < 2 ? em0 : em1) - 7);
        }
        lds_signal(flags + FL_KIMG, lane);
        STAMP(5);

        if (wave < 4) {
            const int n = lane & 15, g = lane >> 4;
            const int slot = wave >> 1;
            const int s = (3 - n) & 3;
            const int uoff = 2 * (256 + 16 * n - 16 + 8 * g) - 64 * 7;
            const int koff = s * KCS + 2 * (2047 - n + 8 * g - s) - 512 * 7 - 64 * 7;
            LAS const char* ub = smem + O_UIMG + slot * UIMG_CH + uoff;
            LAS const char* kl = smem + O_KIMG + slot * KIMG_CH + koff;
            LAS const char* kh = kl + 8;
            asm volatile("" : "+v"(kh));
            f4 acc[8];
#pragma unroll
            for (int t = 0; t < 8; t++) acc[t] = f4{0, 0, 0, 0};
            LAS char* ax = smem + O_ACCX + slot * 8192 + lane * 16;
            lds_wait(flags + FL_KIMG, 8);
            lds_wait(flags + FL_UIMG, 4);
            __builtin_amdgcn_s_setprio(3);
            STAMP(6);
            if ((wave & 1) == 0) {
                conv_run<0, 5>(acc, ub, kl, kh);
                STAMP(7);
                lds_wait(flags + FL_ACC0 + slot, 1);
                const float sc = ysc;
#pragma unroll
                for (int t = 0; t < 8; t++) {
                    f4 o = acc[t] + *(LAS const f4*)(ax + 1024 * t);
                    store_y_tile(rsy, slot ? ch1 : ch0, t, lane, o, sc);
                }
            } else {
                conv_run<5, 8>(acc, ub, kl, kh);
#pragma unroll
                for (int t = 0; t < 8; t++) *(LAS f4*)(ax + 1024 * t) = acc[t];
                lds_signal(flags + FL_ACC0 + slot, lane);
                STAMP(7);
                lds_wait(flags + FL_SIMG, 4);
                LAS const char* ub2 = smem + O_UIMG + 2 * UIMG_CH + uoff;
                LAS const char* kl2 = smem + O_KIMG + 2 * KIMG_CH + koff;
                LAS const char* kh2 = kl2 + 8;
                asm volatile("" : "+v"(kh2));
                float sc = *(LAS const float*)(smem + O_KTAB + 645 * 4);
                f4 a0 = {0, 0, 0, 0}, a1 = a0;
                const int sel = side * 2 + slot;
                if (sel == 0) { conv_pair<0, 7>(a0, a1, ub2, kl2, kh2); store_y_tile(rsy, chs, 0, lane, a0, sc); store_y_tile(rsy, chs, 7, lane, a1, sc); }
                else if (sel == 1) { conv_pair<3, 4>(a0, a1, ub2, kl2, kh2); store_y_tile(rsy, chs, 3, lane, a0, sc); store_y_tile(rsy, chs, 4, lane, a1, sc); }
                else if (sel == 2) { conv_pair<1, 6>(a0, a1, ub2, kl2, kh2); store_y_tile(rsy, chs, 1, lane, a0, sc); store_y_tile(rsy, chs, 6, lane, a1, sc); }
                else { conv_pair<2, 5>(a0, a1, ub2, kl2, kh2); store_y_tile(rsy, chs, 2, lane, a0, sc); store_y_tile(rsy, chs, 5, lane, a1, sc); }
            }
            __builtin_amdgcn_s_setprio(0);
            lds_signal(flags + FL_CONV, lane);
        } else {
            const int ht = tid - 256;
            LAS char* tab = smem + O_KTAB;
            u32x4 pp2 = *(const GAS u32x4*)(&g_postab.p[ht][0]);
            if (ht < 192) {
                auto rs = __builtin_amdgcn_make_buffer_rsrc((void*)(a.xch + (size_t)(b ^ 8) * XCH_STRIDE), 0, XCH_STRIDE, 0x00020000);
                const int hi = ht < 161 ? ht : 160;
                u32x4 v0, v1, v2;
                for (u32 spins = 0; spins < SPIN_MAX; spins++) {
                    v0 = __builtin_amdgcn_raw_buffer_load_b128(rs, hi * 16, 0, 16);
                    v1 = __builtin_amdgcn_raw_buffer_load_b128(rs, (162 + hi) * 16, 0, 16);
                    v2 = __builtin_amdgcn_raw_buffer_load_b128(rs, 160 * 16, 0, 16);
                    bool ok = v0.y == tag && v0.w == tag && v1.y == tag && v1.w == tag && v2.y == tag && v2.w == tag;
                    if (__all(ok)) break;
                    __builtin_amdgcn_s_sleep(4);
                }
                lds_wait(flags + FL_SLOC, 3);
                int em = max(*(LAS const int*)(tab + 641 * 4), (int)v2.z);
                f4 loc = *(LAS const f4*)(tab + hi * 16);
                lds_signal(flags + FL_XRD, lane);
                lds_wait(flags + FL_XRD, 3);
                f4 rem = {__uint_as_float(v0.x), __uint_as_float(v0.z), __uint_as_float(v1.x), __uint_as_float(v1.z)};
                if (ht < 161) *(LAS f4*)(tab + ht * 16) = (loc + rem) * __builtin_amdgcn_ldexpf(1.0f, 7 - em);
                else if (ht == 161) *(LAS f2*)(tab + 644 * 4) = f2{0.f, __builtin_amdgcn_ldexpf(1.0f, em - 7)};
                lds_signal(flags + FL_STAB, lane);
            }
            STAMP(6);
            lds_wait(flags + FL_STAB, 3);
            build_chunk(smem + O_KIMG + 2 * KIMG_CH, tab, pp, tid);
            build_chunk(smem + O_KIMG + 2 * KIMG_CH, tab, pp2, ht);
            lds_signal(flags + FL_SIMG, lane);
            STAMP(7);
        }
    } else {
        __builtin_amdgcn_s_barrier();
        __builtin_amdgcn_s_setprio(2);
        const int hw = wave - 8, ht = tid - 512;
        const int blk = chunk >> 2, cq = chunk & 3;
        const GAS u32* xb = (const GAS u32*)a.x + (size_t)(blk * 32 + 8 * hw) * 320;
        u32 xr[8][5];
        int dofs[5];
#pragma unroll
        for (int i = 0; i < 5; i++) { int d = 80 * cq + lane + 64 * i; dofs[i] = d >= 320 ? d - 320 : d; }
#pragma unroll
        for (int j = 0; j < 8; j++)
#pragma unroll
            for (int i = 0; i < 5; i++) xr[j][i] = xb[j * 320 + dofs[i]];
        const int q4 = lane >> 4, l16 = lane & 15;
        u32 xe0 = xb[(2 * q4) * 320 + 80 * cq + 64 + l16], xe1 = xb[(2 * q4 + 1) * 320 + 80 * cq + 64 + l16];
        float sv[16];
#pragma unroll
        for (int j = 0; j < 8; j++) {
            float s1 = 0.f, s2 = 0.f;
#pragma unroll
            for (int i = 0; i < 5; i++) { s1 = dot2bf(xr[j][i], 0x3f803f80u, s1); s2 = dot2bf(xr[j][i], xr[j][i], s2); }
            sv[j] = s1; sv[8 + j] = s2;
        }
        STAMP(1);
        float tot = reduce16(sv, lane);
        float other = dpp_get<0x128>(tot);
        float S1 = (lane & 8) ? other : tot, S2 = (lane & 8) ? tot : other;
        float mean = S1 * (1.0f / D_);
        float var = fmaf(-mean, mean, S2 * (1.0f / D_));
        if (__any(mean * mean > 16.f * var)) {
            float dv[16];
#pragma unroll
            for (int j = 0; j < 8; j++) {
                float mj = rdl(mean, j), ss = 0.f;
#pragma unroll
                for (int i = 0; i < 5; i++) { float d0 = bflo(xr[j][i]) - mj, d1 = bfhi(xr[j][i]) - mj; ss = fmaf(d0, d0, ss); ss = fmaf(d1, d1, ss); }
                dv[j] = ss; dv[8 + j] = ss;
            }
            var = reduce16(dv, lane) * (1.0f / D_);
        }
        const float mB = rbf(mean);
        const float sB = rbf(__builtin_sqrtf(rbf(rbf(var) + 1e-5f)));
        const float rB = __builtin_amdgcn_rcpf(sB);
        const u32 msp = (__float_as_uint(mB) >> 16) | __float_as_uint(sB);
        if (hw == cq && lane < 8) *(LAS u32*)(smem + O_MISC + 4 * lane) = msp;
        LAS char* stg = smem + O_USTG;
        {
            float ul[8], uh[8];
#pragma unroll
            for (int j = 0; j < 8; j++) {
                float m = rdl(mB, j), s = rdl(sB, j), r = rdl(rB, j);
                u32 d = pk_bf16(bflo(xr[j][0]) - m, bfhi(xr[j][0]) - m);
                u32 u = pk_bf16(div_r(bflo(d), s, r), div_r(bfhi(d), s, r));
                ul[j] = bflo(u); uh[j] = bfhi(u);
            }
            *(LAS u32x4*)(stg + (2 * lane) * 64 + hw * 16) = u32x4{pk_f16(ul[0], ul[1]), pk_f16(ul[2], ul[3]), pk_f16(ul[4], ul[5]), pk_f16(ul[6], ul[7])};
            *(LAS u32x4*)(stg + (2 * lane + 1) * 64 + hw * 16) = u32x4{pk_f16(uh[0], uh[1]), pk_f16(uh[2], uh[3]), pk_f16(uh[4], uh[5]), pk_f16(uh[6], uh[7])};
        }
        {
            u32 ms0 = (u32)__builtin_amdgcn_ds_bpermute(8 * q4, (int)msp), ms1 = (u32)__builtin_amdgcn_ds_bpermute(8 * q4 + 4, (int)msp);
            float r0 = __int_as_float(__builtin_amdgcn_ds_bpermute(8 * q4, __float_as_int(rB)));
            float r1 = __int_as_float(__builtin_amdgcn_ds_bpermute(8 * q4 + 4, __float_as_int(rB)));
            float m0 = bflo(ms0), s0 = bfhi(ms0), m1 = bflo(ms1), s1 = bfhi(ms1);
            u32 d0 = pk_bf16(bflo(xe0) - m0, bfhi(xe0) - m0), d1 = pk_bf16(bflo(xe1) - m1, bfhi(xe1) - m1);
            u32 u0 = pk_bf16(div_r(bflo(d0), s0, r0), div_r(bfhi(d0), s0, r0));
            u32 u1 = pk_bf16(div_r(bflo(d1), s1, r1), div_r(bfhi(d1), s1, r1));
            *(LAS u32*)(stg + (128 + 2 * l16) * 64 + hw * 16 + q4 * 4) = pk_f16(bflo(u0), bflo(u1));
            *(LAS u32*)(stg + (129 + 2 * l16) * 64 + hw * 16 + q4 * 4) = pk_f16(bfhi(u0), bfhi(u1));
        }
        lds_signal(flags + FL_USTG, lane);
        lds_wait(flags + FL_USTG, 4);
        STAMP(2);
        const u32 cke = ep * 0x85EBCA77u + 0x165667B1u;
        auto rsu = __builtin_amdgcn_make_buffer_rsrc((void*)a.ubuf, 0, 64 * D_ * 64, 0x00020000);
        {
            const int gbase = (blk * D_ + 160 * cq) * 64;
#pragma unroll
            for (int ps = 0; ps < 3; ps++) {
                const int un = ht + 256 * ps;
                if (un < 640) {
                    u32x4 d = *(LAS const u32x4*)(stg + un * 16);
                    __builtin_amdgcn_raw_buffer_store_b128(ck_expect(d, cke), rsu, gbase + un * 16, 0, 16);
                }
            }
        }
        __builtin_amdgcn_s_setprio(0);
        STAMP(3);
        if (hw < 3) {
            lds_wait(flags + FL_PSH, 2);
            const u32 tag = ep * 0x9E3779B1u + 0x7F4A7C15u;
            LAS const char* p0 = smem + O_SCR + 6 * SCR;
            LAS char* tab = smem + O_KTAB;
            auto rs = __builtin_amdgcn_make_buffer_rsrc((void*)(a.xch + (size_t)b * XCH_STRIDE), 0, XCH_STRIDE, 0x00020000);
            if (ht < 161) {
                f4 sm = *(LAS const f4*)(p0 + ht * 16) + *(LAS const f4*)(p0 + SCR + ht * 16);
                if (ht == 160) sm.y = __int_as_float(max(*(LAS const int*)(p0 + 645 * 4), *(LAS const int*)(p0 + SCR + 645 * 4)));
                *(LAS f4*)(tab + ht * 16) = sm;
                __builtin_amdgcn_raw_buffer_store_b128(u32x4{__float_as_uint(sm.x), tag, __float_as_uint(sm.y), tag}, rs, ht * 16, 0, 16);
                __builtin_amdgcn_raw_buffer_store_b128(u32x4{__float_as_uint(sm.z), tag, __float_as_uint(sm.w), tag}, rs, (162 + ht) * 16, 0, 16);
            }
            lds_signal(flags + FL_SLOC, lane);
        } else {
            const u32x4 ones = {~0u, ~0u, ~0u, ~0u};
#pragma unroll
            for (int i = 0; i < 8; i++) {
                const int un = lane + 64 * i;
                __builtin_amdgcn_raw_buffer_store_b128(ones, rsy, ((un >> 3) * D_ + ch0) * 64 + (un & 7) * 16, 0, 16);
            }
#pragma unroll
            for (int i = 0; i < 2; i++) {
                const int un = lane + 64 * i;
                const int tq = un >> 5;
                const int T = side ? (tq == 0 ? 1 : (tq == 1 ? 6 : (tq == 2 ? 2 : 5))) : (tq == 0 ? 0 : (tq == 1 ? 7 : (tq == 2 ? 3 : 4)));
                __builtin_amdgcn_raw_buffer_store_b128(ones, rsy, ((8 * T + ((un >> 2) & 7)) * D_ + chs) * 64 + (un & 3) * 16, 0, 16);
            }
            asm volatile("s_waitcnt vmcnt(0)" ::: "memory");
            lds_wait(flags + FL_KIMG, 8);
            if (lane == 0) __hip_atomic_store((GAS u64*)(a.clr + b * 64), ((u64)(ep * 0x2545F491u + 0x9E3779B9u) << 32) | (ep * 0xC2B2AE35u + 0x27D4EB2Fu), RLX_AGENT);
        }
        STAMP(4);
        {
            int goff[3], loff[3];
#pragma unroll
            for (int ps = 0; ps < 3; ps++) {
                const int un = ht + 256 * ps;
                const int rb = un / 12, w12 = un - rb * 12;
                goff[ps] = (rb * D_ + cbase) * 64 + w12 * 16;
                const int c = w12 >> 2;
                const int slot = side ? (c == 0 ? 2 : c - 1) : c;
                loff[ps] = O_UIMG + slot * UIMG_CH + 512 + rb * 64 + (w12 & 3) * 16;
            }
            u32x4 d[3];
            for (u32 spins = 0; spins < SPIN_MAX; spins++) {
                bool ok = true;
#pragma unroll
                for (int ps = 0; ps < 3; ps++) d[ps] = __builtin_amdgcn_raw_buffer_load_b128(rsu, goff[ps], 0, 16);
#pragma unroll
                for (int ps = 0; ps < 3; ps++) {
                    u32x4 ex = ck_expect(d[ps], cke);
                    ok &= ex.x == d[ps].x && ex.y == d[ps].y && ex.z == d[ps].z && ex.w == d[ps].w;
                }
                if (__all(ok)) break;
                __builtin_amdgcn_s_sleep(8);
            }
#pragma unroll
            for (int ps = 0; ps < 3; ps++)
                *(LAS u32x4*)(smem + loff[ps]) = u32x4{d[ps].x & ~CKM, d[ps].y & ~CKM, d[ps].z & ~CKM, d[ps].w & ~CKM};
        }
        lds_signal(flags + FL_UIMG, lane);
        STAMP(5);
        if (hw == 2) {
            const u64 want = ((u64)(ep * 0x2545F491u + 0x9E3779B9u) << 32) | (ep * 0xC2B2AE35u + 0x27D4EB2Fu);
            __builtin_amdgcn_s_sleep(32);
            for (u32 spins = 0; spins < SPIN_MAX; spins++) {
                bool ok = true;
#pragma unroll
                for (int k = 0; k < 4; k++) ok &= __hip_atomic_load((GAS u64*)(a.clr + (lane + 64 * k) * 64), RLX_AGENT) == want;
                if (__all(ok)) break;
                __builtin_amdgcn_s_sleep(16);
            }
            if (b == 0 && lane == 0) __hip_atomic_store((GAS u32*)a.epoch, ep + 1u, RLX_AGENT);
            lds_signal(flags + FL_CLR, lane);
        }
    }
    const int ft = tid - 256, f2t = tid - 512;
    if (wave >= 8) {
        if (is_f1) {
            const u16* wm = (f1u >= 48 ? a.wu : a.wg) + (f1u >= 48 ? f1u - 48 : f1u) * 32 + (f2t & 3) * 8;
#pragma unroll
            for (int i = 0; i < 10; i++) wpf[i] = *(const GAS u32x4*)(wm + (size_t)((f2t >> 2) + 64 * i) * F_);
        } else if (is_f2) {
            const u16* wm = a.wd + f2u * 16 + (f2t & 1) * 8;
#pragma unroll
            for (int i = 0; i < 12; i++) wpf[i] = *(const GAS u32x4*)(wm + (size_t)((f2t >> 1) + 128 * i) * D_);
        }
    }
    const u32 tag2 = ep * 0x9E3779B1u + 0x3C6EF372u;
    const int row = chunk * 8 + (wave & 7);
    u32 xp[5], dp[5], wp[5];
    if (wave < 8) {
        const GAS u32* xr = (const GAS u32*)(a.x + (size_t)row * D_);
#pragma unroll
        for (int i = 0; i < 5; i++) { xp[i] = xr[lane + 64 * i]; dp[i] = ((const GAS u32*)a.Dskip)[lane + 64 * i]; wp[i] = ((const GAS u32*)a.wr)[lane + 64 * i]; }
    }
    if ((wave >= 4 && wave < 8) || wave == 0) {
        const int pt = wave == 0 ? 256 + lane : ft;
        lds_wait(flags + FL_CLR, 1);
        lds_wait(flags + FL_CONV, 4);
        __builtin_amdgcn_s_sleep(YDELAY);
        STAMP(8);
        const int yo = ((chunk >> 2) * D_ + 2 * pt) * 64 + (chunk & 3) * 16;
        u32x4 y0, y1;
        for (u32 spins = 0; spins < SPIN_MAX; spins++) {
            y0 = __builtin_amdgcn_raw_buffer_load_b128(rsy, yo, 0, 16);
            y1 = __builtin_amdgcn_raw_buffer_load_b128(rsy, yo + 64, 0, 16);
            bool bad = false;
#pragma unroll
            for (int k = 0; k < 4; k++) {
                bad |= (y0[k] + 0x00010000u) < 0x00010000u || (u16)y0[k] == 0xffffu;
                bad |= (y1[k] + 0x00010000u) < 0x00010000u || (u16)y1[k] == 0xffffu;
            }
            if (!__any(bad)) break;
            __builtin_amdgcn_s_sleep(8);
        }
        LAS char* ys = smem + O_YS;
#pragma unroll
        for (int k = 0; k < 4; k++) {
            *(LAS u32*)(ys + (2 * k) * YS + 4 * pt) = __builtin_amdgcn_perm(y1[k], y0[k], 0x05040100u);
            *(LAS u32*)(ys + (2 * k + 1) * YS + 4 * pt) = __builtin_amdgcn_perm(y1[k], y0[k], 0x07060302u);
        }
    }
    raw_barrier();
    STAMP(9);
    u32 outp[5], key = 0;
    if (wave < 8) {
        const u32 stp = *(LAS const u32*)(smem + O_MISC + 4 * wave);
        const float m1 = bflo(stp), s1 = bfhi(stp), r1 = __builtin_amdgcn_rcpf(s1);
        float h[10];
#pragma unroll
        for (int i = 0; i < 5; i++) {
            u32 yp = *(LAS const u32*)(smem + O_YS + wave * YS + 4 * (lane + 64 * i));
            float xa = bflo(xp[i]), xb = bfhi(xp[i]);
            u32 d = pk_bf16(xa - m1, xb - m1);
            u32 u = pk_bf16(div_r(bflo(d), s1, r1), div_r(bfhi(d), s1, r1));
            u32 sk = pk_bf16(bflo(u) * bflo(dp[i]), bfhi(u) * bfhi(dp[i]));
            u32 aa = pk_bf16(bflo(yp) + bflo(sk), bfhi(yp) + bfhi(sk));
            u32 hh = pk_bf16(xa + bflo(aa), xb + bfhi(aa));
            h[2 * i] = bflo(hh); h[2 * i + 1] = bfhi(hh);
        }
        float m2, s2;
        ln_stats(h, m2, s2);
        const float r2 = __builtin_amdgcn_rcpf(s2);
        float racc = 0.f;
        u32 x2p[5];
#pragma unroll
        for (int i = 0; i < 5; i++) {
            u32 d = pk_bf16(h[2 * i] - m2, h[2 * i + 1] - m2);
            u32 q = pk_bf16(div_r(bflo(d), s2, r2), div_r(bfhi(d), s2, r2));
            x2p[i] = q;
            racc = fmaf(bflo(q), bflo(wp[i]), racc);
            racc = fmaf(bfhi(q), bfhi(wp[i]), racc);
            outp[i] = pk_bf16(h[2 * i] + bflo(q), h[2 * i + 1] + bfhi(q));
        }
        key = sort_key(rbf(wave_sum(racc)), row);
        if (lane == 0) *(LAS u32*)(smem + O_MISC + 32 + 4 * wave) = key;
        raw_barrier();
        u32x4 k0 = *(LAS const u32x4*)(smem + O_MISC + 32), k1 = *(LAS const u32x4*)(smem + O_MISC + 48);
        u32 lbest = max(max(max(k0.x, k0.y), max(k0.z, k0.w)), max(max(k1.x, k1.y), max(k1.z, k1.w)));
        if (key == lbest) {
            auto rc = __builtin_amdgcn_make_buffer_rsrc((void*)(a.cand + (size_t)chunk * CAND_STRIDE), 0, CAND_STRIDE, 0x00020000);
            if (lane == 0) __hip_atomic_store((GAS u64*)(a.keys + chunk * 64), ((u64)tag2 << 32) | key, RLX_AGENT);
#pragma unroll
            for (int i = 0; i < 5; i++) {
                u32x4 c;
                c.x = (x2p[i] & 0xffffu) | (__float_as_uint(h[2 * i]) & 0xffff0000u);
                c.z = (x2p[i] >> 16) | (__float_as_uint(h[2 * i + 1]) & 0xffff0000u);
                c.y = tag2; c.w = tag2;
                __builtin_amdgcn_raw_buffer_store_b128(c, rc, (lane + 64 * i) * 16, 0, 16);
            }
        } else {
#pragma unroll
            for (int i = 0; i < 5; i++) ((GAS u32*)(a.out + (size_t)row * D_))[lane + 64 * i] = outp[i];
            key = 0;
        }
    } else {
        raw_barrier();
        if (wave == 11) {
            __builtin_amdgcn_s_sleep(KDELAY);
            u32 best = 0;
            for (u32 spins = 0; spins < SPIN_MAX; spins++) {
                bool ok = true; u32 mx = 0;
#pragma unroll
                for (int k = 0; k < 4; k++) {
                    u64 v = __hip_atomic_load((GAS u64*)(a.keys + (lane + 64 * k) * 64), RLX_AGENT);
                    ok &= (u32)(v >> 32) == tag2; mx = max(mx, (u32)v);
                }
                best = mx;
                if (__all(ok)) break;
                __builtin_amdgcn_s_sleep(4);
            }
            best = wave_umax(best);
            STAMP(10);
            if (lane == 0) *(LAS u32*)(smem + O_MISC + 64) = best;
            if (is_f1 || is_f2) {
                const int wrow = 2047 - (int)(best & 0xffffu);
                auto rc = __builtin_amdgcn_make_buffer_rsrc((void*)(a.cand + (size_t)(wrow >> 3) * CAND_STRIDE), 0, CAND_STRIDE, 0x00020000);
                u32x4 c[5];
                for (u32 spins = 0; spins < SPIN_MAX; spins++) {
                    bool ok = true;
#pragma unroll
                    for (int i = 0; i < 5; i++) c[i] = __builtin_amdgcn_raw_buffer_load_b128(rc, (lane + 64 * i) * 16, 0, 16);
#pragma unroll
                    for (int i = 0; i < 5; i++) ok &= c[i].y == tag2 && c[i].w == tag2;
                    if (__all(ok)) break;
                    __builtin_amdgcn_s_sleep(2);
                }
#pragma unroll
                for (int i = 0; i < 5; i++) *(LAS u32x2*)(smem + O_XSEL + (lane + 64 * i) * 8) = u32x2{c[i].x, c[i].z};
            }
        }
    }
    raw_barrier();
    STAMP(11);
    const u32 best = *(LAS const u32*)(smem + O_MISC + 64);
    if (wave < 8 && key != 0 && key != best) {
#pragma unroll
        for (int i = 0; i < 5; i++) ((GAS u32*)(a.out + (size_t)row * D_))[lane + 64 * i] = outp[i];
    }
    if (is_f1) {
        if (wave >= 8) {
            float acc[8];
#pragma unroll
            for (int k = 0; k < 8; k++) acc[k] = 0.f;
#pragma unroll
            for (int i = 0; i < 10; i++) {
                float xs = bflo(*(LAS const u32*)(smem + O_XSEL + ((f2t >> 2) + 64 * i) * 4));
#pragma unroll
                for (int k = 0; k < 4; k++) { acc[2 * k] = fmaf(xs, bflo(wpf[i][k]), acc[2 * k]); acc[2 * k + 1] = fmaf(xs, bfhi(wpf[i][k]), acc[2 * k + 1]); }
            }
            const bool b3 = lane & 8, b2_ = lane & 4;
            float w4[4], z2[2];
#pragma unroll
            for (int k = 0; k < 4; k++) { float keep = b3 ? acc[k + 4] : acc[k], give = b3 ? acc[k] : acc[k + 4]; w4[k] = keep + dpp_get<0x128>(give); }
#pragma unroll
            for (int k = 0; k < 2; k++) {
                float keep = b2_ ? w4[k + 2] : w4[k], give = b2_ ? w4[k] : w4[k + 2];
                float up = dpp_get<0x104>(give), dn = dpp_get<0x114>(give);
                z2[k] = keep + (b2_ ? dn : up);
            }
            z2[0] = xrow_sum(z2[0]); z2[1] = xrow_sum(z2[1]);
            if (lane < 16) *(LAS f2*)(smem + O_RED + (wave - 8) * 128 + ((lane & 3) * 8 + (b3 ? 4 : 0) + (b2_ ? 2 : 0)) * 4) = f2{z2[0], z2[1]};
        }
        raw_barrier();
        if (tid < 8) {
            f4 sm = {0, 0, 0, 0};
#pragma unroll
            for (int w = 0; w < 4; w++) sm += *(LAS const f4*)(smem + O_RED + w * 128 + tid * 16);
            auto rg = __builtin_amdgcn_make_buffer_rsrc((void*)a.gu, 0, 96 * 128, 0x00020000);
            __builtin_amdgcn_raw_buffer_store_b128(u32x4{pk_bf16(sm.x, sm.y), tag2, pk_bf16(sm.z, sm.w), tag2}, rg, f1u * 128 + tid * 16, 0, 16);
        }
    } else if (is_f2) {
        if (wave < 6) {
            auto rg = __builtin_amdgcn_make_buffer_rsrc((void*)a.gu, 0, 96 * 128, 0x00020000);
            __builtin_amdgcn_s_sleep(GDELAY);
            u32x4 g, u;
            for (u32 spins = 0; spins < SPIN_MAX; spins++) {
                g = __builtin_amdgcn_raw_buffer_load_b128(rg, tid * 16, 0, 16);
                u = __builtin_amdgcn_raw_buffer_load_b128(rg, (384 + tid) * 16, 0, 16);
                bool ok = g.y == tag2 && g.w == tag2 && u.y == tag2 && u.w == tag2;
                if (__all(ok)) break;
                __builtin_amdgcn_s_sleep(2);
            }
            float gv[4] = {bflo(g.x), bfhi(g.x), bflo(g.z), bfhi(g.z)}, uv[4] = {bflo(u.x), bfhi(u.x), bflo(u.z), bfhi(u.z)};
            f4 av;
#pragma unroll
            for (int k = 0; k < 4; k++) { float sg = rbf(gv[k] / (1.0f + __expf(-gv[k]))); av[k] = rbf(sg * uv[k]); }
            *(LAS f4*)(smem + O_ACT + tid * 16) = av;
        }
        raw_barrier();
        STAMP(13);
        if (wave >= 8) {
            float acc[8];
#pragma unroll
            for (int k = 0; k < 8; k++) acc[k] = 0.f;
#pragma unroll
            for (int i = 0; i < 12; i++) {
                float av = *(LAS const float*)(smem + O_ACT + ((f2t >> 1) + 128 * i) * 4);
#pragma unroll
                for (int k = 0; k < 4; k++) { acc[2 * k] = fmaf(av, bflo(wpf[i][k]), acc[2 * k]); acc[2 * k + 1] = fmaf(av, bfhi(wpf[i][k]), acc[2 * k + 1]); }
            }
            const bool b3 = lane & 8, b2_ = lane & 4, b1 = lane & 2;
            float w4[4], z2[2];
#pragma unroll
            for (int k = 0; k < 4; k++) { float keep = b3 ? acc[k + 4] : acc[k], give = b3 ? acc[k] : acc[k + 4]; w4[k] = keep + dpp_get<0x128>(give); }
#pragma unroll
            for (int k = 0; k < 2; k++) {
                float keep = b2_ ? w4[k + 2] : w4[k], give = b2_ ? w4[k] : w4[k + 2];
                float up = dpp_get<0x104>(give), dn = dpp_get<0x114>(give);
                z2[k] = keep + (b2_ ? dn : up);
            }
            float keep = b1 ? z2[1] : z2[0], give = b1 ? z2[0] : z2[1];
            float r = keep + dpp_get<0x4E>(give);
            r = xrow_sum(r);
            if (lane < 16) *(LAS float*)(smem + O_RED + (wave - 8) * 64 + ((lane & 1) * 8 + (b3 ? 4 : 0) + (b2_ ? 2 : 0) + (b1 ? 1 : 0)) * 4) = r;
        }
        raw_barrier();
        if (tid < 16) {
            float y = 0.f;
#pragma unroll
            for (int w = 0; w < 4; w++) y += *(LAS const float*)(smem + O_RED + w * 64 + tid * 4);
            y = rbf(y);
            const u32 ku = best >> 16;
            const u32 rb = (ku & 0x8000u) ? (ku & 0x7fffu) : (~ku & 0xffffu);
            const float r = __uint_as_float(rb << 16);
            const float rs = rbf(1.0f / (1.0f + __expf(-r)));
            y = rbf(y * rs);
            const u32 cv = *(LAS const u32*)(smem + O_XSEL + (f2u * 16 + tid) * 4);
            const float bb = rbf(bflo(cv) + y);
            const int wrow = 2047 - (int)(best & 0xffffu);
            a.out[(size_t)wrow * D_ + f2u * 16 + tid] = (u16)(pk_bf16(bfhi(cv) + bb, 0.f) & 0xffffu);
        }
    }
    STAMP(12);
}

extern "C" void kernel_launch(void* const* d_in, const int* in_sizes, int n_in,
                              void* d_out, int out_size, void* d_ws, size_t ws_size,
                              hipStream_t stream) {
    char* ws = (char*)d_ws;
    MainArgs a;
    a.epoch = (u32*)ws;             ws += 256;
    a.ybuf = ws;                    ws += L_ * D_ * 2;
    a.ubuf = ws;                    ws += L_ * D_ * 2;
    a.xch = ws;                     ws += 256 * XCH_STRIDE;
    a.clr = ws;                     ws += 256 * 64;
    a.keys = ws;                    ws += 256 * 64;
    a.cand = ws;                    ws += 256 * CAND_STRIDE;
    a.gu = ws;                      ws += 96 * 128;
    (void)hipFuncSetAttribute((const void*)k_main, hipFuncAttributeMaxDynamicSharedMemorySize, LDS_MAIN);
    a.x = (const u16*)d_in[0]; a.Are = (const u16*)d_in[1]; a.Aim = (const u16*)d_in[2]; a.Bre = (const u16*)d_in[3]; a.Bim = (const u16*)d_in[4];
    a.Cre = (const u16*)d_in[5]; a.Cim = (const u16*)d_in[6]; a.logdt = (const u16*)d_in[7]; a.Dskip = (const u16*)d_in[8];
    a.wr = (const u16*)d_in[10]; a.wg = (const u16*)d_in[11]; a.wu = (const u16*)d_in[12]; a.wd = (const u16*)d_in[13];
    a.out = (u16*)d_out;
    a.dbg = (u64*)((char*)d_ws + (128u << 20));
    k_main<<<256, NT, LDS_MAIN, stream>>>(a);
}
```

```cpp
#include <hip/hip_runtime.h>
#include <stdint.h>

typedef unsigned short u16;
typedef unsigned int u32;
typedef unsigned long long u64;
typedef _Float16 h8 __attribute__((ext_vector_type(8)));
typedef _Float16 h2 __attribute__((ext_vector_type(2)));
typedef __bf16 b2 __attribute__((ext_vector_type(2)));
typedef float f2 __attribute__((ext_vector_type(2)));
typedef float f4 __attribute__((ext_vector_type(4)));
typedef u32 u32x2 __attribute__((ext_vector_type(2)));
typedef u32 u32x3 __attribute__((ext_vector_type(3)));
typedef u32 u32x4 __attribute__((ext_vector_type(4)));
#define LAS __attribute__((address_space(3)))
#define GAS __attribute__((address_space(1)))
#define RLX_AGENT __ATOMIC_RELAXED, __HIP_MEMORY_SCOPE_AGENT

constexpr int L_ = 2048, D_ = 640, N_ = 192, F_ = 1536;

__device__ __forceinline__ float bflo(u32 p) { return __uint_as_float(p << 16); }
__device__ __forceinline__ float bfhi(u32 p) { return __uint_as_float(p & 0xffff0000u); }
__device__ __forceinline__ float bf2f(u16 v) { return __uint_as_float(((u32)v) << 16); }
__device__ __forceinline__ u32 pk_bf16(float a, float b) {
    f2 v = {a, b};
    b2 r = __builtin_convertvector(v, b2);
    return __builtin_bit_cast(u32, r);
}
__device__ __forceinline__ u32 pk_f16(float a, float b) {
    f2 v = {a, b};
    h2 r = __builtin_convertvector(v, h2);
    return __builtin_bit_cast(u32, r);
}
__device__ __forceinline__ float rbf(float a) { return bflo(pk_bf16(a, a)); }
__device__ __forceinline__ float dot2bf(u32 a, u32 b, float c) {
    return __builtin_amdgcn_fdot2_f32_bf16(__builtin_bit_cast(b2, a), __builtin_bit_cast(b2, b), c, false);
}

template <int CTRL, int RM = 0xf, int BM = 0xf>
__device__ __forceinline__ float dpp_get(float v) {
    return __int_as_float(__builtin_amdgcn_update_dpp(0, __float_as_int(v), CTRL, RM, BM, false));
}
template <int CTRL, int RM = 0xf, int BM = 0xf>
__device__ __forceinline__ float dpp_add(float v) { return v + dpp_get<CTRL, RM, BM>(v); }
__device__ __forceinline__ float rdl(float v, int l) { return __int_as_float(__builtin_amdgcn_readlane(__float_as_int(v), l)); }
__device__ __forceinline__ float wave_sum(float v) {
    v = dpp_add<0xB1>(v);
    v = dpp_add<0x4E>(v);
    v = dpp_add<0x141>(v);
    v = dpp_add<0x140>(v);
    v = dpp_add<0x142, 0xa>(v);
    v = dpp_add<0x143, 0xc>(v);
    return rdl(v, 63);
}
template <int CTRL, int RM = 0xf, int BM = 0xf>
__device__ __forceinline__ u32 dpp_umax(u32 v) {
    u32 t = (u32)__builtin_amdgcn_update_dpp(0, (int)v, CTRL, RM, BM, false);
    return v > t ? v : t;
}
__device__ __forceinline__ u32 wave_umax(u32 v) {
    v = dpp_umax<0xB1>(v);
    v = dpp_umax<0x4E>(v);
    v = dpp_umax<0x141>(v);
    v = dpp_umax<0x140>(v);
    v = dpp_umax<0x142, 0xa>(v);
    v = dpp_umax<0x143, 0xc>(v);
    return (u32)__builtin_amdgcn_readlane((int)v, 63);
}
__device__ __forceinline__ float xrow_sum(float v) {
    u32 u = __float_as_uint(v);
    auto p = __builtin_amdgcn_permlane16_swap(u, u, false, false);
    v = __uint_as_float(p[0]) + __uint_as_float(p[1]);
    u = __float_as_uint(v);
    auto q = __builtin_amdgcn_permlane32_swap(u, u, false, false);
    return __uint_as_float(q[0]) + __uint_as_float(q[1]);
}
__device__ __forceinline__ float reduce16(const float (&v)[16], int lane) {
    const bool b3 = lane & 8, b2_ = lane & 4, b1 = lane & 2, b0 = lane & 1;
    float w[8], z[4], y[2];
#pragma unroll
    for (int k = 0; k < 8; k++) { float keep = b3 ? v[k + 8] : v[k], give = b3 ? v[k] : v[k + 8]; w[k] = keep + dpp_get<0x140>(give); }
#pragma unroll
    for (int k = 0; k < 4; k++) { float keep = b2_ ? w[k + 4] : w[k], give = b2_ ? w[k] : w[k + 4]; z[k] = keep + dpp_get<0x141>(give); }
#pragma unroll
    for (int k = 0; k < 2; k++) { float keep = b1 ? z[k + 2] : z[k], give = b1 ? z[k] : z[k + 2]; y[k] = keep + dpp_get<0x1B>(give); }
    float keep = b0 ? y[1] : y[0], give = b0 ? y[0] : y[1];
    float r = keep + dpp_get<0xB1>(give);
    return xrow_sum(r);
}

__device__ __forceinline__ void ln_stats(const float (&x)[10], float& m, float& s) {
    float sum = 0.f;
#pragma unroll
    for (int i = 0; i < 10; i++) sum += x[i];
    float mean = wave_sum(sum) * (1.0f / D_);
    float ss = 0.f;
#pragma unroll
    for (int i = 0; i < 10; i++) { float d = x[i] - mean; ss = fmaf(d, d, ss); }
    float var = wave_sum(ss) * (1.0f / D_);
    m = rbf(mean);
    s = rbf(__builtin_sqrtf(rbf(rbf(var) + 1e-5f)));
}
__device__ __forceinline__ float div_r(float a, float s, float r) {
    float q = a * r;
    float e = fmaf(-q, s, a);
    return fmaf(e, r, q);
}

struct cf { float r, i; };
__device__ __forceinline__ cf cmul(cf a, cf b) {
    cf o;
    o.r = fmaf(a.r, b.r, -(a.i * b.i));
    o.i = fmaf(a.r, b.i, a.i * b.r);
    return o;
}
__device__ __forceinline__ cf cmulc(cf a, cf b) {
    cf o;
    o.r = fmaf(a.r, b.r, a.i * b.i);
    o.i = fmaf(a.i, b.r, -(a.r * b.i));
    return o;
}
__device__ __forceinline__ cf csq(cf a) {
    cf o;
    o.r = fmaf(a.r, a.r, -(a.i * a.i));
    o.i = 2.0f * a.r * a.i;
    return o;
}
__device__ __forceinline__ cf cexp_dir(float alpha, float bh, float bl, float x) {
    float mag = __builtin_amdgcn_exp2f(alpha * x);
    float ph = __builtin_amdgcn_fractf(bh * x);
    ph = fmaf(bl, x, ph);
    cf o;
    o.r = mag * __builtin_amdgcn_cosf(ph);
    o.i = mag * __builtin_amdgcn_sinf(ph);
    return o;
}
__device__ __forceinline__ f4 mfma16(h8 a, h8 b, f4 c) { return __builtin_amdgcn_mfma_f32_16x16x32_f16(a, b, c, 0, 0, 0); }
__device__ __forceinline__ void wave_fence() {
    __builtin_amdgcn_fence(__ATOMIC_ACQ_REL, "wavefront");
    __builtin_amdgcn_wave_barrier();
}
__device__ __forceinline__ void lds_signal(LAS u32* w, int lane) {
    asm volatile("s_waitcnt lgkmcnt(0)" ::: "memory");
    if (lane == 0) __hip_atomic_fetch_add(w, 1u, __ATOMIC_RELAXED, __HIP_MEMORY_SCOPE_WORKGROUP);
    asm volatile("" ::: "memory");
}
__device__ __forceinline__ void lds_wait(LAS u32* w, u32 want) {
    asm volatile("" ::: "memory");
    while (__builtin_amdgcn_readfirstlane(__hip_atomic_load(w, __ATOMIC_RELAXED, __HIP_MEMORY_SCOPE_WORKGROUP)) < want)
        __builtin_amdgcn_s_sleep(0);
    asm volatile("" ::: "memory");
}

constexpr int NT = 832;
#define YDELAY 16
#define KDELAY 8
#define GDELAY 16

constexpr int UIMG_CH = (256 + L_) * 2;
constexpr int O_UIMG = 0;
constexpr int KCS = 4160;
constexpr int KIMG_CH = 4 * KCS;
constexpr int O_KIMG = O_UIMG + 3 * UIMG_CH;
constexpr int O_KTAB = O_KIMG + 3 * KIMG_CH;
constexpr int KTAB_CH = 656 * 4;
constexpr int O_ACCX = O_KTAB + KTAB_CH;
constexpr int O_USTG = O_ACCX;
constexpr int SCR = 32 * 272;
constexpr int O_SCR = O_ACCX + 2 * 8192;
constexpr int O_FLAG = O_SCR + 8 * SCR;
constexpr int LDS_MAIN = O_FLAG + 64 + 128;
static_assert(O_KIMG % 16 == 0 && O_KTAB % 16 == 0 && O_ACCX % 16 == 0 && O_FLAG % 16 == 0 && O_SCR % 16 == 0, "align");
enum { FL_POWN = 0, FL_PSH, FL_SLOC, FL_KIMG, FL_UIMG, FL_ACC0, FL_ACC1, FL_STAB, FL_SIMG, FL_USTG, FL_XRD, FL_CLR, FL_CONV };
constexpr int O_MISC = O_FLAG + 64;
constexpr int YS = 1280 + 16;
constexpr int O_YS = O_SCR;
constexpr int O_XSEL = O_YS + 8 * YS;
constexpr int O_ACT = O_XSEL + 2560;
constexpr int O_RED = O_ACT + 6144;

struct PosTab { u16 p[512][8]; };
constexpr int rne_lag(int j) {
    if (j < 256) return j;
    int sh = j < 512 ? 1 : (j < 1024 ? 2 : 3);
    int q = j >> sh, rem = j & ((1 << sh) - 1), half = 1 << (sh - 1);
    if (rem > half || (rem == half && (q & 1))) q++;
    return q << sh;
}
constexpr int ktab_pos_c(int j) {
    if (j < 0 || j >= 2048) return 644;
    int l = rne_lag(j);
    if (l < 256) return l;
    if (l < 512) return 256 + ((l - 256) >> 1);
    if (l < 1024) return 384 + ((l - 512) >> 2);
    return 512 + ((l - 1024) >> 3);
}
constexpr PosTab make_postab() {
    PosTab t{};
    for (int c = 0; c < 512; c++) for (int i = 0; i < 8; i++) t.p[c][i] = (u16)(4 * ktab_pos_c(2047 - 4 * c - i));
    return t;
}
__device__ const __attribute__((aligned(16))) PosTab g_postab = make_postab();

#define STAMP(i)

constexpr int XCH_STRIDE = 5376;
constexpr int CAND_STRIDE = 5120;
struct MainArgs {
    const u16 *x, *Are, *Aim, *Bre, *Bim, *Cre, *Cim, *logdt, *Dskip, *wr, *wg, *wu, *wd;
    u16* out;
    u32* epoch;
    char* xch;
    char* ubuf;
    char* ybuf;
    char* clr;
    char* keys;
    char* cand;
    char* gu;
    u64* dbg;
};
constexpr u32 SPIN_MAX = 1u << 16;

__device__ __forceinline__ void lds_w32(LAS char* p, int off, u32 v) { *(LAS u32*)(p + off) = v; }

typedef float f16v __attribute__((ext_vector_type(16)));
__device__ __forceinline__ f2 rot(f2 b) { return f2{-b.y, b.x}; }
__device__ __forceinline__ f2 cm(f2 a, f2 b, f2 rb) { return __builtin_elementwise_fma(a.yy, rb, a.xx * b); }
__device__ __forceinline__ f2 csq2(f2 a) { return f2{fmaf(a.x, a.x, -(a.y * a.y)), 2.0f * a.x * a.y}; }
template <bool CONJ>
__device__ __forceinline__ f2 cexp2(float alpha, float bh, float bl, float x) {
    float mag = __builtin_amdgcn_exp2f(alpha * x);
    float ph = __builtin_amdgcn_fractf(bh * x);
    ph = fmaf(bl, x, ph);
    f2 cs = {__builtin_amdgcn_cosf(ph), __builtin_amdgcn_sinf(CONJ ? -ph : ph)};
    return cs * mag;
}
__device__ __forceinline__ void put(LAS char* sp, int row, f2 v) { lds_w32(sp, row * 272, pk_f16(v.x, v.y)); }

__device__ __forceinline__ h8 ld_b(LAS const char* ub, int t, int mu) { return *(LAS const h8*)(ub + 512 * t + 64 * (7 - mu)); }
__device__ __forceinline__ h8 ld_a(LAS const char* kl, LAS const char* kh, int M, int mu) {
    u32x2 lo = *(LAS const u32x2*)(kl + 512 * (7 - M) + 64 * (7 - mu));
    u32x2 hi = *(LAS const u32x2*)(kh + 512 * (7 - M) + 64 * (7 - mu));
    u32x4 a4 = {lo.x, lo.y, hi.x, hi.y};
    return __builtin_bit_cast(h8, a4);
}
template <int MU0, int MU1>
__device__ __forceinline__ void conv_run(f4 (&acc)[8], LAS const char* ub, LAS const char* kl, LAS const char* kh) {
    h8 A[8], B[8];
#pragma unroll
    for (int i = 0; i < 8; i++) { B[7 - i] = ld_b(ub, 7 - i, MU0); A[i] = ld_a(kl, kh, i, MU0); __builtin_amdgcn_sched_barrier(0); }
#pragma unroll
    for (int mu = MU0; mu < MU1; mu++) {
#pragma unroll
        for (int M = 0; M < 8; M++) {
#pragma unroll
            for (int t = 7 - M; t >= 0; t--) acc[t + M] = mfma16(A[M], B[t], acc[t + M]);
            if (mu + 1 < MU1) { A[M] = ld_a(kl, kh, M, mu + 1); B[7 - M] = ld_b(ub, 7 - M, mu + 1); }
            __builtin_amdgcn_sched_barrier(0);
        }
    }
}
template <int TA, int TB>
__device__ __forceinline__ void conv_pair(f4& a0, f4& a1, LAS const char* ub, LAS const char* kl, LAS const char* kh) {
    h8 A[TB + 1], B[TB + 1];
    f4 a2 = {0, 0, 0, 0};
#pragma unroll
    for (int i = 0; i <= TB; i++) { B[TB - i] = ld_b(ub, TB - i, 0); A[i] = ld_a(kl, kh, i, 0); __builtin_amdgcn_sched_barrier(0); }
#pragma unroll
    for (int mu = 0; mu < 8; mu++) {
#pragma unroll
        for (int M = 0; M <= TB; M++) {
            if (M & 1) a2 = mfma16(A[M], B[TB - M], a2); else a1 = mfma16(A[M], B[TB - M], a1);
            if (M <= TA) a0 = mfma16(A[M], B[TA - M], a0);
            if (mu + 1 < 8) { A[M] = ld_a(kl, kh, M, mu + 1); B[TB - M] = ld_b(ub, TB - M, mu + 1); }
            __builtin_amdgcn_sched_barrier(0);
        }
    }
    a1 += a2;
}

typedef __amdgpu_buffer_rsrc_t rsrc_t;
__device__ __forceinline__ void store_y_tile(rsrc_t rsy, int ch, int T, int lane, f4 a, float sc) {
    u32x2 o;
    o.x = pk_bf16(a.x * sc, a.y * sc);
    o.y = pk_bf16(a.z * sc, a.w * sc);
    const int n = lane & 15, g = lane >> 4;
    __builtin_amdgcn_raw_buffer_store_b64(o, rsy, (((n >> 1) * D_ + ch) * 64 + (n & 1) * 32 + g * 8) + T * (8 * D_ * 64), 0, 16);
}
__device__ __forceinline__ void raw_barrier() {
    asm volatile("s_waitcnt lgkmcnt(0)\n\ts_barrier" ::: "memory");
}
__device__ __forceinline__ u32 sort_key(float r, int row) {
    u32 u = __float_as_uint(r) >> 16;
    u = (u & 0x8000u) ? (~u & 0xffffu) : (u | 0x8000u);
    return (u << 16) | (u32)(2047 - row);
}

__device__ __forceinline__ void build_chunk(LAS char* kimg, LAS const char* tab, u32x4 pp, int c) {
    float v[7];
#pragma unroll
    for (int i = 0; i < 7; i++) v[i] = *(LAS const float*)(tab + ((i & 1) ? (pp[i >> 1] >> 16) : (pp[i >> 1] & 0xffffu)));
    u32 p01 = pk_f16(v[0], v[1]), p23 = pk_f16(v[2], v[3]), p45 = pk_f16(v[4], v[5]);
    u32 p12 = pk_f16(v[1], v[2]), p34 = pk_f16(v[3], v[4]), p56 = pk_f16(v[5], v[6]);
    *(LAS u32x2*)(kimg + 0 * KCS + 32 + 8 * c) = u32x2{p01, p23};
    *(LAS u32x2*)(kimg + 1 * KCS + 32 + 8 * c) = u32x2{p12, p34};
    *(LAS u32x2*)(kimg + 2 * KCS + 32 + 8 * c) = u32x2{p23, p45};
    *(LAS u32x2*)(kimg + 3 * KCS + 32 + 8 * c) = u32x2{p34, p56};
}

__device__ __forceinline__ void build_chunk3(LAS char* kimg, LAS const char* part, const u32 (&po)[7], int c, float sc) {
    float v[7];
#pragma unroll
    for (int i = 0; i < 7; i++)
        v[i] = (*(LAS const float*)(part + po[i]) + *(LAS const float*)(part + po[i] + SCR) + *(LAS const float*)(part + po[i] + 2 * SCR)) * sc;
    u32 p01 = pk_f16(v[0], v[1]), p23 = pk_f16(v[2], v[3]), p45 = pk_f16(v[4], v[5]);
    u32 p12 = pk_f16(v[1], v[2]), p34 = pk_f16(v[3], v[4]), p56 = pk_f16(v[5], v[6]);
    *(LAS u32x2*)(kimg + 0 * KCS + 32 + 8 * c) = u32x2{p01, p23};
    *(LAS u32x2*)(kimg + 1 * KCS + 32 + 8 * c) = u32x2{p12, p34};
    *(LAS u32x2*)(kimg + 2 * KCS + 32 + 8 * c) = u32x2{p23, p45};
    *(LAS u32x2*)(kimg + 3 * KCS + 32 + 8 * c) = u32x2{p34, p56};
}

constexpr u32 CKM = 0x00070007u;
__device__ __forceinline__ u32x4 ck_expect(u32x4 d, u32 e) {
    u32 m0 = d.x & ~CKM, m1 = d.y & ~CKM, m2 = d.z & ~CKM, m3 = d.w & ~CKM;
    u32 s = (m0 + m1) + (m2 + m3) + e;
    s ^= s >> 9;
    u32x4 o;
    o.x = m0 | (s & CKM);
    o.y = m1 | (__builtin_amdgcn_alignbit(s, s, 3) & CKM);
    o.z = m2 | (__builtin_amdgcn_alignbit(s, s, 6) & CKM);
    o.w = m3 | (__builtin_amdgcn_alignbit(s, s, 9) & CKM);
    return o;
}

__global__ void __launch_bounds__(NT, 4) k_main(MainArgs a) {
    extern __shared__ __attribute__((aligned(16))) char smem_[];
    LAS char* smem = (LAS char*)smem_;
    u64 pc; u32 csz;
    asm volatile("s_getpc_b64 %0\n.Lk_begin:\n\ts_mov_b32 %1, .Lk_end-.Lk_begin" : "=s"(pc), "=s"(csz));
    const int tid = threadIdx.x, lane = tid & 63;
    const int wave = __builtin_amdgcn_readfirstlane(tid >> 6);
    const int b = blockIdx.x;
    const int pairi = (b & 7) * 16 + (b >> 4), side = (b >> 3) & 1;
    const int cbase = 5 * pairi + 2 * side;
    const int ch0 = side ? cbase + 1 : cbase, ch1 = ch0 + 1, chs = 5 * pairi + 2;
    const int chunk = (b & 7) * 32 + (b >> 3);
    LAS u32* flags = (LAS u32*)(smem + O_FLAG);
    if (wave == 12) {
        if (csz > 49152u || csz < 4096u) csz = 128u;
        u32 acc = 0;
#pragma unroll
        for (int k = 0; k < 6; k++) acc ^= *(const GAS u32*)((pc & ~127ull) + min((u32)(lane * 128 + k * 8192), csz - 128u));
        const char* p;
        if (lane < 31) p = (const char*)a.epoch + (size_t)lane * (256 << 10);
        else if (lane < 39) p = (const char*)a.wg + (size_t)(lane - 31) * (240 << 10);
        else if (lane < 47) p = (const char*)a.wu + (size_t)(lane - 39) * (240 << 10);
        else if (lane < 55) p = (const char*)a.wd + (size_t)(lane - 47) * (240 << 10);
        else if (lane == 55) p = (const char*)a.Dskip;
        else if (lane == 56) p = (const char*)a.wr;
        else p = (const char*)(a.out + (size_t)(chunk * 8 + (lane - 57)) * D_);
        acc ^= *(const GAS u32*)p;
        asm volatile("" :: "v"(acc));
        return;
    }
    STAMP(0);
    const u32 ep = __hip_atomic_load((GAS u32*)a.epoch, RLX_AGENT);
    const rsrc_t rsy = __builtin_amdgcn_make_buffer_rsrc((void*)a.ybuf, 0, 64 * D_ * 64, 0x00020000);
    const int jl = b >> 3;
    const bool is_f1 = jl < 12, is_f2 = jl >= 12 && jl < 17;
    const int f1u = (b & 7) * 12 + jl;
    const int f2u = (b & 7) * 5 + (jl - 12);
    u32x4 wpf[12];
#pragma unroll
    for (int i = 0; i < 12; i++) wpf[i] = u32x4{0, 0, 0, 0};

    if (wave < 8) {
        const int slot_g = wave < 3 ? 0 : (wave < 6 ? 1 : 2);
        const int ch_g = slot_g == 0 ? ch0 : (slot_g == 1 ? ch1 : chs);
        int mode; bool act = true;
        if (slot_g < 2) mode = (wave - 3 * slot_g) * 64 + lane;
        else { mode = 96 * side + (wave - 6) * 64 + lane; act = (wave == 6) || (lane < 32); if (!act) mode = 0; }
        const int gi = ch_g * N_ + mode;
        u16 pdt = a.logdt[ch_g];
        u16 pAre = a.Are[gi], pAim = a.Aim[gi], pBre = a.Bre[gi], pBim = a.Bim[gi], pCre = a.Cre[gi], pCim = a.Cim[gi];
        u32x4 pp = *(const GAS u32x4*)(&g_postab.p[tid][0]);
        if (tid < 16) flags[tid] = 0;
        if (tid >= 64 && tid < 160) { int t = tid - 64; *(LAS u32x4*)(smem + O_UIMG + (t >> 5) * UIMG_CH + (t & 31) * 16) = u32x4{0, 0, 0, 0}; }
        if (tid >= 192 && tid < 216) { int t = tid - 192; *(LAS u32x4*)(smem + O_KIMG + (t >> 1) * KCS + 4128 + (t & 1) * 16) = u32x4{0, 0, 0, 0}; }
        asm volatile("s_waitcnt lgkmcnt(0)" ::: "memory");
        __builtin_amdgcn_s_barrier();
        float Are = bf2f(pAre), Aim = bf2f(pAim), Bre = bf2f(pBre), Bim = bf2f(pBim), Cre = bf2f(pCre), Cim = bf2f(pCim);
        float dt = rbf(expf(bf2f(pdt)));
        float are = dt * Are, aim = dt * Aim, br = dt * Bre, bi = dt * Bim;
        f2 wc = {fmaf(Cre, br, -(Cim * bi)), -fmaf(Cre, bi, Cim * br)};
        if (!act) wc = f2{0.f, 0.f};
        STAMP(1);
        float alpha = are * 1.44269504088896341f;
        const float CH = 0.15915494f, CL = 6.4206382e-09f;
        float p_ = aim * CH;
        float pe = fmaf(aim, CL, fmaf(aim, CH, -p_));
        float pf = p_ - __builtin_rintf(p_);
        float bh = __uint_as_float(__float_as_uint(pf + pe) & 0xfffff800u);
        float bl = (pf - bh) + pe;
        float mw = fabsf(wc.x) + fabsf(wc.y);
        float v0r = 16384.f;
        if (__any(alpha > 0.f)) {
            float ap = fmaxf(alpha, 0.f);
            float g1 = __builtin_amdgcn_exp2f(ap * 1024.f);
            wc = (wc * g1) * g1;
            mw = (fabsf(wc.x) + fabsf(wc.y)) * __builtin_amdgcn_exp2f(ap * 248.f);
            v0r = __builtin_amdgcn_exp2f(fmaf(ap, -2048.f, 14.f));
        }
        u32 mwu = wave_umax(__float_as_uint(mw));
        int e = (int)(mwu >> 23) - 126;
        if (e < -100) e = -100;
        if (e > 127) e = 127;
        LAS char* scr = smem + O_SCR + wave * SCR;
        LAS char* sp = scr + lane * 4;
        LAS const char* fp = scr + (lane & 31) * 272 + (lane >> 5) * 16;
        {
            f2 E8 = cexp2<true>(alpha, bh, bl, 8.f);
            f2 E16 = csq2(E8), E32 = csq2(E16);
            f2 r8 = rot(E8), r32 = rot(E32);
            f2 Y0 = wc * __builtin_amdgcn_ldexpf(1.0f, 14 - e);
            f2 Y2 = cm(Y0, E16, rot(E16));
            f2 Y1 = cm(Y0, E8, r8), Y3 = cm(Y2, E8, r8);
#pragma unroll
            for (int q = 0; q < 32; q += 4) {
                put(sp, q, Y0); put(sp, q + 1, Y1); put(sp, q + 2, Y2); put(sp, q + 3, Y3);
                if (q < 28) { Y0 = cm(Y0, E32, r32); Y1 = cm(Y1, E32, r32); Y2 = cm(Y2, E32, r32); Y3 = cm(Y3, E32, r32); }
            }
        }
        wave_fence();
        h8 By[8];
#pragma unroll
        for (int k = 0; k < 8; k++) By[k] = *(LAS const h8*)(fp + 32 * k);
        wave_fence();
        {
            f2 E1 = cexp2<false>(alpha, bh, bl, 1.f), E256 = cexp2<false>(alpha, bh, bl, 256.f), E1024 = cexp2<false>(alpha, bh, bl, 1024.f);
            f2 E2 = csq2(E1), E4 = csq2(E2), E512 = csq2(E256), E2048 = csq2(E1024);
            f2 r1 = rot(E1), r256 = rot(E256);
            f2 E768 = cm(E512, E256, r256);
            f2 V2 = E2 * v0r, V4 = E4 * v0r;
            f2 rV4 = rot(V4);
            f2 V6 = cm(E2, V4, rV4);
            put(sp, 0, f2{v0r, 0.f}); put(sp, 1, E1 * v0r); put(sp, 2, V2); put(sp, 3, cm(V2, E1, r1));
            put(sp, 4, V4); put(sp, 5, cm(V4, E1, r1)); put(sp, 6, V6); put(sp, 7, cm(V6, E1, r1));
            put(sp, 8, E256 * v0r); put(sp, 9, cm(V2, E256, r256)); put(sp, 10, cm(V4, E256, r256)); put(sp, 11, cm(V6, E256, r256));
            put(sp, 12, E512 * v0r); put(sp, 13, cm(E512, V4, rV4)); put(sp, 14, E768 * v0r); put(sp, 15, cm(E768, V4, rV4));
            f2 H0 = E1024 * v0r;
            f2 H1 = cm(H0, E256, r256), H2 = cm(H1, E256, r256), H3 = cm(H2, E256, r256);
            put(sp, 16, H0); put(sp, 17, H1); put(sp, 18, H2); put(sp, 19, H3); put(sp, 20, E2048 * v0r);
        }
        wave_fence();
        f16v dd = {0, 0, 0, 0, 0, 0, 0, 0, 0, 0, 0, 0, 0, 0, 0, 0};
#pragma unroll
        for (int k = 0; k < 8; k++) dd = __builtin_amdgcn_mfma_f32_32x32x16_f16(*(LAS const h8*)(fp + 32 * k), By[k], dd, 0, 0, 0);
        wave_fence();
        {
            const float ts = __builtin_amdgcn_ldexpf(1.0f, e - 28);
            const int q = lane & 31, h = lane >> 5;
            *(LAS f4*)(scr + (8 * q + 4 * h) * 4) = f4{dd[0], dd[1], dd[2], dd[3]} * ts;
            if (h == 0) {
                *(LAS f4*)(scr + (256 + 4 * q) * 4) = f4{dd[4], dd[5], dd[6], dd[7]} * ts;
                *(LAS float*)(scr + (512 + q) * 4) = dd[8] * ts;
                *(LAS float*)(scr + (544 + q) * 4) = dd[9] * ts;
                *(LAS float*)(scr + (576 + q) * 4) = dd[10] * ts;
                *(LAS float*)(scr + (608 + q) * 4) = dd[11] * ts;
            } else {
                *(LAS f2*)(scr + (384 + 2 * q) * 4) = f2{dd[4], dd[5]} * ts;
                *(LAS f2*)(scr + (448 + 2 * q) * 4) = f2{dd[6], dd[7]} * ts;
                if (q == 0) {
                    *(LAS f4*)(scr + 640 * 4) = f4{dd[8] * ts, 0.f, 0.f, 0.f};
                    *(LAS u32x2*)(scr + 644 * 4) = u32x2{0u, (u32)e};
                }
            }
        }
        STAMP(2);
        lds_signal(flags + (slot_g < 2 ? FL_POWN : FL_PSH), lane);
        u32 po[7];
#pragma unroll
        for (int i = 0; i < 7; i++) po[i] = (i & 1) ? (pp[i >> 1] >> 16) : (pp[i >> 1] & 0xffffu);
        const u32 tag = ep * 0x9E3779B1u + 0x7F4A7C15u;
        asm volatile("" :: "v"(tag));
        lds_wait(flags + FL_POWN, 6);
        STAMP(3);
        float ysc;
        {
            LAS const char* p0 = smem + O_SCR;
            int em0 = max(max(*(LAS const int*)(p0 + 645 * 4), *(LAS const int*)(p0 + SCR + 645 * 4)), *(LAS const int*)(p0 + 2 * SCR + 645 * 4));
            int em1 = max(max(*(LAS const int*)(p0 + 3 * SCR + 645 * 4), *(LAS const int*)(p0 + 4 * SCR + 645 * 4)), *(LAS const int*)(p0 + 5 * SCR + 645 * 4));
            build_chunk3(smem + O_KIMG, p0, po, tid, __builtin_amdgcn_ldexpf(1.0f, 7 - em0));
            build_chunk3(smem + O_KIMG + KIMG_CH, p0 + 3 * SCR, po, tid, __builtin_amdgcn_ldexpf(1.0f, 7 - em1));
            ysc = __builtin_amdgcn_ldexpf(1.0f, (wave < 2 ? em0 : em1) - 7);
        }
        lds_signal(flags + FL_KIMG, lane);
        STAMP(5);

        if (wave < 4) {
            const int n = lane & 15, g = lane >> 4;
            const int slot = wave >> 1;
            const int s = (3 - n) & 3;
            const int uoff = 2 * (256 + 16 * n - 16 + 8 * g) - 64 * 7;
            const int koff = s * KCS + 2 * (2047 - n + 8 * g - s) - 512 * 7 - 64 * 7;
            LAS const char* ub = smem + O_UIMG + slot * UIMG_CH + uoff;
            LAS const char* kl = smem + O_KIMG + slot * KIMG_CH + koff;
            LAS const char* kh = kl + 8;
            asm volatile("" : "+v"(kh));
            f4 acc[8];
#pragma unroll
            for (int t = 0; t < 8; t++) acc[t] = f4{0, 0, 0, 0};
            LAS char* ax = smem + O_ACCX + slot * 8192 + lane * 16;
            lds_wait(flags + FL_KIMG, 8);
            lds_wait(flags + FL_UIMG, 4);
            __builtin_amdgcn_s_setprio(3);
            STAMP(6);
            if ((wave & 1) == 0) {
                conv_run<0, 5>(acc, ub, kl, kh);
                STAMP(7);
                lds_wait(flags + FL_ACC0 + slot, 1);
                const float sc = ysc;
#pragma unroll
                for (int t = 0; t < 8; t++) {
                    f4 o = acc[t] + *(LAS const f4*)(ax + 1024 * t);
                    store_y_tile(rsy, slot ? ch1 : ch0, t, lane, o, sc);
                }
            } else {
                conv_run<5, 8>(acc, ub, kl, kh);
#pragma unroll
                for (int t = 0; t < 8; t++) *(LAS f4*)(ax + 1024 * t) = acc[t];
                lds_signal(flags + FL_ACC0 + slot, lane);
                STAMP(7);
                lds_wait(flags + FL_SIMG, 4);
                LAS const char* ub2 = smem + O_UIMG + 2 * UIMG_CH + uoff;
                LAS const char* kl2 = smem + O_KIMG + 2 * KIMG_CH + koff;
                LAS const char* kh2 = kl2 + 8;
                asm volatile("" : "+v"(kh2));
                float sc = *(LAS const float*)(smem + O_KTAB + 645 * 4);
                f4 a0 = {0, 0, 0, 0}, a1 = a0;
                const int sel = side * 2 + slot;
                if (sel == 0) { conv_pair<0, 7>(a0, a1, ub2, kl2, kh2); store_y_tile(rsy, chs, 0, lane, a0, sc); store_y_tile(rsy, chs, 7, lane, a1, sc); }
                else if (sel == 1) { conv_pair<3, 4>(a0, a1, ub2, kl2, kh2); store_y_tile(rsy, chs, 3, lane, a0, sc); store_y_tile(rsy, chs, 4, lane, a1, sc); }
                else if (sel == 2) { conv_pair<1, 6>(a0, a1, ub2, kl2, kh2); store_y_tile(rsy, chs, 1, lane, a0, sc); store_y_tile(rsy, chs, 6, lane, a1, sc); }
                else { conv_pair<2, 5>(a0, a1, ub2, kl2, kh2); store_y_tile(rsy, chs, 2, lane, a0, sc); store_y_tile(rsy, chs, 5, lane, a1, sc); }
            }
            __builtin_amdgcn_s_setprio(0);
            lds_signal(flags + FL_CONV, lane);
        } else {
            const int ht = tid - 256;
            LAS char* tab = smem + O_KTAB;
            u32x4 pp2 = *(const GAS u32x4*)(&g_postab.p[ht][0]);
            if (ht < 192) {
                auto rs = __builtin_amdgcn_make_buffer_rsrc((void*)(a.xch + (size_t)(b ^ 8) * XCH_STRIDE), 0, XCH_STRIDE, 0x00020000);
                const int hi = ht < 161 ? ht : 160;
                u32x4 v0, v1, v2;
                for (u32 spins = 0; spins < SPIN_MAX; spins++) {
                    v0 = __builtin_amdgcn_raw_buffer_load_b128(rs, hi * 16, 0, 16);
                    v1 = __builtin_amdgcn_raw_buffer_load_b128(rs, (162 + hi) * 16, 0, 16);
                    v2 = __builtin_amdgcn_raw_buffer_load_b128(rs, 160 * 16, 0, 16);
                    bool ok = v0.y == tag && v0.w == tag && v1.y == tag && v1.w == tag && v2.y == tag && v2.w == tag;
                    if (__all(ok)) break;
                    __builtin_amdgcn_s_sleep(4);
                }
                lds_wait(flags + FL_SLOC, 3);
                int em = max(*(LAS const int*)(tab + 641 * 4), (int)v2.z);
                f4 loc = *(LAS const f4*)(tab + hi * 16);
                lds_signal(flags + FL_XRD, lane);
                lds_wait(flags + FL_XRD, 3);
                f4 rem = {__uint_as_float(v0.x), __uint_as_float(v0.z), __uint_as_float(v1.x), __uint_as_float(v1.z)};
                if (ht < 161) *(LAS f4*)(tab + ht * 16) = (loc + rem) * __builtin_amdgcn_ldexpf(1.0f, 7 - em);
                else if (ht == 161) *(LAS f2*)(tab + 644 * 4) = f2{0.f, __builtin_amdgcn_ldexpf(1.0f, em - 7)};
                lds_signal(flags + FL_STAB, lane);
            }
            STAMP(6);
            lds_wait(flags + FL_STAB, 3);
            build_chunk(smem + O_KIMG + 2 * KIMG_CH, tab, pp, tid);
            build_chunk(smem + O_KIMG + 2 * KIMG_CH, tab, pp2, ht);
            lds_signal(flags + FL_SIMG, lane);
            STAMP(7);
        }
    } else {
        __builtin_amdgcn_s_barrier();
        __builtin_amdgcn_s_setprio(2);
        const int hw = wave - 8, ht = tid - 512;
        const int blk = chunk >> 2, cq = chunk & 3;
        const GAS u32* xb = (const GAS u32*)a.x + (size_t)(blk * 32 + 8 * hw) * 320;
        u32 xr[8][5];
        int dofs[5];
#pragma unroll
        for (int i = 0; i < 5; i++) { int d = 80 * cq + lane + 64 * i; dofs[i] = d >= 320 ? d - 320 : d; }
#pragma unroll
        for (int j = 0; j < 8; j++)
#pragma unroll
            for (int i = 0; i < 5; i++) xr[j][i] = xb[j * 320 + dofs[i]];
        const int q4 = lane >> 4, l16 = lane & 15;
        u32 xe0 = xb[(2 * q4) * 320 + 80 * cq + 64 + l16], xe1 = xb[(2 * q4 + 1) * 320 + 80 * cq + 64 + l16];
        float sv[16];
#pragma unroll
        for (int j = 0; j < 8; j++) {
            float s1 = 0.f, s2 = 0.f;
#pragma unroll
            for (int i = 0; i < 5; i++) { s1 = dot2bf(xr[j][i], 0x3f803f80u, s1); s2 = dot2bf(xr[j][i], xr[j][i], s2); }
            sv[j] = s1; sv[8 + j] = s2;
        }
        STAMP(1);
        float tot = reduce16(sv, lane);
        float other = dpp_get<0x128>(tot);
        float S1 = (lane & 8) ? other : tot, S2 = (lane & 8) ? tot : other;
        float mean = S1 * (1.0f / D_);
        float var = fmaf(-mean, mean, S2 * (1.0f / D_));
        if (__any(mean * mean > 16.f * var)) {
            float dv[16];
#pragma unroll
            for (int j = 0; j < 8; j++) {
                float mj = rdl(mean, j), ss = 0.f;
#pragma unroll
                for (int i = 0; i < 5; i++) { float d0 = bflo(xr[j][i]) - mj, d1 = bfhi(xr[j][i]) - mj; ss = fmaf(d0, d0, ss); ss = fmaf(d1, d1, ss); }
                dv[j] = ss; dv[8 + j] = ss;
            }
            var = reduce16(dv, lane) * (1.0f / D_);
        }
        const float mB = rbf(mean);
        const float sB = rbf(__builtin_sqrtf(rbf(rbf(var) + 1e-5f)));
        const float rB = __builtin_amdgcn_rcpf(sB);
        const u32 msp = (__float_as_uint(mB) >> 16) | __float_as_uint(sB);
        if (hw == cq && lane < 8) *(LAS u32*)(smem + O_MISC + 4 * lane) = msp;
        LAS char* stg = smem + O_USTG;
        {
            float ul[8], uh[8];
#pragma unroll
            for (int j = 0; j < 8; j++) {
                float m = rdl(mB, j), s = rdl(sB, j), r = rdl(rB, j);
                u32 d = pk_bf16(bflo(xr[j][0]) - m, bfhi(xr[j][0]) - m);
                u32 u = pk_bf16(div_r(bflo(d), s, r), div_r(bfhi(d), s, r));
                ul[j] = bflo(u); uh[j] = bfhi(u);
            }
            *(LAS u32x4*)(stg + (2 * lane) * 64 + hw * 16) = u32x4{pk_f16(ul[0], ul[1]), pk_f16(ul[2], ul[3]), pk_f16(ul[4], ul[5]), pk_f16(ul[6], ul[7])};
            *(LAS u32x4*)(stg + (2 * lane + 1) * 64 + hw * 16) = u32x4{pk_f16(uh[0], uh[1]), pk_f16(uh[2], uh[3]), pk_f16(uh[4], uh[5]), pk_f16(uh[6], uh[7])};
        }
        {
            u32 ms0 = (u32)__builtin_amdgcn_ds_bpermute(8 * q4, (int)msp), ms1 = (u32)__builtin_amdgcn_ds_bpermute(8 * q4 + 4, (int)msp);
            float r0 = __int_as_float(__builtin_amdgcn_ds_bpermute(8 * q4, __float_as_int(rB)));
            float r1 = __int_as_float(__builtin_amdgcn_ds_bpermute(8 * q4 + 4, __float_as_int(rB)));
            float m0 = bflo(ms0), s0 = bfhi(ms0), m1 = bflo(ms1), s1 = bfhi(ms1);
            u32 d0 = pk_bf16(bflo(xe0) - m0, bfhi(xe0) - m0), d1 = pk_bf16(bflo(xe1) - m1, bfhi(xe1) - m1);
            u32 u0 = pk_bf16(div_r(bflo(d0), s0, r0), div_r(bfhi(d0), s0, r0));
            u32 u1 = pk_bf16(div_r(bflo(d1), s1, r1), div_r(bfhi(d1), s1, r1));
            *(LAS u32*)(stg + (128 + 2 * l16) * 64 + hw * 16 + q4 * 4) = pk_f16(bflo(u0), bflo(u1));
            *(LAS u32*)(stg + (129 + 2 * l16) * 64 + hw * 16 + q4 * 4) = pk_f16(bfhi(u0), bfhi(u1));
        }
        lds_signal(flags + FL_USTG, lane);
        lds_wait(flags + FL_USTG, 4);
        STAMP(2);
        const u32 cke = ep * 0x85EBCA77u + 0x165667B1u;
        auto rsu = __builtin_amdgcn_make_buffer_rsrc((void*)a.ubuf, 0, 64 * D_ * 64, 0x00020000);
        {
            const int gbase = (blk * D_ + 160 * cq) * 64;
#pragma unroll
            for (int ps = 0; ps < 3; ps++) {
                const int un = ht + 256 * ps;
                if (un < 640) {
                    u32x4 d = *(LAS const u32x4*)(stg + un * 16);
                    __builtin_amdgcn_raw_buffer_store_b128(ck_expect(d, cke), rsu, gbase + un * 16, 0, 16);
                }
            }
        }
        __builtin_amdgcn_s_setprio(0);
        STAMP(3);
        if (hw < 3) {
            lds_wait(flags + FL_PSH, 2);
            const u32 tag = ep * 0x9E3779B1u + 0x7F4A7C15u;
            LAS const char* p0 = smem + O_SCR + 6 * SCR;
            LAS char* tab = smem + O_KTAB;
            auto rs = __builtin_amdgcn_make_buffer_rsrc((void*)(a.xch + (size_t)b * XCH_STRIDE), 0, XCH_STRIDE, 0x00020000);
            if (ht < 161) {
                f4 sm = *(LAS const f4*)(p0 + ht * 16) + *(LAS const f4*)(p0 + SCR + ht * 16);
                if (ht == 160) sm.y = __int_as_float(max(*(LAS const int*)(p0 + 645 * 4), *(LAS const int*)(p0 + SCR + 645 * 4)));
                *(LAS f4*)(tab + ht * 16) = sm;
                __builtin_amdgcn_raw_buffer_store_b128(u32x4{__float_as_uint(sm.x), tag, __float_as_uint(sm.y), tag}, rs, ht * 16, 0, 16);
                __builtin_amdgcn_raw_buffer_store_b128(u32x4{__float_as_uint(sm.z), tag, __float_as_uint(sm.w), tag}, rs, (162 + ht) * 16, 0, 16);
            }
            lds_signal(flags + FL_SLOC, lane);
        } else {
            const u32x4 ones = {~0u, ~0u, ~0u, ~0u};
#pragma unroll
            for (int i = 0; i < 8; i++) {
                const int un = lane + 64 * i;
                __builtin_amdgcn_raw_buffer_store_b128(ones, rsy, ((un >> 3) * D_ + ch0) * 64 + (un & 7) * 16, 0, 16);
            }
#pragma unroll
            for (int i = 0; i < 2; i++) {
                const int un = lane + 64 * i;
                const int tq = un >> 5;
                const int T = side ? (tq == 0 ? 1 : (tq == 1 ? 6 : (tq == 2 ? 2 : 5))) : (tq == 0 ? 0 : (tq == 1 ? 7 : (tq == 2 ? 3 : 4)));
                __builtin_amdgcn_raw_buffer_store_b128(ones, rsy, ((8 * T + ((un >> 2) & 7)) * D_ + chs) * 64 + (un & 3) * 16, 0, 16);
            }
            asm volatile("s_waitcnt vmcnt(0)" ::: "memory");
            lds_wait(flags + FL_KIMG, 8);
            if (lane == 0) __hip_atomic_store((GAS u64*)(a.clr + b * 64), ((u64)(ep * 0x2545F491u + 0x9E3779B9u) << 32) | (ep * 0xC2B2AE35u + 0x27D4EB2Fu), RLX_AGENT);
        }
        STAMP(4);
        {
            int goff[3], loff[3];
#pragma unroll
            for (int ps = 0; ps < 3; ps++) {
                const int un = ht + 256 * ps;
                const int rb = un / 12, w12 = un - rb * 12;
                goff[ps] = (rb * D_ + cbase) * 64 + w12 * 16;
                const int c = w12 >> 2;
                const int slot = side ? (c == 0 ? 2 : c - 1) : c;
                loff[ps] = O_UIMG + slot * UIMG_CH + 512 + rb * 64 + (w12 & 3) * 16;
            }
            u32x4 d[3];
            for (u32 spins = 0; spins < SPIN_MAX; spins++) {
                bool ok = true;
#pragma unroll
                for (int ps = 0; ps < 3; ps++) d[ps] = __builtin_amdgcn_raw_buffer_load_b128(rsu, goff[ps], 0, 16);
#pragma unroll
                for (int ps = 0; ps < 3; ps++) {
                    u32x4 ex = ck_expect(d[ps], cke);
                    ok &= ex.x == d[ps].x && ex.y == d[ps].y && ex.z == d[ps].z && ex.w == d[ps].w;
                }
                if (__all(ok)) break;
                __builtin_amdgcn_s_sleep(8);
            }
#pragma unroll
            for (int ps = 0; ps < 3; ps++)
                *(LAS u32x4*)(smem + loff[ps]) = u32x4{d[ps].x & ~CKM, d[ps].y & ~CKM, d[ps].z & ~CKM, d[ps].w & ~CKM};
        }
        lds_signal(flags + FL_UIMG, lane);
        STAMP(5);
        if (hw == 2) {
            const u64 want = ((u64)(ep * 0x2545F491u + 0x9E3779B9u) << 32) | (ep * 0xC2B2AE35u + 0x27D4EB2Fu);
            __builtin_amdgcn_s_sleep(32);
            for (u32 spins = 0; spins < SPIN_MAX; spins++) {
                bool ok = true;
#pragma unroll
                for (int k = 0; k < 4; k++) ok &= __hip_atomic_load((GAS u64*)(a.clr + (lane + 64 * k) * 64), RLX_AGENT) == want;
                if (__all(ok)) break;
                __builtin_amdgcn_s_sleep(16);
            }
            if (b == 0 && lane == 0) __hip_atomic_store((GAS u32*)a.epoch, ep + 1u, RLX_AGENT);
            lds_signal(flags + FL_CLR, lane);
        }
    }
    const int ft = tid - 256, f2t = tid - 512;
    if (wave >= 8) {
        if (is_f1) {
            const u16* wm = (f1u >= 48 ? a.wu : a.wg) + (f1u >= 48 ? f1u - 48 : f1u) * 32 + (f2t & 3) * 8;
#pragma unroll
            for (int i = 0; i < 10; i++) wpf[i] = *(const GAS u32x4*)(wm + (size_t)((f2t >> 2) + 64 * i) * F_);
        } else if (is_f2) {
            const u16* wm = a.wd + f2u * 16 + (f2t & 1) * 8;
#pragma unroll
            for (int i = 0; i < 12; i++) wpf[i] = *(const GAS u32x4*)(wm + (size_t)((f2t >> 1) + 128 * i) * D_);
        }
    }
    const u32 tag2 = ep * 0x9E3779B1u + 0x3C6EF372u;
    const int row = chunk * 8 + (wave & 7);
    u32 xp[5], dp[5], wp[5];
    if (wave < 8) {
        const GAS u32* xr = (const GAS u32*)(a.x + (size_t)row * D_);
#pragma unroll
        for (int i = 0; i < 5; i++) { xp[i] = xr[lane + 64 * i]; dp[i] = ((const GAS u32*)a.Dskip)[lane + 64 * i]; wp[i] = ((const GAS u32*)a.wr)[lane + 64 * i]; }
    }
    if ((wave >= 4 && wave < 8) || wave == 0) {
        const int pt = wave == 0 ? 256 + lane : ft;
        lds_wait(flags + FL_CLR, 1);
        lds_wait(flags + FL_CONV, 4);
        __builtin_amdgcn_s_sleep(YDELAY);
        STAMP(8);
        const int yo = ((chunk >> 2) * D_ + 2 * pt) * 64 + (chunk & 3) * 16;
        u32x4 y0, y1;
        for (u32 spins = 0; spins < SPIN_MAX; spins++) {
            y0 = __builtin_amdgcn_raw_buffer_load_b128(rsy, yo, 0, 16);
            y1 = __builtin_amdgcn_raw_buffer_load_b128(rsy, yo + 64, 0, 16);
            bool bad = false;
#pragma unroll
            for (int k = 0; k < 4; k++) {
                bad |= (y0[k] + 0x00010000u) < 0x00010000u || (u16)y0[k] == 0xffffu;
                bad |= (y1[k] + 0x00010000u) < 0x00010000u || (u16)y1[k] == 0xffffu;
            }
            if (!__any(bad)) break;
            __builtin_amdgcn_s_sleep(8);
        }
        LAS char* ys = smem + O_YS;
#pragma unroll
        for (int k = 0; k < 4; k++) {
            *(LAS u32*)(ys + (2 * k) * YS + 4 * pt) = __builtin_amdgcn_perm(y1[k], y0[k], 0x05040100u);
            *(LAS u32*)(ys + (2 * k + 1) * YS + 4 * pt) = __builtin_amdgcn_perm(y1[k], y0[k], 0x07060302u);
        }
    }
    raw_barrier();
    STAMP(9);
    u32 outp[5], key = 0;
    if (wave < 8) {
        const u32 stp = *(LAS const u32*)(smem + O_MISC + 4 * wave);
        const float m1 = bflo(stp), s1 = bfhi(stp), r1 = __builtin_amdgcn_rcpf(s1);
        float h[10];
#pragma unroll
        for (int i = 0; i < 5; i++) {
            u32 yp = *(LAS const u32*)(smem + O_YS + wave * YS + 4 * (lane + 64 * i));
            float xa = bflo(xp[i]), xb = bfhi(xp[i]);
            u32 d = pk_bf16(xa - m1, xb - m1);
            u32 u = pk_bf16(div_r(bflo(d), s1, r1), div_r(bfhi(d), s1, r1));
            u32 sk = pk_bf16(bflo(u) * bflo(dp[i]), bfhi(u) * bfhi(dp[i]));
            u32 aa = pk_bf16(bflo(yp) + bflo(sk), bfhi(yp) + bfhi(sk));
            u32 hh = pk_bf16(xa + bflo(aa), xb + bfhi(aa));
            h[2 * i] = bflo(hh); h[2 * i + 1] = bfhi(hh);
        }
        float m2, s2;
        ln_stats(h, m2, s2);
        const float r2 = __builtin_amdgcn_rcpf(s2);
        float racc = 0.f;
        u32 x2p[5];
#pragma unroll
        for (int i = 0; i < 5; i++) {
            u32 d = pk_bf16(h[2 * i] - m2, h[2 * i + 1] - m2);
            u32 q = pk_bf16(div_r(bflo(d), s2, r2), div_r(bfhi(d), s2, r2));
            x2p[i] = q;
            racc = fmaf(bflo(q), bflo(wp[i]), racc);
            racc = fmaf(bfhi(q), bfhi(wp[i]), racc);
            outp[i] = pk_bf16(h[2 * i] + bflo(q), h[2 * i + 1] + bfhi(q));
        }
        key = sort_key(rbf(wave_sum(racc)), row);
        if (lane == 0) *(LAS u32*)(smem + O_MISC + 32 + 4 * wave) = key;
        raw_barrier();
        u32x4 k0 = *(LAS const u32x4*)(smem + O_MISC + 32), k1 = *(LAS const u32x4*)(smem + O_MISC + 48);
        u32 lbest = max(max(max(k0.x, k0.y), max(k0.z, k0.w)), max(max(k1.x, k1.y), max(k1.z, k1.w)));
        if (key == lbest) {
            auto rc = __builtin_amdgcn_make_buffer_rsrc((void*)(a.cand + (size_t)chunk * CAND_STRIDE), 0, CAND_STRIDE, 0x00020000);
            if (lane == 0) __hip_atomic_store((GAS u64*)(a.keys + chunk * 64), ((u64)tag2 << 32) | key, RLX_AGENT);
#pragma unroll
            for (int i = 0; i < 5; i++) {
                u32x4 c;
                c.x = (x2p[i] & 0xffffu) | (__float_as_uint(h[2 * i]) & 0xffff0000u);
                c.z = (x2p[i] >> 16) | (__float_as_uint(h[2 * i + 1]) & 0xffff0000u);
                c.y = tag2; c.w = tag2;
                __builtin_amdgcn_raw_buffer_store_b128(c, rc, (lane + 64 * i) * 16, 0, 16);
            }
        } else {
#pragma unroll
            for (int i = 0; i < 5; i++) ((GAS u32*)(a.out + (size_t)row * D_))[lane + 64 * i] = outp[i];
            key = 0;
        }
    } else {
        raw_barrier();
        if (wave == 11) {
            __builtin_amdgcn_s_sleep(KDELAY);
            u32 best = 0;
            for (u32 spins = 0; spins < SPIN_MAX; spins++) {
                bool ok = true; u32 mx = 0;
#pragma unroll
                for (int k = 0; k < 4; k++) {
                    u64 v = __hip_atomic_load((GAS u64*)(a.keys + (lane + 64 * k) * 64), RLX_AGENT);
                    ok &= (u32)(v >> 32) == tag2; mx = max(mx, (u32)v);
                }
                best = mx;
                if (__all(ok)) break;
                __builtin_amdgcn_s_sleep(4);
            }
            best = wave_umax(best);
            STAMP(10);
            if (lane == 0) *(LAS u32*)(smem + O_MISC + 64) = best;
            if (is_f1 || is_f2) {
                const int wrow = 2047 - (int)(best & 0xffffu);
                auto rc = __builtin_amdgcn_make_buffer_rsrc((void*)(a.cand + (size_t)(wrow >> 3) * CAND_STRIDE), 0, CAND_STRIDE, 0x00020000);
                u32x4 c[5];
                for (u32 spins = 0; spins < SPIN_MAX; spins++) {
                    bool ok = true;
#pragma unroll
                    for (int i = 0; i < 5; i++) c[i] = __builtin_amdgcn_raw_buffer_load_b128(rc, (lane + 64 * i) * 16, 0, 16);
#pragma unroll
                    for (int i = 0; i < 5; i++) ok &= c[i].y == tag2 && c[i].w == tag2;
                    if (__all(ok)) break;
                    __builtin_amdgcn_s_sleep(2);
                }
#pragma unroll
                for (int i = 0; i < 5; i++) *(LAS u32x2*)(smem + O_XSEL + (lane + 64 * i) * 8) = u32x2{c[i].x, c[i].z};
            }
        }
    }
    raw_barrier();
    STAMP(11);
    const u32 best = *(LAS const u32*)(smem + O_MISC + 64);
    if (wave < 8 && key != 0 && key != best) {
#pragma unroll
        for (int i = 0; i < 5; i++) ((GAS u32*)(a.out + (size_t)row * D_))[lane + 64 * i] = outp[i];
    }
    if (is_f1) {
        if (wave >= 8) {
            float acc[8];
#pragma unroll
            for (int k = 0; k < 8; k++) acc[k] = 0.f;
#pragma unroll
            for (int i = 0; i < 10; i++) {
                float xs = bflo(*(LAS const u32*)(smem + O_XSEL + ((f2t >> 2) + 64 * i) * 4));
#pragma unroll
                for (int k = 0; k < 4; k++) { acc[2 * k] = fmaf(xs, bflo(wpf[i][k]), acc[2 * k]); acc[2 * k + 1] = fmaf(xs, bfhi(wpf[i][k]), acc[2 * k + 1]); }
            }
            const bool b3 = lane & 8, b2_ = lane & 4;
            float w4[4], z2[2];
#pragma unroll
            for (int k = 0; k < 4; k++) { float keep = b3 ? acc[k + 4] : acc[k], give = b3 ? acc[k] : acc[k + 4]; w4[k] = keep + dpp_get<0x128>(give); }
#pragma unroll
            for (int k = 0; k < 2; k++) {
                float keep = b2_ ? w4[k + 2] : w4[k], give = b2_ ? w4[k] : w4[k + 2];
                float up = dpp_get<0x104>(give), dn = dpp_get<0x114>(give);
                z2[k] = keep + (b2_ ? dn : up);
            }
            z2[0] = xrow_sum(z2[0]); z2[1] = xrow_sum(z2[1]);
            if (lane < 16) *(LAS f2*)(smem + O_RED + (wave - 8) * 128 + ((lane & 3) * 8 + (b3 ? 4 : 0) + (b2_ ? 2 : 0)) * 4) = f2{z2[0], z2[1]};
        }
        raw_barrier();
        if (tid < 8) {
            f4 sm = {0, 0, 0, 0};
#pragma unroll
            for (int w = 0; w < 4; w++) sm += *(LAS const f4*)(smem + O_RED + w * 128 + tid * 16);
            auto rg = __builtin_amdgcn_make_buffer_rsrc((void*)a.gu, 0, 96 * 128, 0x00020000);
            __builtin_amdgcn_raw_buffer_store_b128(u32x4{pk_bf16(sm.x, sm.y), tag2, pk_bf16(sm.z, sm.w), tag2}, rg, f1u * 128 + tid * 16, 0, 16);
        }
    } else if (is_f2) {
        if (wave < 6) {
            auto rg = __builtin_amdgcn_make_buffer_rsrc((void*)a.gu, 0, 96 * 128, 0x00020000);
            __builtin_amdgcn_s_sleep(GDELAY);
            u32x4 g, u;
            for (u32 spins = 0; spins < SPIN_MAX; spins++) {
                g = __builtin_amdgcn_raw_buffer_load_b128(rg, tid * 16, 0, 16);
                u = __builtin_amdgcn_raw_buffer_load_b128(rg, (384 + tid) * 16, 0, 16);
                bool ok = g.y == tag2 && g.w == tag2 && u.y == tag2 && u.w == tag2;
                if (__all(ok)) break;
                __builtin_amdgcn_s_sleep(2);
            }
            float gv[4] = {bflo(g.x), bfhi(g.x), bflo(g.z), bfhi(g.z)}, uv[4] = {bflo(u.x), bfhi(u.x), bflo(u.z), bfhi(u.z)};
            f4 av;
#pragma unroll
            for (int k = 0; k < 4; k++) { float sg = rbf(gv[k] / (1.0f + __expf(-gv[k]))); av[k] = rbf(sg * uv[k]); }
            *(LAS f4*)(smem + O_ACT + tid * 16) = av;
        }
        raw_barrier();
        STAMP(13);
        if (wave >= 8) {
            float acc[8];
#pragma unroll
            for (int k = 0; k < 8; k++) acc[k] = 0.f;
#pragma unroll
            for (int i = 0; i < 12; i++) {
                float av = *(LAS const float*)(smem + O_ACT + ((f2t >> 1) + 128 * i) * 4);
#pragma unroll
                for (int k = 0; k < 4; k++) { acc[2 * k] = fmaf(av, bflo(wpf[i][k]), acc[2 * k]); acc[2 * k + 1] = fmaf(av, bfhi(wpf[i][k]), acc[2 * k + 1]); }
            }
            const bool b3 = lane & 8, b2_ = lane & 4, b1 = lane & 2;
            float w4[4], z2[2];
#pragma unroll
            for (int k = 0; k < 4; k++) { float keep = b3 ? acc[k + 4] : acc[k], give = b3 ? acc[k] : acc[k + 4]; w4[k] = keep + dpp_get<0x128>(give); }
#pragma unroll
            for (int k = 0; k < 2; k++) {
                float keep = b2_ ? w4[k + 2] : w4[k], give = b2_ ? w4[k] : w4[k + 2];
                float up = dpp_get<0x104>(give), dn = dpp_get<0x114>(give);
                z2[k] = keep + (b2_ ? dn : up);
            }
            float keep = b1 ? z2[1] : z2[0], give = b1 ? z2[0] : z2[1];
            float r = keep + dpp_get<0x4E>(give);
            r = xrow_sum(r);
            if (lane < 16) *(LAS float*)(smem + O_RED + (wave - 8) * 64 + ((lane & 1) * 8 + (b3 ? 4 : 0) + (b2_ ? 2 : 0) + (b1 ? 1 : 0)) * 4) = r;
        }
        raw_barrier();
        if (tid < 16) {
            float y = 0.f;
#pragma unroll
            for (int w = 0; w < 4; w++) y += *(LAS const float*)(smem + O_RED + w * 64 + tid * 4);
            y = rbf(y);
            const u32 ku = best >> 16;
            const u32 rb = (ku & 0x8000u) ? (ku & 0x7fffu) : (~ku & 0xffffu);
            const float r = __uint_as_float(rb << 16);
            const float rs = rbf(1.0f / (1.0f + __expf(-r)));
            y = rbf(y * rs);
            const u32 cv = *(LAS const u32*)(smem + O_XSEL + (f2u * 16 + tid) * 4);
            const float bb = rbf(bflo(cv) + y);
            const int wrow = 2047 - (int)(best & 0xffffu);
            a.out[(size_t)wrow * D_ + f2u * 16 + tid] = (u16)(pk_bf16(bfhi(cv) + bb, 0.f) & 0xffffu);
        }
    }
    asm volatile(".Lk_end:" ::: "memory");
    STAMP(12);
}

extern "C" void kernel_launch(void* const* d_in, const int* in_sizes, int n_in,
                              void* d_out, int out_size, void* d_ws, size_t ws_size,
                              hipStream_t stream) {
    char* ws = (char*)d_ws;
    MainArgs a;
    a.epoch = (u32*)ws;             ws += 256;
    a.ybuf = ws;                    ws += L_ * D_ * 2;
    a.ubuf = ws;                    ws += L_ * D_ * 2;
    a.xch = ws;                     ws += 256 * XCH_STRIDE;
    a.clr = ws;                     ws += 256 * 64;
    a.keys = ws;                    ws += 256 * 64;
    a.cand = ws;                    ws += 256 * CAND_STRIDE;
    a.gu = ws;                      ws += 96 * 128;
    (void)hipFuncSetAttribute((const void*)k_main, hipFuncAttributeMaxDynamicSharedMemorySize, LDS_MAIN);
    a.x = (const u16*)d_in[0]; a.Are = (const u16*)d_in[1]; a.Aim = (const u16*)d_in[2]; a.Bre = (const u16*)d_in[3]; a.Bim = (const u16*)d_in[4];
    a.Cre = (const u16*)d_in[5]; a.Cim = (const u16*)d_in[6]; a.logdt = (const u16*)d_in[7]; a.Dskip = (const u16*)d_in[8];
    a.wr = (const u16*)d_in[10]; a.wg = (const u16*)d_in[11]; a.wu = (const u16*)d_in[12]; a.wd = (const u16*)d_in[13];
    a.out = (u16*)d_out;
    a.dbg = (u64*)((char*)d_ws + (128u << 20));
    k_main<<<256, NT, LDS_MAIN, stream>>>(a);
}
```

```cpp
#include <hip/hip_runtime.h>
#include <stdint.h>

typedef unsigned short u16;
typedef unsigned int u32;
typedef unsigned long long u64;
typedef _Float16 h8 __attribute__((ext_vector_type(8)));
typedef _Float16 h2 __attribute__((ext_vector_type(2)));
typedef __bf16 b2 __attribute__((ext_vector_type(2)));
typedef float f2 __attribute__((ext_vector_type(2)));
typedef float f4 __attribute__((ext_vector_type(4)));
typedef u32 u32x2 __attribute__((ext_vector_type(2)));
typedef u32 u32x3 __attribute__((ext_vector_type(3)));
typedef u32 u32x4 __attribute__((ext_vector_type(4)));
#define LAS __attribute__((address_space(3)))
#define GAS __attribute__((address_space(1)))
#define RLX_AGENT __ATOMIC_RELAXED, __HIP_MEMORY_SCOPE_AGENT

constexpr int L_ = 2048, D_ = 640, N_ = 192, F_ = 1536;

__device__ __forceinline__ float bflo(u32 p) { return __uint_as_float(p << 16); }
__device__ __forceinline__ float bfhi(u32 p) { return __uint_as_float(p & 0xffff0000u); }
__device__ __forceinline__ float bf2f(u16 v) { return __uint_as_float(((u32)v) << 16); }
__device__ __forceinline__ u32 pk_bf16(float a, float b) {
    f2 v = {a, b};
    b2 r = __builtin_convertvector(v, b2);
    return __builtin_bit_cast(u32, r);
}
__device__ __forceinline__ u32 pk_f16(float a, float b) {
    f2 v = {a, b};
    h2 r = __builtin_convertvector(v, h2);
    return __builtin_bit_cast(u32, r);
}
__device__ __forceinline__ float rbf(float a) { return bflo(pk_bf16(a, a)); }
__device__ __forceinline__ float dot2bf(u32 a, u32 b, float c) {
    return __builtin_amdgcn_fdot2_f32_bf16(__builtin_bit_cast(b2, a), __builtin_bit_cast(b2, b), c, false);
}

template <int CTRL, int RM = 0xf, int BM = 0xf>
__device__ __forceinline__ float dpp_get(float v) {
    return __int_as_float(__builtin_amdgcn_update_dpp(0, __float_as_int(v), CTRL, RM, BM, false));
}
template <int CTRL, int RM = 0xf, int BM = 0xf>
__device__ __forceinline__ float dpp_add(float v) { return v + dpp_get<CTRL, RM, BM>(v); }
__device__ __forceinline__ float rdl(float v, int l) { return __int_as_float(__builtin_amdgcn_readlane(__float_as_int(v), l)); }
__device__ __forceinline__ float wave_sum(float v) {
    v = dpp_add<0xB1>(v);
    v = dpp_add<0x4E>(v);
    v = dpp_add<0x141>(v);
    v = dpp_add<0x140>(v);
    v = dpp_add<0x142, 0xa>(v);
    v = dpp_add<0x143, 0xc>(v);
    return rdl(v, 63);
}
template <int CTRL, int RM = 0xf, int BM = 0xf>
__device__ __forceinline__ u32 dpp_umax(u32 v) {
    u32 t = (u32)__builtin_amdgcn_update_dpp(0, (int)v, CTRL, RM, BM, false);
    return v > t ? v : t;
}
__device__ __forceinline__ u32 wave_umax(u32 v) {
    v = dpp_umax<0xB1>(v);
    v = dpp_umax<0x4E>(v);
    v = dpp_umax<0x141>(v);
    v = dpp_umax<0x140>(v);
    v = dpp_umax<0x142, 0xa>(v);
    v = dpp_umax<0x143, 0xc>(v);
    return (u32)__builtin_amdgcn_readlane((int)v, 63);
}
__device__ __forceinline__ float xrow_sum(float v) {
    u32 u = __float_as_uint(v);
    auto p = __builtin_amdgcn_permlane16_swap(u, u, false, false);
    v = __uint_as_float(p[0]) + __uint_as_float(p[1]);
    u = __float_as_uint(v);
    auto q = __builtin_amdgcn_permlane32_swap(u, u, false, false);
    return __uint_as_float(q[0]) + __uint_as_float(q[1]);
}
__device__ __forceinline__ float reduce16(const float (&v)[16], int lane) {
    const bool b3 = lane & 8, b2_ = lane & 4, b1 = lane & 2, b0 = lane & 1;
    float w[8], z[4], y[2];
#pragma unroll
    for (int k = 0; k < 8; k++) { float keep = b3 ? v[k + 8] : v[k], give = b3 ? v[k] : v[k + 8]; w[k] = keep + dpp_get<0x140>(give); }
#pragma unroll
    for (int k = 0; k < 4; k++) { float keep = b2_ ? w[k + 4] : w[k], give = b2_ ? w[k] : w[k + 4]; z[k] = keep + dpp_get<0x141>(give); }
#pragma unroll
    for (int k = 0; k < 2; k++) { float keep = b1 ? z[k + 2] : z[k], give = b1 ? z[k] : z[k + 2]; y[k] = keep + dpp_get<0x1B>(give); }
    float keep = b0 ? y[1] : y[0], give = b0 ? y[0] : y[1];
    float r = keep + dpp_get<0xB1>(give);
    return xrow_sum(r);
}

__device__ __forceinline__ void ln_stats(const float (&x)[10], float& m, float& s) {
    float sum = 0.f;
#pragma unroll
    for (int i = 0; i < 10; i++) sum += x[i];
    float mean = wave_sum(sum) * (1.0f / D_);
    float ss = 0.f;
#pragma unroll
    for (int i = 0; i < 10; i++) { float d = x[i] - mean; ss = fmaf(d, d, ss); }
    float var = wave_sum(ss) * (1.0f / D_);
    m = rbf(mean);
    s = rbf(__builtin_sqrtf(rbf(rbf(var) + 1e-5f)));
}
__device__ __forceinline__ float div_r(float a, float s, float r) {
    float q = a * r;
    float e = fmaf(-q, s, a);
    return fmaf(e, r, q);
}

struct cf { float r, i; };
__device__ __forceinline__ cf cmul(cf a, cf b) {
    cf o;
    o.r = fmaf(a.r, b.r, -(a.i * b.i));
    o.i = fmaf(a.r, b.i, a.i * b.r);
    return o;
}
__device__ __forceinline__ cf cmulc(cf a, cf b) {
    cf o;
    o.r = fmaf(a.r, b.r, a.i * b.i);
    o.i = fmaf(a.i, b.r, -(a.r * b.i));
    return o;
}
__device__ __forceinline__ cf csq(cf a) {
    cf o;
    o.r = fmaf(a.r, a.r, -(a.i * a.i));
    o.i = 2.0f * a.r * a.i;
    return o;
}
__device__ __forceinline__ cf cexp_dir(float alpha, float bh, float bl, float x) {
    float mag = __builtin_amdgcn_exp2f(alpha * x);
    float ph = __builtin_amdgcn_fractf(bh * x);
    ph = fmaf(bl, x, ph);
    cf o;
    o.r = mag * __builtin_amdgcn_cosf(ph);
    o.i = mag * __builtin_amdgcn_sinf(ph);
    return o;
}
__device__ __forceinline__ f4 mfma16(h8 a, h8 b, f4 c) { return __builtin_amdgcn_mfma_f32_16x16x32_f16(a, b, c, 0, 0, 0); }
__device__ __forceinline__ void wave_fence() {
    __builtin_amdgcn_fence(__ATOMIC_ACQ_REL, "wavefront");
    __builtin_amdgcn_wave_barrier();
}
__device__ __forceinline__ void lds_signal(LAS u32* w, int lane) {
    asm volatile("s_waitcnt lgkmcnt(0)" ::: "memory");
    if (lane == 0) __hip_atomic_fetch_add(w, 1u, __ATOMIC_RELAXED, __HIP_MEMORY_SCOPE_WORKGROUP);
    asm volatile("" ::: "memory");
}
template <int SLP = 0>
__device__ __forceinline__ void lds_wait(LAS u32* w, u32 want) {
    asm volatile("" ::: "memory");
    while (__builtin_amdgcn_readfirstlane(__hip_atomic_load(w, __ATOMIC_RELAXED, __HIP_MEMORY_SCOPE_WORKGROUP)) < want)
        __builtin_amdgcn_s_sleep(SLP);
    asm volatile("" ::: "memory");
}

#define KSTR 8
constexpr int NT = 768;
#define UDELAY 24
#define LAZY 60
#define YDELAY 10
#define KDELAY 12
#define GDELAY 12

constexpr int UIMG_CH = (256 + L_) * 2;
constexpr int O_UIMG = 64;
constexpr int KCS = 4160;
constexpr int KIMG_CH = 4 * KCS;
constexpr int O_KIMG = O_UIMG + 3 * UIMG_CH;
constexpr int SCR = 32 * 272;
constexpr int O_SCR = O_KIMG;
constexpr int O_KTAB = O_SCR + 8 * SCR;
constexpr int KTAB_CH = 656 * 4;
constexpr int O_ACCX = O_KTAB + 3 * KTAB_CH;
constexpr int O_USTG = O_ACCX;
constexpr int URS = 336;
constexpr int O_WBUF = O_ACCX + 2 * 8192;
constexpr int O_FLAG = O_WBUF + 49152;
constexpr int O_MISC = O_FLAG + 64;
constexpr int LDS_MAIN = O_MISC + 128;
constexpr int YS = 1280 + 16;
constexpr int O_YS = O_KIMG;
constexpr int O_XSEL = O_YS + 8 * YS;
constexpr int O_XSB = O_XSEL + 2560;
constexpr int O_ACT = O_XSB + 1280;
constexpr int O_RED = O_ACT + 3072;
static_assert(8 * SCR >= 3 * KIMG_CH && LDS_MAIN <= 163840, "lds");
static_assert(O_KIMG % 16 == 0 && O_KTAB % 16 == 0 && O_ACCX % 16 == 0 && O_FLAG % 16 == 0 && O_WBUF % 16 == 0 && O_RED % 16 == 0, "align");
enum { FL_P0 = 0, FL_P1, FL_P2, FL_TAB, FL_KIMG, FL_UIMG, FL_ACC0, FL_ACC1, FL_STAB, FL_SIMG, FL_USTG, FL_XRD, FL_CLR, FL_CONV };

struct PosTab { u16 p[512][8]; };
constexpr int rne_lag(int j) {
    if (j < 256) return j;
    int sh = j < 512 ? 1 : (j < 1024 ? 2 : 3);
    int q = j >> sh, rem = j & ((1 << sh) - 1), half = 1 << (sh - 1);
    if (rem > half || (rem == half && (q & 1))) q++;
    return q << sh;
}
constexpr int ktab_pos_c(int j) {
    if (j < 0 || j >= 2048) return 644;
    int l = rne_lag(j);
    if (l < 256) return l;
    if (l < 512) return 256 + ((l - 256) >> 1);
    if (l < 1024) return 384 + ((l - 512) >> 2);
    return 512 + ((l - 1024) >> 3);
}
constexpr PosTab make_postab() {
    PosTab t{};
    for (int c = 0; c < 512; c++) for (int i = 0; i < 8; i++) t.p[c][i] = (u16)(4 * ktab_pos_c(2047 - 4 * c - i));
    return t;
}
__device__ const __attribute__((aligned(16))) PosTab g_postab = make_postab();

#define STAMP(i)

constexpr int XCH_STRIDE = 5376;
constexpr int CAND_STRIDE = 5120;
struct MainArgs {
    const u16 *x, *Are, *Aim, *Bre, *Bim, *Cre, *Cim, *logdt, *Dskip, *wr, *wg, *wu, *wd;
    u16* out;
    u32* epoch;
    char* xch;
    char* ubuf;
    char* ybuf;
    char* clr;
    char* keys;
    char* cand;
    char* gu;
    u64* dbg;
};
constexpr u32 SPIN_MAX = 1u << 16;

typedef float f16v __attribute__((ext_vector_type(16)));
typedef short s4 __attribute__((ext_vector_type(4)));
typedef __bf16 b8 __attribute__((ext_vector_type(8)));
typedef __amdgpu_buffer_rsrc_t rsrc_t;
__device__ __forceinline__ f2 rot(f2 b) { return f2{-b.y, b.x}; }
__device__ __forceinline__ f2 cm(f2 a, f2 b, f2 rb) { return __builtin_elementwise_fma(a.yy, rb, a.xx * b); }
__device__ __forceinline__ f2 csq2(f2 a) { return f2{fmaf(a.x, a.x, -(a.y * a.y)), 2.0f * a.x * a.y}; }
template <bool CONJ>
__device__ __forceinline__ f2 cexp2(float alpha, float bh, float bl, float x) {
    float mag = __builtin_amdgcn_exp2f(alpha * x);
    float ph = __builtin_amdgcn_fractf(bh * x);
    ph = fmaf(bl, x, ph);
    f2 cs = {__builtin_amdgcn_cosf(ph), __builtin_amdgcn_sinf(CONJ ? -ph : ph)};
    return cs * mag;
}
__device__ __forceinline__ void put(LAS char* sp, int row, f2 v) { *(LAS u32*)(sp + row * 272) = pk_f16(v.x, v.y); }

__device__ __forceinline__ h8 ld_b(LAS const char* ub, int t) { return *(LAS const h8*)(ub + 512 * t); }
__device__ __forceinline__ h8 ld_a(LAS const char* kl, LAS const char* kh, int M) {
    u32x2 lo = *(LAS const u32x2*)(kl + 512 * (7 - M));
    u32x2 hi = *(LAS const u32x2*)(kh + 512 * (7 - M));
    u32x4 a4 = {lo.x, lo.y, hi.x, hi.y};
    return __builtin_bit_cast(h8, a4);
}
#define SB() __builtin_amdgcn_sched_barrier(0)
__device__ __forceinline__ void conv_run(f4 (&acc)[8], LAS const char* ub, LAS const char* kl, LAS const char* kh, int nmu) {
    h8 A[8], B[8], Bn[3];
#pragma unroll
    for (int i = 0; i < 8; i++) { A[i] = ld_a(kl, kh, i); if (i < 5) B[7 - i] = ld_b(ub, 7 - i); if (i < 3) B[i] = ld_b(ub, i); SB(); }
#pragma nounroll
    for (int it = 0; it < nmu; it++) {
        ub -= 64; kl -= 64; kh -= 64;
#pragma unroll
        for (int M = 0; M < 8; M++) {
#pragma unroll
            for (int t = 7 - M; t >= 0; t--) acc[t + M] = mfma16(A[M], B[t], acc[t + M]);
            SB();
            A[M] = ld_a(kl, kh, M);
            if (M < 5) B[7 - M] = ld_b(ub, 7 - M);
            if (M < 3) Bn[M] = ld_b(ub, M);
            if (M >= 5) { B[7 - M] = Bn[7 - M]; asm volatile("" : "+v"(B[7 - M])); }
            SB();
        }
    }
}
template <int TA, int TB>
__device__ __forceinline__ void conv_pair(f4& a0, f4& a1, LAS const char* ub, LAS const char* kl, LAS const char* kh) {
    h8 A[TB + 1], B[TB + 1];
    f4 a2 = {0, 0, 0, 0};
#pragma unroll
    for (int i = 0; i <= TB; i++) { A[i] = ld_a(kl, kh, i); B[TB - i] = ld_b(ub, TB - i); SB(); }
#pragma nounroll
    for (int it = 0; it < 8; it++) {
        ub -= 64; kl -= 64; kh -= 64;
#pragma unroll
        for (int M = 0; M <= TB; M++) {
            if (M & 1) a2 = mfma16(A[M], B[TB - M], a2); else a1 = mfma16(A[M], B[TB - M], a1);
            if (M <= TA) a0 = mfma16(A[M], B[TA - M], a0);
            SB();
            A[M] = ld_a(kl, kh, M); B[TB - M] = ld_b(ub, TB - M);
            SB();
        }
    }
    a1 += a2;
}

__device__ __forceinline__ void store_y_tile(rsrc_t rsy, int ch, int T, int lane, f4 a, float sc) {
    u32x2 o;
    o.x = pk_bf16(a.x * sc, a.y * sc);
    o.y = pk_bf16(a.z * sc, a.w * sc);
    const int n = lane & 15, g = lane >> 4;
    __builtin_amdgcn_raw_buffer_store_b64(o, rsy, (((n >> 1) * D_ + ch) * 64 + (n & 1) * 32 + g * 8) + T * (8 * D_ * 64), 0, 16);
}
__device__ __forceinline__ void raw_barrier() { asm volatile("s_waitcnt lgkmcnt(0)\n\ts_barrier" ::: "memory"); }
__device__ __forceinline__ u32 sort_key(float r, int row) {
    u32 u = __float_as_uint(r) >> 16;
    u = (u & 0x8000u) ? (~u & 0xffffu) : (u | 0x8000u);
    return (u << 16) | (u32)(2047 - row);
}
__device__ __forceinline__ void build_chunk(LAS char* kimg, LAS const char* tab, u32x4 pp, int c) {
    float v[7];
#pragma unroll
    for (int i = 0; i < 7; i++) v[i] = *(LAS const float*)(tab + ((i & 1) ? (pp[i >> 1] >> 16) : (pp[i >> 1] & 0xffffu)));
    u32 p01 = pk_f16(v[0], v[1]), p23 = pk_f16(v[2], v[3]), p45 = pk_f16(v[4], v[5]);
    u32 p12 = pk_f16(v[1], v[2]), p34 = pk_f16(v[3], v[4]), p56 = pk_f16(v[5], v[6]);
    *(LAS u32x2*)(kimg + 0 * KCS + 32 + 8 * c) = u32x2{p01, p23};
    *(LAS u32x2*)(kimg + 1 * KCS + 32 + 8 * c) = u32x2{p12, p34};
    *(LAS u32x2*)(kimg + 2 * KCS + 32 + 8 * c) = u32x2{p23, p45};
    *(LAS u32x2*)(kimg + 3 * KCS + 32 + 8 * c) = u32x2{p34, p56};
}
constexpr u32 CKM = 0x00070007u;
__device__ __forceinline__ u32x4 ck_expect(u32x4 d, u32 e) {
    u32 m0 = d.x & ~CKM, m1 = d.y & ~CKM, m2 = d.z & ~CKM, m3 = d.w & ~CKM;
    u32 s = (m0 + m1) + (m2 + m3) + e;
    s ^= s >> 9;
    u32x4 o;
    o.x = m0 | (s & CKM);
    o.y = m1 | (__builtin_amdgcn_alignbit(s, s, 3) & CKM);
    o.z = m2 | (__builtin_amdgcn_alignbit(s, s, 6) & CKM);
    o.w = m3 | (__builtin_amdgcn_alignbit(s, s, 9) & CKM);
    return o;
}
__device__ __forceinline__ float row16_sum(float v) {
    v = dpp_add<0xB1>(v); v = dpp_add<0x4E>(v); v = dpp_add<0x141>(v); v = dpp_add<0x140>(v);
    return v;
}
__device__ __forceinline__ u32 ln_pair(u32 x, float m, float s, float r) {
    u32 d = pk_bf16(bflo(x) - m, bfhi(x) - m);
    return pk_bf16(div_r(bflo(d), s, r), div_r(bfhi(d), s, r));
}
__device__ __forceinline__ u32x4 norm8(u32x4 x, float m, float s, float r) {
    u32x4 o;
#pragma unroll
    for (int k = 0; k < 4; k++) { u32 u = ln_pair(x[k], m, s, r); o[k] = pk_f16(bflo(u), bfhi(u)); }
    return o;
}
__device__ __forceinline__ void glds16(const void* gsrc, u32 lds_dst) {
    u32 keep;
    asm volatile("s_mov_b32 %0, m0\n\ts_mov_b32 m0, %2\n\ts_nop 0\n\tglobal_load_lds_dwordx4 %1, off\n\ts_mov_b32 m0, %0" : "=&s"(keep) : "v"(gsrc), "s"(lds_dst) : "memory");
}
__device__ __forceinline__ b8 tr_frag(LAS const char* p, int hi_off) {
    s4 lo = __builtin_amdgcn_ds_read_tr16_b64_v4i16((LAS s4*)p);
    s4 hi = __builtin_amdgcn_ds_read_tr16_b64_v4i16((LAS s4*)(p + hi_off));
    u32x2 l2 = __builtin_bit_cast(u32x2, lo), h2_ = __builtin_bit_cast(u32x2, hi);
    return __builtin_bit_cast(b8, u32x4{l2.x, l2.y, h2_.x, h2_.y});
}
__device__ __forceinline__ f4 mfma16b(b8 a, b8 b, f4 c) { return __builtin_amdgcn_mfma_f32_16x16x32_bf16(a, b, c, 0, 0, 0); }

__global__ void __launch_bounds__(NT, 3) k_main(MainArgs a) {
    extern __shared__ __attribute__((aligned(16))) char smem_[];
    LAS char* smem = (LAS char*)smem_;
    const int tid = threadIdx.x, lane = tid & 63;
    u32 warm;
    {
        u64 pc; u32 csz;
        asm volatile("s_getpc_b64 %0\n.Lk_begin:\n\ts_mov_b32 %1, .Lk_end-.Lk_begin" : "=s"(pc), "=s"(csz));
        if (csz > 49152u || csz < 4096u) csz = 128u;
        warm = *(const GAS u32*)((pc & ~127ull) + min((u32)(tid * 64), csz - 128u));
    }
    const int wave = __builtin_amdgcn_readfirstlane(tid >> 6);
    const int b = blockIdx.x;
    const int pairi = (b & 7) * 16 + (b >> 4), side = (b >> 3) & 1;
    const int cbase = 5 * pairi + 2 * side;
    const int ch0 = side ? cbase + 1 : cbase, ch1 = ch0 + 1, chs = 5 * pairi + 2;
    const int chunk = (b & 7) * 32 + (b >> 3);
    LAS u32* flags = (LAS u32*)(smem + O_FLAG);
    STAMP(0);
    const u32 ep = __hip_atomic_load((GAS u32*)a.epoch, RLX_AGENT);
    const rsrc_t rsy = __builtin_amdgcn_make_buffer_rsrc((void*)a.ybuf, 0, 64 * D_ * 64, 0x00020000);
    const int jl = b >> 3;
    const bool is_f1 = jl < 12, is_f2 = jl >= 12 && jl < 17;
    const int f1u = (b & 7) * 12 + jl;
    const int f2u = (b & 7) * 5 + (jl - 12);
    const int n16 = lane & 15, g4 = lane >> 4;

    if (wave < 8) {
        const int slot_g = wave < 3 ? 0 : (wave < 6 ? 1 : 2);
        const int ch_g = slot_g == 0 ? ch0 : (slot_g == 1 ? ch1 : chs);
        int mode; bool act = true;
        if (slot_g < 2) mode = (wave - 3 * slot_g) * 64 + lane;
        else { mode = 96 * side + (wave - 6) * 64 + lane; act = (wave == 6) || (lane < 32); if (!act) mode = 0; }
        const int gi = ch_g * N_ + mode;
        u16 pdt = a.logdt[ch_g];
        u16 pAre = a.Are[gi], pAim = a.Aim[gi], pBre = a.Bre[gi], pBim = a.Bim[gi], pCre = a.Cre[gi], pCim = a.Cim[gi];
        u32x4 pp = *(const GAS u32x4*)(&g_postab.p[tid][0]);
        if (tid < 16) flags[tid] = 0;
        if (tid >= 64 && tid < 160) { int t = tid - 64; *(LAS u32x4*)(smem + O_UIMG + (t >> 5) * UIMG_CH + (t & 31) * 16) = u32x4{0, 0, 0, 0}; }
        raw_barrier();
        const int wn = is_f1 ? 40 : (is_f2 ? 48 : 0);
        const int wjs = is_f1 ? 16 * F_ * 2 : 32 * D_ * 2;
        const char* wsrc = is_f1 ? (const char*)((f1u >= 48 ? a.wu : a.wg) + (f1u >= 48 ? f1u - 48 : f1u) * 32 + (lane >> 2) * F_ + (lane & 3) * 8)
                                 : (const char*)(a.wd + f2u * 16 + (lane >> 1) * D_ + (lane & 1) * 8);
        wsrc += wave * wjs;
#define WCOND
#define WLOAD(i) do { if (WCOND wave + 8 * (i) < wn) glds16(wsrc + (size_t)(8 * (i)) * wjs, (u32)(O_WBUF + (wave + 8 * (i)) * 1024)); } while (0)
        float Are = bf2f(pAre), Aim = bf2f(pAim), Bre = bf2f(pBre), Bim = bf2f(pBim), Cre = bf2f(pCre), Cim = bf2f(pCim);
        float dt = rbf(expf(bf2f(pdt)));
        float are = dt * Are, aim = dt * Aim, br = dt * Bre, bi = dt * Bim;
        f2 wc = {fmaf(Cre, br, -(Cim * bi)), -fmaf(Cre, bi, Cim * br)};
        if (!act) wc = f2{0.f, 0.f};
        STAMP(1);
        WLOAD(0);
        float alpha = are * 1.44269504088896341f;
        const float CH = 0.15915494f, CL = 6.4206382e-09f;
        float p_ = aim * CH;
        float pe = fmaf(aim, CL, fmaf(aim, CH, -p_));
        float pf = p_ - __builtin_rintf(p_);
        float bh = __uint_as_float(__float_as_uint(pf + pe) & 0xfffff800u);
        float bl = (pf - bh) + pe;
        float mw = fabsf(wc.x) + fabsf(wc.y);
        float v0r = 16384.f;
        if (__builtin_expect(__any(alpha > 0.f), 0)) {
            float ap = fmaxf(alpha, 0.f);
            float g1 = __builtin_amdgcn_exp2f(ap * 1024.f);
            wc = (wc * g1) * g1;
            mw = (fabsf(wc.x) + fabsf(wc.y)) * __builtin_amdgcn_exp2f(ap * 248.f);
            v0r = __builtin_amdgcn_exp2f(fmaf(ap, -2048.f, 14.f));
        }
        u32 mwu = wave_umax(__float_as_uint(mw));
        int e = (int)(mwu >> 23) - 126;
        if (e < -100) e = -100;
        if (e > 127) e = 127;
        LAS char* scr = smem + O_SCR + wave * SCR;
        LAS char* sp = scr + lane * 4;
        LAS const char* fp = scr + (lane & 31) * 272 + (lane >> 5) * 16;
        WLOAD(1);
        {
            f2 E8 = cexp2<true>(alpha, bh, bl, 8.f);
            f2 E16 = csq2(E8), E32 = csq2(E16);
            f2 r8 = rot(E8), r32 = rot(E32);
            f2 Y0 = wc * __builtin_amdgcn_ldexpf(1.0f, 14 - e);
            f2 Y2 = cm(Y0, E16, rot(E16));
            f2 Y1 = cm(Y0, E8, r8), Y3 = cm(Y2, E8, r8);
#pragma unroll
            for (int q = 0; q < 32; q += 4) {
                put(sp, q, Y0); put(sp, q + 1, Y1); put(sp, q + 2, Y2); put(sp, q + 3, Y3);
                if (q < 28) { Y0 = cm(Y0, E32, r32); Y1 = cm(Y1, E32, r32); Y2 = cm(Y2, E32, r32); Y3 = cm(Y3, E32, r32); }
                if (q == 12) WLOAD(2);
            }
        }
        wave_fence();
        h8 By[8];
#pragma unroll
        for (int k = 0; k < 8; k++) By[k] = *(LAS const h8*)(fp + 32 * k);
        wave_fence();
        WLOAD(3);
        {
            f2 E1 = cexp2<false>(alpha, bh, bl, 1.f), E256 = cexp2<false>(alpha, bh, bl, 256.f), E1024 = cexp2<false>(alpha, bh, bl, 1024.f);
            f2 E2 = csq2(E1), E4 = csq2(E2), E512 = csq2(E256), E2048 = csq2(E1024);
            f2 r1 = rot(E1), r256 = rot(E256);
            f2 E768 = cm(E512, E256, r256);
            f2 V2 = E2 * v0r, V4 = E4 * v0r;
            f2 rV4 = rot(V4);
            f2 V6 = cm(E2, V4, rV4);
            put(sp, 0, f2{v0r, 0.f}); put(sp, 1, E1 * v0r); put(sp, 2, V2); put(sp, 3, cm(V2, E1, r1));
            put(sp, 4, V4); put(sp, 5, cm(V4, E1, r1)); put(sp, 6, V6); put(sp, 7, cm(V6, E1, r1));
            WLOAD(4);
            put(sp, 8, E256 * v0r); put(sp, 9, cm(V2, E256, r256)); put(sp, 10, cm(V4, E256, r256)); put(sp, 11, cm(V6, E256, r256));
            put(sp, 12, E512 * v0r); put(sp, 13, cm(E512, V4, rV4)); put(sp, 14, E768 * v0r); put(sp, 15, cm(E768, V4, rV4));
            f2 H0 = E1024 * v0r;
            f2 H1 = cm(H0, E256, r256), H2 = cm(H1, E256, r256), H3 = cm(H2, E256, r256);
            put(sp, 16, H0); put(sp, 17, H1); put(sp, 18, H2); put(sp, 19, H3); put(sp, 20, E2048 * v0r);
        }
        WLOAD(5);
        wave_fence();
        f16v dd = {0, 0, 0, 0, 0, 0, 0, 0, 0, 0, 0, 0, 0, 0, 0, 0};
#pragma unroll
        for (int k = 0; k < 8; k++) dd = __builtin_amdgcn_mfma_f32_32x32x16_f16(*(LAS const h8*)(fp + 32 * k), By[k], dd, 0, 0, 0);
        wave_fence();
        {
            const float ts = __builtin_amdgcn_ldexpf(1.0f, e - 28);
            const int q = lane & 31, h = lane >> 5;
            *(LAS f4*)(scr + (8 * q + 4 * h) * 4) = f4{dd[0], dd[1], dd[2], dd[3]} * ts;
            if (h == 0) {
                *(LAS f4*)(scr + (256 + 4 * q) * 4) = f4{dd[4], dd[5], dd[6], dd[7]} * ts;
                *(LAS float*)(scr + (512 + q) * 4) = dd[8] * ts;
                *(LAS float*)(scr + (544 + q) * 4) = dd[9] * ts;
                *(LAS float*)(scr + (576 + q) * 4) = dd[10] * ts;
                *(LAS float*)(scr + (608 + q) * 4) = dd[11] * ts;
            } else {
                *(LAS f2*)(scr + (384 + 2 * q) * 4) = f2{dd[4], dd[5]} * ts;
                *(LAS f2*)(scr + (448 + 2 * q) * 4) = f2{dd[6], dd[7]} * ts;
                if (q == 0) {
                    *(LAS f4*)(scr + 640 * 4) = f4{dd[8] * ts, 0.f, 0.f, 0.f};
                    *(LAS u32x2*)(scr + 644 * 4) = u32x2{0u, (u32)e};
                }
            }
        }
        STAMP(2);
        lds_signal(flags + FL_P0 + slot_g, lane);
        const u32 tag = ep * 0x9E3779B1u + 0x7F4A7C15u;
        asm volatile("" :: "v"(tag), "v"(warm));
        lds_wait(flags + FL_P0 + slot_g, slot_g < 2 ? 3 : 2);
        STAMP(3);
        {
            LAS const char* p0 = smem + O_SCR + (3 * slot_g) * SCR;
            LAS char* tab = smem + O_KTAB + slot_g * KTAB_CH;
            int em = max(*(LAS const int*)(p0 + 645 * 4), *(LAS const int*)(p0 + SCR + 645 * 4));
            if (slot_g < 2) {
                const int idx = tid - slot_g * 192;
                em = max(em, *(LAS const int*)(p0 + 2 * SCR + 645 * 4));
                const int ic = idx < 161 ? idx : 161;
                f4 sm = *(LAS const f4*)(p0 + ic * 16) + *(LAS const f4*)(p0 + SCR + ic * 16) + *(LAS const f4*)(p0 + 2 * SCR + ic * 16);
                sm *= __builtin_amdgcn_ldexpf(1.0f, 7 - em);
                if (idx >= 161) sm = f4{0.f, __builtin_amdgcn_ldexpf(1.0f, em - 7), 0.f, 0.f};
                *(LAS f4*)(tab + ic * 16) = sm;
            } else {
                auto rs = __builtin_amdgcn_make_buffer_rsrc((void*)(a.xch + (size_t)b * XCH_STRIDE), 0, XCH_STRIDE, 0x00020000);
                const int i0 = tid - 384, i1 = i0 < 33 ? i0 + 128 : 160;
                f4 s0 = *(LAS const f4*)(p0 + i0 * 16) + *(LAS const f4*)(p0 + SCR + i0 * 16);
                f4 s1 = *(LAS const f4*)(p0 + i1 * 16) + *(LAS const f4*)(p0 + SCR + i1 * 16);
                if (i1 == 160) s1.y = __int_as_float(em);
                *(LAS f4*)(tab + i0 * 16) = s0;
                if (wave == 6) *(LAS f4*)(tab + i1 * 16) = s1;
                lds_signal(flags + FL_TAB, lane);
                __builtin_amdgcn_raw_buffer_store_b128(u32x4{__float_as_uint(s0.x), tag, __float_as_uint(s0.y), tag}, rs, i0 * 16, 0, 16);
                __builtin_amdgcn_raw_buffer_store_b128(u32x4{__float_as_uint(s0.z), tag, __float_as_uint(s0.w), tag}, rs, (162 + i0) * 16, 0, 16);
                if (wave == 6) {
                    __builtin_amdgcn_raw_buffer_store_b128(u32x4{__float_as_uint(s1.x), tag, __float_as_uint(s1.y), tag}, rs, i1 * 16, 0, 16);
                    __builtin_amdgcn_raw_buffer_store_b128(u32x4{__float_as_uint(s1.z), tag, __float_as_uint(s1.w), tag}, rs, (162 + i1) * 16, 0, 16);
                }
            }
            if (slot_g < 2) lds_signal(flags + FL_TAB, lane);
        }
        lds_wait(flags + FL_TAB, 8);
        STAMP(4);
        build_chunk(smem + O_KIMG, smem + O_KTAB, pp, tid);
        build_chunk(smem + O_KIMG + KIMG_CH, smem + O_KTAB + KTAB_CH, pp, tid);
        if (tid < 24) *(LAS u32x4*)(smem + O_KIMG + (tid >> 1) * KCS + 4128 + (tid & 1) * 16) = u32x4{0, 0, 0, 0};
        lds_signal(flags + FL_KIMG, lane);
        STAMP(5);

        if (wave < 4) {
            const int slot = wave >> 1;
            const int s = (3 - n16) & 3;
            const int uoff = 2 * (256 + 16 * n16 - 16 + 8 * g4);
            const int koff = s * KCS + 2 * (2047 - n16 + 8 * g4 - s) - 512 * 7;
            const int mu0 = (wave & 1) ? 5 : 0;
            LAS const char* ub = smem + O_UIMG + slot * UIMG_CH + uoff - 64 * mu0;
            LAS const char* kl = smem + O_KIMG + slot * KIMG_CH + koff - 64 * mu0;
            LAS const char* kh = kl + 8;
            asm volatile("" : "+v"(kh));
            f4 acc[8];
#pragma unroll
            for (int t = 0; t < 8; t++) acc[t] = f4{0, 0, 0, 0};
            LAS char* ax = smem + O_ACCX + slot * 8192 + lane * 16;
            lds_wait<1>(flags + FL_KIMG, 8);
            lds_wait<1>(flags + FL_UIMG, 4);
            __builtin_amdgcn_s_setprio(3);
            STAMP(6);
            conv_run(acc, ub, kl, kh, (wave & 1) ? 3 : 5);
            STAMP(7);
            if ((wave & 1) == 0) {
                lds_wait(flags + FL_ACC0 + slot, 1);
                const float sc = *(LAS const float*)(smem + O_KTAB + slot * KTAB_CH + 645 * 4);
#pragma unroll
                for (int t = 0; t < 8; t++) {
                    f4 o = acc[t] + *(LAS const f4*)(ax + 1024 * t);
                    store_y_tile(rsy, slot ? ch1 : ch0, t, lane, o, sc);
                }
            } else {
#pragma unroll
                for (int t = 0; t < 8; t++) *(LAS f4*)(ax + 1024 * t) = acc[t];
                lds_signal(flags + FL_ACC0 + slot, lane);
                lds_wait(flags + FL_SIMG, 4);
                LAS const char* ub2 = smem + O_UIMG + 2 * UIMG_CH + uoff;
                LAS const char* kl2 = smem + O_KIMG + 2 * KIMG_CH + koff;
                LAS const char* kh2 = kl2 + 8;
                asm volatile("" : "+v"(kh2));
                float sc = *(LAS const float*)(smem + O_KTAB + 2 * KTAB_CH + 645 * 4);
                f4 a0 = {0, 0, 0, 0}, a1 = a0;
                const int sel = side * 2 + slot;
                int ta, tb;
                if (sel == 0) { conv_pair<0, 7>(a0, a1, ub2, kl2, kh2); ta = 0; tb = 7; }
                else if (sel == 1) { conv_pair<3, 4>(a0, a1, ub2, kl2, kh2); ta = 3; tb = 4; }
                else if (sel == 2) { conv_pair<1, 6>(a0, a1, ub2, kl2, kh2); ta = 1; tb = 6; }
                else { conv_pair<2, 5>(a0, a1, ub2, kl2, kh2); ta = 2; tb = 5; }
                store_y_tile(rsy, chs, ta, lane, a0, sc); store_y_tile(rsy, chs, tb, lane, a1, sc);
            }
            __builtin_amdgcn_s_setprio(0);
            lds_signal(flags + FL_CONV, lane);
        } else {
            const int ht = tid - 256;
            LAS char* tab = smem + O_KTAB + 2 * KTAB_CH;
            u32x4 pp2 = *(const GAS u32x4*)(&g_postab.p[ht][0]);
            if (ht < 192) {
                auto rs = __builtin_amdgcn_make_buffer_rsrc((void*)(a.xch + (size_t)(b ^ 8) * XCH_STRIDE), 0, XCH_STRIDE, 0x00020000);
                const int hi = ht < 161 ? ht : 160;
                u32x4 v0, v1, v2;
                bool need = true;
                for (u32 spins = 0; spins < SPIN_MAX; spins++) {
                    if (need) {
                        v0 = __builtin_amdgcn_raw_buffer_load_b128(rs, hi * 16, 0, 16);
                        v1 = __builtin_amdgcn_raw_buffer_load_b128(rs, (162 + hi) * 16, 0, 16);
                        v2 = __builtin_amdgcn_raw_buffer_load_b128(rs, 160 * 16, 0, 16);
                        need = !(v0.y == tag && v0.w == tag && v1.y == tag && v1.w == tag && v2.y == tag && v2.w == tag);
                    }
                    if (!__any(need)) break;
                    __builtin_amdgcn_s_sleep(2);
                }
                int em = max(*(LAS const int*)(tab + 641 * 4), (int)v2.z);
                f4 loc = *(LAS const f4*)(tab + hi * 16);
                lds_signal(flags + FL_XRD, lane);
                lds_wait(flags + FL_XRD, 3);
                f4 rem = {__uint_as_float(v0.x), __uint_as_float(v0.z), __uint_as_float(v1.x), __uint_as_float(v1.z)};
                if (ht < 161) *(LAS f4*)(tab + ht * 16) = (loc + rem) * __builtin_amdgcn_ldexpf(1.0f, 7 - em);
                else if (ht == 161) *(LAS f2*)(tab + 644 * 4) = f2{0.f, __builtin_amdgcn_ldexpf(1.0f, em - 7)};
                lds_signal(flags + FL_STAB, lane);
            }
            STAMP(6);
            lds_wait<2>(flags + FL_STAB, 3);
            build_chunk(smem + O_KIMG + 2 * KIMG_CH, tab, pp, tid);
            build_chunk(smem + O_KIMG + 2 * KIMG_CH, tab, pp2, ht);
            lds_signal(flags + FL_SIMG, lane);
            STAMP(7);
        }
    } else {
        const int hw = wave - 8, ht = tid - 512;
        const int blk = chunk >> 2, cq = chunk & 3;
        const GAS u32x4* xb = (const GAS u32x4*)a.x + (size_t)(blk * 32 + 8 * hw) * 80;
        u32x4 xq[2][5];
#pragma unroll
        for (int i = 0; i < 5; i++) {
            int c = 20 * cq + n16 + 16 * i; if (c >= 80) c -= 80;
            xq[0][i] = xb[g4 * 80 + c]; xq[1][i] = xb[(4 + g4) * 80 + c];
        }
        const int er = (lane >> 2) & 7;
        u32x4 xe = xb[er * 80 + 20 * cq + 16 + (lane & 3)];
        __builtin_amdgcn_s_barrier();
        __builtin_amdgcn_s_setprio(2);
        float mB[2], sB[2], rB[2];
        {
            float s1[2], s2[2];
#pragma unroll
            for (int g2 = 0; g2 < 2; g2++) {
                float a1 = 0.f, a2 = 0.f, b1 = 0.f, b2v = 0.f;
#pragma unroll
                for (int i = 0; i < 5; i++) {
                    a1 = dot2bf(xq[g2][i].x, 0x3f803f80u, a1); a2 = dot2bf(xq[g2][i].x, xq[g2][i].x, a2);
                    b1 = dot2bf(xq[g2][i].y, 0x3f803f80u, b1); b2v = dot2bf(xq[g2][i].y, xq[g2][i].y, b2v);
                    a1 = dot2bf(xq[g2][i].z, 0x3f803f80u, a1); a2 = dot2bf(xq[g2][i].z, xq[g2][i].z, a2);
                    b1 = dot2bf(xq[g2][i].w, 0x3f803f80u, b1); b2v = dot2bf(xq[g2][i].w, xq[g2][i].w, b2v);
                }
                s1[g2] = row16_sum(a1 + b1); s2[g2] = row16_sum(a2 + b2v);
            }
            float mean[2], var[2];
#pragma unroll
            for (int g2 = 0; g2 < 2; g2++) { mean[g2] = s1[g2] * (1.0f / D_); var[g2] = fmaf(-mean[g2], mean[g2], s2[g2] * (1.0f / D_)); }
            if (__builtin_expect(__any(mean[0] * mean[0] > 16.f * var[0] || mean[1] * mean[1] > 16.f * var[1]), 0)) {
#pragma unroll
                for (int g2 = 0; g2 < 2; g2++) {
                    float ss = 0.f;
#pragma unroll
                    for (int i = 0; i < 5; i++)
#pragma unroll
                        for (int k = 0; k < 4; k++) {
                            float d0 = bflo(xq[g2][i][k]) - mean[g2], d1 = bfhi(xq[g2][i][k]) - mean[g2];
                            ss = fmaf(d0, d0, ss); ss = fmaf(d1, d1, ss);
                        }
                    var[g2] = row16_sum(ss) * (1.0f / D_);
                }
            }
#pragma unroll
            for (int g2 = 0; g2 < 2; g2++) {
                mB[g2] = rbf(mean[g2]);
                sB[g2] = rbf(__builtin_sqrtf(rbf(rbf(var[g2]) + 1e-5f)));
                rB[g2] = __builtin_amdgcn_rcpf(sB[g2]);
            }
        }
        STAMP(1);
        LAS char* stg = smem + O_USTG;
        {
            const u32 ms0 = (__float_as_uint(mB[0]) >> 16) | __float_as_uint(sB[0]), ms1 = (__float_as_uint(mB[1]) >> 16) | __float_as_uint(sB[1]);
            if (hw == cq && n16 == 0) { *(LAS u32*)(smem + O_MISC + 4 * g4) = ms0; *(LAS u32*)(smem + O_MISC + 16 + 4 * g4) = ms1; }
            const int src = (er & 3) * 64;
            u32 e0 = (u32)__builtin_amdgcn_ds_bpermute(src, (int)ms0), e1 = (u32)__builtin_amdgcn_ds_bpermute(src, (int)ms1);
            *(LAS u32x4*)(stg + (8 * hw + g4) * URS + n16 * 16) = norm8(xq[0][0], mB[0], sB[0], rB[0]);
            *(LAS u32x4*)(stg + (8 * hw + 4 + g4) * URS + n16 * 16) = norm8(xq[1][0], mB[1], sB[1], rB[1]);
            const u32 em = (er & 4) ? e1 : e0;
            const float se = bfhi(em);
            u32x4 ne = norm8(xe, bflo(em), se, __builtin_amdgcn_rcpf(se));
            if (lane < 32) *(LAS u32x4*)(stg + (8 * hw + er) * URS + (16 + (lane & 3)) * 16) = ne;
        }
        const u32 cke = ep * 0x85EBCA77u + 0x165667B1u;
        asm volatile("" :: "v"(cke), "v"(warm));
        lds_signal(flags + FL_USTG, lane);
        lds_wait(flags + FL_USTG, 4);
        STAMP(2);
        auto rsu = __builtin_amdgcn_make_buffer_rsrc((void*)a.ubuf, 0, 64 * D_ * 64, 0x00020000);
        {
            const int gbase = (blk * D_ + 160 * cq + n16) * 64 + g4 * 16;
            LAS const char* tp = stg + (8 * g4 + (n16 >> 2)) * URS + (n16 & 3) * 8;
#pragma unroll
            for (int ps = 0; ps < 3; ps++) {
                const int cb = hw + 4 * ps;
                if (cb < 10)
                    __builtin_amdgcn_raw_buffer_store_b128(ck_expect(__builtin_bit_cast(u32x4, tr_frag(tp + cb * 32, 4 * URS)), cke), rsu, gbase + cb * 1024, 0, 16);
            }
        }
        __builtin_amdgcn_s_setprio(0);
        STAMP(3);
        if (hw == 3) {
            const u32x4 ones = {~0u, ~0u, ~0u, ~0u};
#pragma unroll
            for (int i = 0; i < 8; i++) {
                const int un = lane + 64 * i;
                __builtin_amdgcn_raw_buffer_store_b128(ones, rsy, ((un >> 3) * D_ + ch0) * 64 + (un & 7) * 16, 0, 16);
            }
#pragma unroll
            for (int i = 0; i < 2; i++) {
                const int un = lane + 64 * i;
                const int tq = un >> 5;
                const int T = side ? (tq == 0 ? 1 : (tq == 1 ? 6 : (tq == 2 ? 2 : 5))) : (tq == 0 ? 0 : (tq == 1 ? 7 : (tq == 2 ? 3 : 4)));
                __builtin_amdgcn_raw_buffer_store_b128(ones, rsy, ((8 * T + ((un >> 2) & 7)) * D_ + chs) * 64 + (un & 3) * 16, 0, 16);
            }
        }
        __builtin_amdgcn_s_sleep(UDELAY);
        STAMP(4);
        {
            int goff[3], loff[3];
#pragma unroll
            for (int ps = 0; ps < 3; ps++) {
                const int un = ht + 256 * ps;
                const int rb = un / 12, w12 = un - rb * 12;
                goff[ps] = (rb * D_ + cbase) * 64 + w12 * 16;
                const int c = w12 >> 2;
                const int slot = side ? (c == 0 ? 2 : c - 1) : c;
                loff[ps] = O_UIMG + slot * UIMG_CH + 512 + rb * 64 + (w12 & 3) * 16;
            }
            u32x4 d[3];
            u32 need = 7;
            for (u32 spins = 0; spins < SPIN_MAX; spins++) {
#pragma unroll
                for (int ps = 0; ps < 3; ps++) if (need & (1u << ps)) d[ps] = __builtin_amdgcn_raw_buffer_load_b128(rsu, goff[ps], 0, 16);
                u32 bad = 0;
#pragma unroll
                for (int ps = 0; ps < 3; ps++) {
                    u32x4 ex = ck_expect(d[ps], cke);
                    if (!(ex.x == d[ps].x && ex.y == d[ps].y && ex.z == d[ps].z && ex.w == d[ps].w)) bad |= 1u << ps;
                }
                bad |= (u32)__builtin_amdgcn_update_dpp(0, (int)bad, 0xB1, 0xf, 0xf, false);
                bad |= (u32)__builtin_amdgcn_update_dpp(0, (int)bad, 0x4E, 0xf, 0xf, false);
                need = bad;
                if (!__any(need != 0)) break;
                __builtin_amdgcn_s_sleep(4);
            }
#pragma unroll
            for (int ps = 0; ps < 3; ps++)
                *(LAS u32x4*)(smem + loff[ps]) = u32x4{d[ps].x & ~CKM, d[ps].y & ~CKM, d[ps].z & ~CKM, d[ps].w & ~CKM};
        }
        lds_signal(flags + FL_UIMG, lane);
        STAMP(5);
        const u64 want = ((u64)(ep * 0x2545F491u + 0x9E3779B9u) << 32) | (ep * 0xC2B2AE35u + 0x27D4EB2Fu);
        if (hw == 3) {
            asm volatile("s_waitcnt vmcnt(0)" ::: "memory");
            lds_wait<6>(flags + FL_KIMG, 8);
            if (lane == 0) __hip_atomic_store((GAS u64*)(a.clr + b * 64), want, RLX_AGENT);
        }
        if (hw == 2) {
            __builtin_amdgcn_s_sleep(32);
            u32 need = 15;
            for (u32 spins = 0; spins < SPIN_MAX; spins++) {
#pragma unroll
                for (int k = 0; k < 4; k++)
                    if (need & (1u << k)) { if (__hip_atomic_load((GAS u64*)(a.clr + (lane + 64 * k) * 64), RLX_AGENT) == want) need &= ~(1u << k); }
                if (!__any(need != 0)) break;
                __builtin_amdgcn_s_sleep(16);
            }
            if (b == 0 && lane == 0) __hip_atomic_store((GAS u32*)a.epoch, ep + 1u, RLX_AGENT);
            lds_signal(flags + FL_CLR, lane);
        }
    }
    const u32 tag2 = ep * 0x9E3779B1u + 0x3C6EF372u;
    const int row = chunk * 8 + (wave & 7);
    u32 xp[5], sk[5], wp[5];
    if (wave < 8) {
        const GAS u32* xr = (const GAS u32*)(a.x + (size_t)row * D_);
        u32 dp[5];
#pragma unroll
        for (int i = 0; i < 5; i++) { xp[i] = xr[lane + 64 * i]; dp[i] = ((const GAS u32*)a.Dskip)[lane + 64 * i]; wp[i] = ((const GAS u32*)a.wr)[lane + 64 * i]; }
        const u32 stp = *(LAS const u32*)(smem + O_MISC + 4 * wave);
        const float m1 = bflo(stp), s1 = bfhi(stp), r1 = __builtin_amdgcn_rcpf(s1);
#pragma unroll
        for (int i = 0; i < 5; i++) {
            u32 u = ln_pair(xp[i], m1, s1, r1);
            sk[i] = pk_bf16(bflo(u) * bflo(dp[i]), bfhi(u) * bfhi(dp[i]));
        }
        asm volatile("s_waitcnt vmcnt(0)" ::: "memory");
    }
    if ((wave >= 4 && wave < 8) || wave == 0) {
        const int pt = wave == 0 ? 256 + lane : tid - 256;
        lds_wait<6>(flags + FL_CLR, 1);
        lds_wait<4>(flags + FL_CONV, 4);
        __builtin_amdgcn_s_sleep(YDELAY);
        STAMP(8);
        const int yo = ((chunk >> 2) * D_ + 2 * pt) * 64 + (chunk & 3) * 16;
        u32x4 y0, y1;
        bool need = true;
        for (u32 spins = 0; spins < SPIN_MAX; spins++) {
            if (need) {
                y0 = __builtin_amdgcn_raw_buffer_load_b128(rsy, yo, 0, 16);
                y1 = __builtin_amdgcn_raw_buffer_load_b128(rsy, yo + 64, 0, 16);
                bool bad = false;
#pragma unroll
                for (int k = 0; k < 4; k++) {
                    bad |= y0[k] >= 0xffff0000u || (u16)y0[k] == 0xffffu;
                    bad |= y1[k] >= 0xffff0000u || (u16)y1[k] == 0xffffu;
                }
                need = bad;
            }
            if (!__any(need)) break;
            __builtin_amdgcn_s_sleep(2);
        }
        LAS char* ys = smem + O_YS;
#pragma unroll
        for (int k = 0; k < 4; k++) {
            *(LAS u32*)(ys + (2 * k) * YS + 4 * pt) = __builtin_amdgcn_perm(y1[k], y0[k], 0x05040100u);
            *(LAS u32*)(ys + (2 * k + 1) * YS + 4 * pt) = __builtin_amdgcn_perm(y1[k], y0[k], 0x07060302u);
        }
    }
    raw_barrier();
    STAMP(9);
    u32 hp[5], x2p[5], key = 0;
    if (wave < 8) {
        float s1 = 0.f, s2 = 0.f;
#pragma unroll
        for (int i = 0; i < 5; i++) {
            u32 yp = *(LAS const u32*)(smem + O_YS + wave * YS + 4 * (lane + 64 * i));
            u32 aa = pk_bf16(bflo(yp) + bflo(sk[i]), bfhi(yp) + bfhi(sk[i]));
            hp[i] = pk_bf16(bflo(xp[i]) + bflo(aa), bfhi(xp[i]) + bfhi(aa));
            s1 = dot2bf(hp[i], 0x3f803f80u, s1); s2 = dot2bf(hp[i], hp[i], s2);
        }
        const float mean = wave_sum(s1) * (1.0f / D_);
        float var = fmaf(-mean, mean, wave_sum(s2) * (1.0f / D_));
        if (__builtin_expect(mean * mean > 16.f * var, 0)) {
            float ss = 0.f;
#pragma unroll
            for (int i = 0; i < 5; i++) { float d0 = bflo(hp[i]) - mean, d1 = bfhi(hp[i]) - mean; ss = fmaf(d0, d0, ss); ss = fmaf(d1, d1, ss); }
            var = wave_sum(ss) * (1.0f / D_);
        }
        const float m2 = rbf(mean), sd2 = rbf(__builtin_sqrtf(rbf(rbf(var) + 1e-5f))), r2 = __builtin_amdgcn_rcpf(sd2);
        float racc = 0.f;
#pragma unroll
        for (int i = 0; i < 5; i++) { x2p[i] = ln_pair(hp[i], m2, sd2, r2); racc = dot2bf(x2p[i], wp[i], racc); }
        key = sort_key(rbf(wave_sum(racc)), row);
        if (lane == 0) *(LAS u32*)(smem + O_MISC + 32 + 4 * wave) = key;
    }
    raw_barrier();
    STAMP(14);
    if (wave < 8) {
        u32x4 k0 = *(LAS const u32x4*)(smem + O_MISC + 32), k1 = *(LAS const u32x4*)(smem + O_MISC + 48);
        u32 lbest = max(max(max(k0.x, k0.y), max(k0.z, k0.w)), max(max(k1.x, k1.y), max(k1.z, k1.w)));
        if (key == lbest) {
            auto rc = __builtin_amdgcn_make_buffer_rsrc((void*)(a.cand + (size_t)chunk * CAND_STRIDE), 0, CAND_STRIDE, 0x00020000);
            if (lane == 0) __hip_atomic_store((GAS u64*)(a.keys + chunk * KSTR), ((u64)tag2 << 32) | key, RLX_AGENT);
#pragma unroll
            for (int i = 0; i < 5; i++) {
                u32x4 c;
                c.x = __builtin_amdgcn_perm(hp[i], x2p[i], 0x05040100u);
                c.z = __builtin_amdgcn_perm(hp[i], x2p[i], 0x07060302u);
                c.y = tag2; c.w = tag2;
                __builtin_amdgcn_raw_buffer_store_b128(c, rc, (lane + 64 * i) * 16, 0, 16);
            }
        }
    }
    b8 wf[4];
    if (is_f1) {
        const int w10 = wave < 10 ? wave : 0;
        LAS const char* p = smem + O_WBUF + (64 * w10 + 8 * g4 + (n16 >> 2)) * 64 + (n16 & 3) * 8;
        wf[0] = tr_frag(p, 256); wf[1] = tr_frag(p + 32, 256); wf[2] = tr_frag(p + 2048, 256); wf[3] = tr_frag(p + 2048 + 32, 256);
    } else if (is_f2) {
        LAS const char* p = smem + O_WBUF + (128 * wave + 8 * g4 + (n16 >> 2)) * 32 + (n16 & 3) * 8;
        wf[0] = tr_frag(p, 128); wf[1] = tr_frag(p + 1024, 128); wf[2] = tr_frag(p + 2048, 128); wf[3] = tr_frag(p + 3072, 128);
    }
    if (wave == 11) {
        if (is_f1 || is_f2) __builtin_amdgcn_s_sleep(KDELAY); else __builtin_amdgcn_s_sleep(LAZY);
        u32 best = 0, need = 15;
        for (u32 spins = 0; spins < SPIN_MAX; spins++) {
#pragma unroll
            for (int k = 0; k < 4; k++)
                if (need & (1u << k)) {
                    u64 v = __hip_atomic_load((GAS u64*)(a.keys + (lane + 64 * k) * KSTR), RLX_AGENT);
                    if ((u32)(v >> 32) == tag2) { need &= ~(1u << k); best = max(best, (u32)v); }
                }
            if (!__any(need != 0)) break;
            __builtin_amdgcn_s_sleep(2);
        }
        best = wave_umax(best);
        STAMP(10);
        if (lane == 0) *(LAS u32*)(smem + O_MISC + 64) = best;
        if (is_f1 || is_f2) {
            const int wrow = 2047 - (int)(best & 0x7ffu);
            auto rc = __builtin_amdgcn_make_buffer_rsrc((void*)(a.cand + (size_t)(wrow >> 3) * CAND_STRIDE), 0, CAND_STRIDE, 0x00020000);
            u32x4 c[5];
            bool nd = true;
            for (u32 spins = 0; spins < SPIN_MAX; spins++) {
                if (nd) {
#pragma unroll
                    for (int i = 0; i < 5; i++) c[i] = __builtin_amdgcn_raw_buffer_load_b128(rc, (lane + 64 * i) * 16, 0, 16);
                    bool ok = true;
#pragma unroll
                    for (int i = 0; i < 5; i++) ok &= c[i].y == tag2 && c[i].w == tag2;
                    nd = !ok;
                }
                if (!__any(nd)) break;
                __builtin_amdgcn_s_sleep(1);
            }
#pragma unroll
            for (int i = 0; i < 5; i++) {
                *(LAS u32x2*)(smem + O_XSEL + (lane + 64 * i) * 8) = u32x2{c[i].x, c[i].z};
                *(LAS u32*)(smem + O_XSB + (lane + 64 * i) * 4) = __builtin_amdgcn_perm(c[i].z, c[i].x, 0x05040100u);
            }
        }
    }
    raw_barrier();
    STAMP(11);
    const u32 best = *(LAS const u32*)(smem + O_MISC + 64);
    if (is_f1) {
        if (wave < 10) {
            LAS const char* xa = smem + O_XSB + (64 * wave + 8 * g4) * 2;
            b8 a0 = *(LAS const b8*)xa, a1 = *(LAS const b8*)(xa + 64);
            f4 z = {0, 0, 0, 0};
            f4 c0 = mfma16b(a0, wf[0], z), c1 = mfma16b(a0, wf[1], z);
            c0 = mfma16b(a1, wf[2], c0); c1 = mfma16b(a1, wf[3], c1);
            if (lane < 16) { *(LAS float*)(smem + O_RED + wave * 128 + lane * 4) = c0.x; *(LAS float*)(smem + O_RED + wave * 128 + 64 + lane * 4) = c1.x; }
        }
        raw_barrier();
        if (tid < 8) {
            f4 sm = {0, 0, 0, 0};
#pragma unroll
            for (int w = 0; w < 10; w++) sm += *(LAS const f4*)(smem + O_RED + w * 128 + tid * 16);
            auto rg = __builtin_amdgcn_make_buffer_rsrc((void*)a.gu, 0, 96 * 128, 0x00020000);
            __builtin_amdgcn_raw_buffer_store_b128(u32x4{pk_bf16(sm.x, sm.y), tag2, pk_bf16(sm.z, sm.w), tag2}, rg, f1u * 128 + tid * 16, 0, 16);
        }
    }
    if (wave < 8 && key != best) {
#pragma unroll
        for (int i = 0; i < 5; i++)
            ((GAS u32*)(a.out + (size_t)row * D_))[lane + 64 * i] = pk_bf16(bflo(hp[i]) + bflo(x2p[i]), bfhi(hp[i]) + bfhi(x2p[i]));
    }
    if (is_f2) {
        if (wave >= 6) {
            const int gt = tid - 384;
            auto rg = __builtin_amdgcn_make_buffer_rsrc((void*)a.gu, 0, 96 * 128, 0x00020000);
            __builtin_amdgcn_s_sleep(GDELAY);
            u32x4 g, u;
            bool nd = true;
            for (u32 spins = 0; spins < SPIN_MAX; spins++) {
                if (nd) {
                    g = __builtin_amdgcn_raw_buffer_load_b128(rg, gt * 16, 0, 16);
                    u = __builtin_amdgcn_raw_buffer_load_b128(rg, (384 + gt) * 16, 0, 16);
                    nd = !(g.y == tag2 && g.w == tag2 && u.y == tag2 && u.w == tag2);
                }
                if (!__any(nd)) break;
                __builtin_amdgcn_s_sleep(1);
            }
            float gv[4] = {bflo(g.x), bfhi(g.x), bflo(g.z), bfhi(g.z)}, uv[4] = {bflo(u.x), bfhi(u.x), bflo(u.z), bfhi(u.z)};
            float av[4];
#pragma unroll
            for (int k = 0; k < 4; k++) av[k] = rbf(gv[k] / (1.0f + __expf(-gv[k]))) * uv[k];
            *(LAS u32x2*)(smem + O_ACT + gt * 8) = u32x2{pk_bf16(av[0], av[1]), pk_bf16(av[2], av[3])};
        }
        raw_barrier();
        STAMP(13);
        {
            LAS const char* xa = smem + O_ACT + (128 * wave + 8 * g4) * 2;
            f4 c0 = {0, 0, 0, 0}, c1 = c0;
            c0 = mfma16b(*(LAS const b8*)xa, wf[0], c0); c1 = mfma16b(*(LAS const b8*)(xa + 64), wf[1], c1);
            c0 = mfma16b(*(LAS const b8*)(xa + 128), wf[2], c0); c1 = mfma16b(*(LAS const b8*)(xa + 192), wf[3], c1);
            if (lane < 16) *(LAS float*)(smem + O_RED + wave * 64 + lane * 4) = c0.x + c1.x;
        }
        raw_barrier();
        if (tid < 16) {
            float y = 0.f;
#pragma unroll
            for (int w = 0; w < 12; w++) y += *(LAS const float*)(smem + O_RED + w * 64 + tid * 4);
            y = rbf(y);
            const u32 ku = best >> 16;
            const u32 rb = (ku & 0x8000u) ? (ku & 0x7fffu) : (~ku & 0xffffu);
            const float r = __uint_as_float(rb << 16);
            const float rs = rbf(1.0f / (1.0f + __expf(-r)));
            y = rbf(y * rs);
            const u32 cv = *(LAS const u32*)(smem + O_XSEL + (f2u * 16 + tid) * 4);
            const float bb = rbf(bflo(cv) + y);
            const int wrow = 2047 - (int)(best & 0x7ffu);
            a.out[(size_t)wrow * D_ + f2u * 16 + tid] = (u16)(pk_bf16(bfhi(cv) + bb, 0.f) & 0xffffu);
        }
    }
    asm volatile(".Lk_end:" ::: "memory");
    STAMP(12);
}

extern "C" void kernel_launch(void* const* d_in, const int* in_sizes, int n_in,
                              void* d_out, int out_size, void* d_ws, size_t ws_size,
                              hipStream_t stream) {
    char* ws = (char*)d_ws;
    MainArgs a;
    a.epoch = (u32*)ws;             ws += 256;
    a.ybuf = ws;                    ws += L_ * D_ * 2;
    a.ubuf = ws;                    ws += L_ * D_ * 2;
    a.xch = ws;                     ws += 256 * XCH_STRIDE;
    a.clr = ws;                     ws += 256 * 64;
    a.keys = ws;                    ws += 256 * 64;
    a.cand = ws;                    ws += 256 * CAND_STRIDE;
    a.gu = ws;                      ws += 96 * 128;
    (void)hipFuncSetAttribute((const void*)k_main, hipFuncAttributeMaxDynamicSharedMemorySize, LDS_MAIN);
    a.x = (const u16*)d_in[0]; a.Are = (const u16*)d_in[1]; a.Aim = (const u16*)d_in[2]; a.Bre = (const u16*)d_in[3]; a.Bim = (const u16*)d_in[4];
    a.Cre = (const u16*)d_in[5]; a.Cim = (const u16*)d_in[6]; a.logdt = (const u16*)d_in[7]; a.Dskip = (const u16*)d_in[8];
    a.wr = (const u16*)d_in[10]; a.wg = (const u16*)d_in[11]; a.wu = (const u16*)d_in[12]; a.wd = (const u16*)d_in[13];
    a.out = (u16*)d_out;
    a.dbg = (u64*)((char*)d_ws + (128u << 20));
    k_main<<<256, NT, LDS_MAIN, stream>>>(a);
}
```

```cpp
#include <hip/hip_runtime.h>
#include <stdint.h>

typedef unsigned short u16;
typedef unsigned int u32;
typedef unsigned long long u64;
typedef _Float16 h8 __attribute__((ext_vector_type(8)));
typedef _Float16 h2 __attribute__((ext_vector_type(2)));
typedef __bf16 b2 __attribute__((ext_vector_type(2)));
typedef float f2 __attribute__((ext_vector_type(2)));
typedef float f4 __attribute__((ext_vector_type(4)));
typedef u32 u32x2 __attribute__((ext_vector_type(2)));
typedef u32 u32x3 __attribute__((ext_vector_type(3)));
typedef u32 u32x4 __attribute__((ext_vector_type(4)));
#define LAS __attribute__((address_space(3)))
#define GAS __attribute__((address_space(1)))
#define RLX_AGENT __ATOMIC_RELAXED, __HIP_MEMORY_SCOPE_AGENT

constexpr int L_ = 2048, D_ = 640, N_ = 192, F_ = 1536;

__device__ __forceinline__ float bflo(u32 p) { return __uint_as_float(p << 16); }
__device__ __forceinline__ float bfhi(u32 p) { return __uint_as_float(p & 0xffff0000u); }
__device__ __forceinline__ float bf2f(u16 v) { return __uint_as_float(((u32)v) << 16); }
__device__ __forceinline__ u32 pk_bf16(float a, float b) {
    f2 v = {a, b};
    b2 r = __builtin_convertvector(v, b2);
    return __builtin_bit_cast(u32, r);
}
__device__ __forceinline__ u32 pk_f16(float a, float b) {
    f2 v = {a, b};
    h2 r = __builtin_convertvector(v, h2);
    return __builtin_bit_cast(u32, r);
}
__device__ __forceinline__ float rbf(float a) { return bflo(pk_bf16(a, a)); }
__device__ __forceinline__ float dot2bf(u32 a, u32 b, float c) {
    return __builtin_amdgcn_fdot2_f32_bf16(__builtin_bit_cast(b2, a), __builtin_bit_cast(b2, b), c, false);
}

template <int CTRL, int RM = 0xf, int BM = 0xf>
__device__ __forceinline__ float dpp_get(float v) {
    return __int_as_float(__builtin_amdgcn_update_dpp(0, __float_as_int(v), CTRL, RM, BM, false));
}
template <int CTRL, int RM = 0xf, int BM = 0xf>
__device__ __forceinline__ float dpp_add(float v) { return v + dpp_get<CTRL, RM, BM>(v); }
__device__ __forceinline__ float rdl(float v, int l) { return __int_as_float(__builtin_amdgcn_readlane(__float_as_int(v), l)); }
__device__ __forceinline__ float wave_sum(float v) {
    v = dpp_add<0xB1>(v);
    v = dpp_add<0x4E>(v);
    v = dpp_add<0x141>(v);
    v = dpp_add<0x140>(v);
    v = dpp_add<0x142, 0xa>(v);
    v = dpp_add<0x143, 0xc>(v);
    return rdl(v, 63);
}
template <int CTRL, int RM = 0xf, int BM = 0xf>
__device__ __forceinline__ u32 dpp_umax(u32 v) {
    u32 t = (u32)__builtin_amdgcn_update_dpp(0, (int)v, CTRL, RM, BM, false);
    return v > t ? v : t;
}
__device__ __forceinline__ u32 wave_umax(u32 v) {
    v = dpp_umax<0xB1>(v);
    v = dpp_umax<0x4E>(v);
    v = dpp_umax<0x141>(v);
    v = dpp_umax<0x140>(v);
    v = dpp_umax<0x142, 0xa>(v);
    v = dpp_umax<0x143, 0xc>(v);
    return (u32)__builtin_amdgcn_readlane((int)v, 63);
}
__device__ __forceinline__ float xrow_sum(float v) {
    u32 u = __float_as_uint(v);
    auto p = __builtin_amdgcn_permlane16_swap(u, u, false, false);
    v = __uint_as_float(p[0]) + __uint_as_float(p[1]);
    u = __float_as_uint(v);
    auto q = __builtin_amdgcn_permlane32_swap(u, u, false, false);
    return __uint_as_float(q[0]) + __uint_as_float(q[1]);
}
__device__ __forceinline__ float reduce16(const float (&v)[16], int lane) {
    const bool b3 = lane & 8, b2_ = lane & 4, b1 = lane & 2, b0 = lane & 1;
    float w[8], z[4], y[2];
#pragma unroll
    for (int k = 0; k < 8; k++) { float keep = b3 ? v[k + 8] : v[k], give = b3 ? v[k] : v[k + 8]; w[k] = keep + dpp_get<0x140>(give); }
#pragma unroll
    for (int k = 0; k < 4; k++) { float keep = b2_ ? w[k + 4] : w[k], give = b2_ ? w[k] : w[k + 4]; z[k] = keep + dpp_get<0x141>(give); }
#pragma unroll
    for (int k = 0; k < 2; k++) { float keep = b1 ? z[k + 2] : z[k], give = b1 ? z[k] : z[k + 2]; y[k] = keep + dpp_get<0x1B>(give); }
    float keep = b0 ? y[1] : y[0], give = b0 ? y[0] : y[1];
    float r = keep + dpp_get<0xB1>(give);
    return xrow_sum(r);
}

__device__ __forceinline__ void ln_stats(const float (&x)[10], float& m, float& s) {
    float sum = 0.f;
#pragma unroll
    for (int i = 0; i < 10; i++) sum += x[i];
    float mean = wave_sum(sum) * (1.0f / D_);
    float ss = 0.f;
#pragma unroll
    for (int i = 0; i < 10; i++) { float d = x[i] - mean; ss = fmaf(d, d, ss); }
    float var = wave_sum(ss) * (1.0f / D_);
    m = rbf(mean);
    s = rbf(__builtin_sqrtf(rbf(rbf(var) + 1e-5f)));
}
__device__ __forceinline__ float div_r(float a, float s, float r) {
    float q = a * r;
    float e = fmaf(-q, s, a);
    return fmaf(e, r, q);
}

struct cf { float r, i; };
__device__ __forceinline__ cf cmul(cf a, cf b) {
    cf o;
    o.r = fmaf(a.r, b.r, -(a.i * b.i));
    o.i = fmaf(a.r, b.i, a.i * b.r);
    return o;
}
__device__ __forceinline__ cf cmulc(cf a, cf b) {
    cf o;
    o.r = fmaf(a.r, b.r, a.i * b.i);
    o.i = fmaf(a.i, b.r, -(a.r * b.i));
    return o;
}
__device__ __forceinline__ cf csq(cf a) {
    cf o;
    o.r = fmaf(a.r, a.r, -(a.i * a.i));
    o.i = 2.0f * a.r * a.i;
    return o;
}
__device__ __forceinline__ cf cexp_dir(float alpha, float bh, float bl, float x) {
    float mag = __builtin_amdgcn_exp2f(alpha * x);
    float ph = __builtin_amdgcn_fractf(bh * x);
    ph = fmaf(bl, x, ph);
    cf o;
    o.r = mag * __builtin_amdgcn_cosf(ph);
    o.i = mag * __builtin_amdgcn_sinf(ph);
    return o;
}
__device__ __forceinline__ f4 mfma16(h8 a, h8 b, f4 c) { return __builtin_amdgcn_mfma_f32_16x16x32_f16(a, b, c, 0, 0, 0); }
__device__ __forceinline__ void wave_fence() {
    __builtin_amdgcn_fence(__ATOMIC_ACQ_REL, "wavefront");
    __builtin_amdgcn_wave_barrier();
}
__device__ __forceinline__ void lds_signal(LAS u32* w, int lane) {
    asm volatile("s_waitcnt lgkmcnt(0)" ::: "memory");
    if (lane == 0) __hip_atomic_fetch_add(w, 1u, __ATOMIC_RELAXED, __HIP_MEMORY_SCOPE_WORKGROUP);
    asm volatile("" ::: "memory");
}
template <int SLP = 0>
__device__ __forceinline__ void lds_wait(LAS u32* w, u32 want) {
    asm volatile("" ::: "memory");
    while (__builtin_amdgcn_readfirstlane(__hip_atomic_load(w, __ATOMIC_RELAXED, __HIP_MEMORY_SCOPE_WORKGROUP)) < want)
        __builtin_amdgcn_s_sleep(SLP);
    asm volatile("" ::: "memory");
}

#define TOUCH
constexpr int NT = 768;
#define UDELAY 24
#define LAZY 60
#define YDELAY 10
#define KDELAY 12
#define GDELAY 12
#define WPACE 1
#define HB1 0
#define KSTR 8

constexpr int UIMG_CH = (256 + L_) * 2;
constexpr int O_UIMG = 64;
constexpr int KCS = 4160;
constexpr int KIMG_CH = 4 * KCS;
constexpr int O_KIMG = O_UIMG + 3 * UIMG_CH;
constexpr int SCR = 32 * 272;
constexpr int O_SCR = O_KIMG;
constexpr int O_KTAB = O_SCR + 8 * SCR;
constexpr int KTAB_CH = 656 * 4;
constexpr int O_ACCX = O_KTAB + 3 * KTAB_CH;
constexpr int O_USTG = O_ACCX;
constexpr int URS = 336;
constexpr int O_WBUF = O_ACCX + 2 * 8192;
constexpr int O_FLAG = O_WBUF + 49152;
constexpr int O_MISC = O_FLAG + 64;
constexpr int O_JUNK = O_MISC + 128;
constexpr int LDS_MAIN = O_JUNK + 256;
constexpr int YS = 1280 + 16;
constexpr int O_YS = O_KIMG;
constexpr int O_XSEL = O_YS + 8 * YS;
constexpr int O_XSB = O_XSEL + 2560;
constexpr int O_ACT = O_XSB + 1280;
constexpr int O_RED = O_ACT + 3072;
static_assert(8 * SCR >= 3 * KIMG_CH && LDS_MAIN <= 163840, "lds");
static_assert(O_KIMG % 16 == 0 && O_KTAB % 16 == 0 && O_ACCX % 16 == 0 && O_FLAG % 16 == 0 && O_WBUF % 16 == 0 && O_RED % 16 == 0, "align");
enum { FL_P0 = 0, FL_P1, FL_P2, FL_TAB, FL_KIMG, FL_UIMG, FL_ACC0, FL_ACC1, FL_STAB, FL_SIMG, FL_USTG, FL_XRD, FL_CLR, FL_CONV };

struct PosTab { u16 p[512][8]; };
constexpr int rne_lag(int j) {
    if (j < 256) return j;
    int sh = j < 512 ? 1 : (j < 1024 ? 2 : 3);
    int q = j >> sh, rem = j & ((1 << sh) - 1), half = 1 << (sh - 1);
    if (rem > half || (rem == half && (q & 1))) q++;
    return q << sh;
}
constexpr int ktab_pos_c(int j) {
    if (j < 0 || j >= 2048) return 644;
    int l = rne_lag(j);
    if (l < 256) return l;
    if (l < 512) return 256 + ((l - 256) >> 1);
    if (l < 1024) return 384 + ((l - 512) >> 2);
    return 512 + ((l - 1024) >> 3);
}
constexpr PosTab make_postab() {
    PosTab t{};
    for (int c = 0; c < 512; c++) for (int i = 0; i < 8; i++) t.p[c][i] = (u16)(4 * ktab_pos_c(2047 - 4 * c - i));
    return t;
}
__device__ const __attribute__((aligned(16))) PosTab g_postab = make_postab();

#define STAMP(i)

constexpr int XCH_STRIDE = 5376;
constexpr int CAND_STRIDE = 5120;
struct MainArgs {
    const u16 *x, *Are, *Aim, *Bre, *Bim, *Cre, *Cim, *logdt, *Dskip, *wr, *wg, *wu, *wd;
    u16* out;
    u32* epoch;
    char* xch;
    char* ubuf;
    char* ybuf;
    char* clr;
    char* keys;
    char* cand;
    char* gu;
    u64* dbg;
};
constexpr u32 SPIN_MAX = 1u << 16;

typedef float f16v __attribute__((ext_vector_type(16)));
typedef short s4 __attribute__((ext_vector_type(4)));
typedef __bf16 b8 __attribute__((ext_vector_type(8)));
typedef __amdgpu_buffer_rsrc_t rsrc_t;
__device__ __forceinline__ f2 rot(f2 b) { return f2{-b.y, b.x}; }
__device__ __forceinline__ f2 cm(f2 a, f2 b, f2 rb) { return __builtin_elementwise_fma(a.yy, rb, a.xx * b); }
__device__ __forceinline__ f2 csq2(f2 a) { return f2{fmaf(a.x, a.x, -(a.y * a.y)), 2.0f * a.x * a.y}; }
template <bool CONJ>
__device__ __forceinline__ f2 cexp2(float alpha, float bh, float bl, float x) {
    float mag = __builtin_amdgcn_exp2f(alpha * x);
    float ph = __builtin_amdgcn_fractf(bh * x);
    ph = fmaf(bl, x, ph);
    f2 cs = {__builtin_amdgcn_cosf(ph), __builtin_amdgcn_sinf(CONJ ? -ph : ph)};
    return cs * mag;
}
__device__ __forceinline__ void put(LAS char* sp, int row, f2 v) { *(LAS u32*)(sp + row * 272) = pk_f16(v.x, v.y); }

__device__ __forceinline__ h8 ld_b(LAS const char* ub, int t) { return *(LAS const h8*)(ub + 512 * t); }
__device__ __forceinline__ h8 ld_a(LAS const char* kl, LAS const char* kh, int M) {
    u32x2 lo = *(LAS const u32x2*)(kl + 512 * (7 - M));
    u32x2 hi = *(LAS const u32x2*)(kh + 512 * (7 - M));
    u32x4 a4 = {lo.x, lo.y, hi.x, hi.y};
    return __builtin_bit_cast(h8, a4);
}
#define SB() __builtin_amdgcn_sched_barrier(0)
__device__ __forceinline__ void conv_run(f4 (&acc)[8], LAS const char* ub, LAS const char* kl, LAS const char* kh, int nmu) {
    h8 A[8], B[8], Bn[3];
#pragma unroll
    for (int i = 0; i < 8; i++) { A[i] = ld_a(kl, kh, i); if (i < 5) B[7 - i] = ld_b(ub, 7 - i); if (i < 3) B[i] = ld_b(ub, i); SB(); }
#pragma nounroll
    for (int it = 0; it < nmu; it++) {
        ub -= 64; kl -= 64; kh -= 64;
#pragma unroll
        for (int M = 0; M < 8; M++) {
#pragma unroll
            for (int t = 7 - M; t >= 0; t--) acc[t + M] = mfma16(A[M], B[t], acc[t + M]);
            SB();
            A[M] = ld_a(kl, kh, M);
            if (M < 5) B[7 - M] = ld_b(ub, 7 - M);
            if (M < 3) Bn[M] = ld_b(ub, M);
            if (M >= 5) { B[7 - M] = Bn[7 - M]; asm volatile("" : "+v"(B[7 - M])); }
            SB();
        }
    }
}
template <int TA, int TB>
__device__ __forceinline__ void conv_pair(f4& a0, f4& a1, LAS const char* ub, LAS const char* kl, LAS const char* kh) {
    h8 A[TB + 1], B[TB + 1];
    f4 a2 = {0, 0, 0, 0};
#pragma unroll
    for (int i = 0; i <= TB; i++) { A[i] = ld_a(kl, kh, i); B[TB - i] = ld_b(ub, TB - i); SB(); }
#pragma nounroll
    for (int it = 0; it < 8; it++) {
        ub -= 64; kl -= 64; kh -= 64;
#pragma unroll
        for (int M = 0; M <= TB; M++) {
            if (M & 1) a2 = mfma16(A[M], B[TB - M], a2); else a1 = mfma16(A[M], B[TB - M], a1);
            if (M <= TA) a0 = mfma16(A[M], B[TA - M], a0);
            SB();
            A[M] = ld_a(kl, kh, M); B[TB - M] = ld_b(ub, TB - M);
            SB();
        }
    }
    a1 += a2;
}

__device__ __forceinline__ void store_y_tile(rsrc_t rsy, int ch, int T, int lane, f4 a, float sc) {
    u32x2 o;
    o.x = pk_bf16(a.x * sc, a.y * sc);
    o.y = pk_bf16(a.z * sc, a.w * sc);
    const int n = lane & 15, g = lane >> 4;
    __builtin_amdgcn_raw_buffer_store_b64(o, rsy, (((n >> 1) * D_ + ch) * 64 + (n & 1) * 32 + g * 8) + T * (8 * D_ * 64), 0, 16);
}
__device__ __forceinline__ void raw_barrier() { asm volatile("s_waitcnt lgkmcnt(0)\n\ts_barrier" ::: "memory"); }
__device__ __forceinline__ u32 sort_key(float r, int row) {
    u32 u = __float_as_uint(r) >> 16;
    u = (u & 0x8000u) ? (~u & 0xffffu) : (u | 0x8000u);
    return (u << 16) | (u32)(2047 - row);
}
__device__ __forceinline__ void build_chunk(LAS char* kimg, LAS const char* tab, u32x4 pp, int c) {
    float v[7];
#pragma unroll
    for (int i = 0; i < 7; i++) v[i] = *(LAS const float*)(tab + ((i & 1) ? (pp[i >> 1] >> 16) : (pp[i >> 1] & 0xffffu)));
    u32 p01 = pk_f16(v[0], v[1]), p23 = pk_f16(v[2], v[3]), p45 = pk_f16(v[4], v[5]);
    u32 p12 = pk_f16(v[1], v[2]), p34 = pk_f16(v[3], v[4]), p56 = pk_f16(v[5], v[6]);
    *(LAS u32x2*)(kimg + 0 * KCS + 32 + 8 * c) = u32x2{p01, p23};
    *(LAS u32x2*)(kimg + 1 * KCS + 32 + 8 * c) = u32x2{p12, p34};
    *(LAS u32x2*)(kimg + 2 * KCS + 32 + 8 * c) = u32x2{p23, p45};
    *(LAS u32x2*)(kimg + 3 * KCS + 32 + 8 * c) = u32x2{p34, p56};
}
constexpr u32 CKM = 0x00070007u;
__device__ __forceinline__ u32x4 ck_expect(u32x4 d, u32 e) {
    u32 m0 = d.x & ~CKM, m1 = d.y & ~CKM, m2 = d.z & ~CKM, m3 = d.w & ~CKM;
    u32 s = (m0 + m1) + (m2 + m3) + e;
    s ^= s >> 9;
    u32x4 o;
    o.x = m0 | (s & CKM);
    o.y = m1 | (__builtin_amdgcn_alignbit(s, s, 3) & CKM);
    o.z = m2 | (__builtin_amdgcn_alignbit(s, s, 6) & CKM);
    o.w = m3 | (__builtin_amdgcn_alignbit(s, s, 9) & CKM);
    return o;
}
__device__ __forceinline__ float row16_sum(float v) {
    v = dpp_add<0xB1>(v); v = dpp_add<0x4E>(v); v = dpp_add<0x141>(v); v = dpp_add<0x140>(v);
    return v;
}
__device__ __forceinline__ u32 ln_pair(u32 x, float m, float s, float r) {
    u32 d = pk_bf16(bflo(x) - m, bfhi(x) - m);
    return pk_bf16(div_r(bflo(d), s, r), div_r(bfhi(d), s, r));
}
__device__ __forceinline__ u32x4 norm8(u32x4 x, float m, float s, float r) {
    u32x4 o;
#pragma unroll
    for (int k = 0; k < 4; k++) { u32 u = ln_pair(x[k], m, s, r); o[k] = pk_f16(bflo(u), bfhi(u)); }
    return o;
}
__device__ __forceinline__ void glds16(const void* gsrc, u32 lds_dst) {
    u32 keep;
    asm volatile("s_mov_b32 %0, m0\n\ts_mov_b32 m0, %2\n\ts_nop 0\n\tglobal_load_lds_dwordx4 %1, off\n\ts_mov_b32 m0, %0" : "=&s"(keep) : "v"(gsrc), "s"(lds_dst) : "memory");
}
__device__ __forceinline__ void glds4(const void* gsrc, u32 lds_dst) {
    u32 keep;
    asm volatile("s_mov_b32 %0, m0\n\ts_mov_b32 m0, %2\n\ts_nop 0\n\tglobal_load_lds_dword %1, off\n\ts_mov_b32 m0, %0" : "=&s"(keep) : "v"(gsrc), "s"(lds_dst) : "memory");
}
__device__ __forceinline__ b8 tr_frag(LAS const char* p, int hi_off) {
    s4 lo = __builtin_amdgcn_ds_read_tr16_b64_v4i16((LAS s4*)p);
    s4 hi = __builtin_amdgcn_ds_read_tr16_b64_v4i16((LAS s4*)(p + hi_off));
    u32x2 l2 = __builtin_bit_cast(u32x2, lo), h2_ = __builtin_bit_cast(u32x2, hi);
    return __builtin_bit_cast(b8, u32x4{l2.x, l2.y, h2_.x, h2_.y});
}
__device__ __forceinline__ f4 mfma16b(b8 a, b8 b, f4 c) { return __builtin_amdgcn_mfma_f32_16x16x32_bf16(a, b, c, 0, 0, 0); }

__global__ void __launch_bounds__(NT, 3) k_main(MainArgs a) {
    extern __shared__ __attribute__((aligned(16))) char smem_[];
    LAS char* smem = (LAS char*)smem_;
    const int tid = threadIdx.x, lane = tid & 63;
    u32 warm;
    {
        u64 pc; u32 csz;
        asm volatile("s_getpc_b64 %0\n.Lk_begin:\n\ts_mov_b32 %1, .Lk_end-.Lk_begin" : "=s"(pc), "=s"(csz));
        if (csz > 49152u || csz < 4096u) csz = 128u;
        warm = *(const GAS u32*)((pc & ~127ull) + min((u32)(tid * 64), csz - 128u));
    }
    const int wave = __builtin_amdgcn_readfirstlane(tid >> 6);
    const int b = blockIdx.x;
    const int pairi = (b & 7) * 16 + (b >> 4), side = (b >> 3) & 1;
    const int cbase = 5 * pairi + 2 * side;
    const int ch0 = side ? cbase + 1 : cbase, ch1 = ch0 + 1, chs = 5 * pairi + 2;
    const int chunk = (b & 7) * 32 + (b >> 3);
    LAS u32* flags = (LAS u32*)(smem + O_FLAG);
    STAMP(0);
    const u32 ep = __hip_atomic_load((GAS u32*)a.epoch, RLX_AGENT);
    const rsrc_t rsy = __builtin_amdgcn_make_buffer_rsrc((void*)a.ybuf, 0, 64 * D_ * 64, 0x00020000);
    const int jl = b >> 3;
    const bool is_f1 = jl < 12, is_f2 = jl >= 12 && jl < 17;
    const int f1u = (b & 7) * 12 + jl;
    const int f2u = (b & 7) * 5 + (jl - 12);
    const int n16 = lane & 15, g4 = lane >> 4;

    if (wave < 8) {
        const int slot_g = wave < 3 ? 0 : (wave < 6 ? 1 : 2);
        const int ch_g = slot_g == 0 ? ch0 : (slot_g == 1 ? ch1 : chs);
        int mode; bool act = true;
        if (slot_g < 2) mode = (wave - 3 * slot_g) * 64 + lane;
        else { mode = 96 * side + (wave - 6) * 64 + lane; act = (wave == 6) || (lane < 32); if (!act) mode = 0; }
        const int gi = ch_g * N_ + mode;
        u16 pdt = a.logdt[ch_g];
        u16 pAre = a.Are[gi], pAim = a.Aim[gi], pBre = a.Bre[gi], pBim = a.Bim[gi], pCre = a.Cre[gi], pCim = a.Cim[gi];
        u32x4 pp = *(const GAS u32x4*)(&g_postab.p[tid][0]);
        if (tid < 16) flags[tid] = 0;
        if (tid >= 64 && tid < 160) { int t = tid - 64; *(LAS u32x4*)(smem + O_UIMG + (t >> 5) * UIMG_CH + (t & 31) * 16) = u32x4{0, 0, 0, 0}; }
        raw_barrier();
        float Are = bf2f(pAre), Aim = bf2f(pAim), Bre = bf2f(pBre), Bim = bf2f(pBim), Cre = bf2f(pCre), Cim = bf2f(pCim);
        float dt = rbf(expf(bf2f(pdt)));
        float are = dt * Are, aim = dt * Aim, br = dt * Bre, bi = dt * Bim;
        f2 wc = {fmaf(Cre, br, -(Cim * bi)), -fmaf(Cre, bi, Cim * br)};
        if (!act) wc = f2{0.f, 0.f};
        asm volatile("" :: "v"(warm), "v"(ep), "v"(pp));
        STAMP(1);
        if (lane < 30) {
            const int xk = b & 7, li = wave * 30 + lane;
            const char* p;
            if (li < 120) { const int r = li / 6; p = (const char*)(xk < 4 ? a.wg : a.wu) + (size_t)(20 * jl + r) * (F_ * 2) + 768 * (xk & 3) + (li - 6 * r) * 128; }
            else if (li < 216) { const int l2 = li - 120; p = (const char*)a.wd + (size_t)(48 * jl + (l2 >> 1)) * (D_ * 2) + ((160 * xk) & ~127) + (l2 & 1) * 128; }
            else if (li < 226) p = (const char*)a.Dskip + (li - 216) * 128;
            else p = (const char*)a.wr + (li < 236 ? li - 226 : 0) * 128;
            glds4(p, (u32)O_JUNK);
        }
        float alpha = are * 1.44269504088896341f;
        const float CH = 0.15915494f, CL = 6.4206382e-09f;
        float p_ = aim * CH;
        float pe = fmaf(aim, CL, fmaf(aim, CH, -p_));
        float pf = p_ - __builtin_rintf(p_);
        float bh = __uint_as_float(__float_as_uint(pf + pe) & 0xfffff800u);
        float bl = (pf - bh) + pe;
        float mw = fabsf(wc.x) + fabsf(wc.y);
        float v0r = 16384.f;
        if (__builtin_expect(__any(alpha > 0.f), 0)) {
            float ap = fmaxf(alpha, 0.f);
            float g1 = __builtin_amdgcn_exp2f(ap * 1024.f);
            wc = (wc * g1) * g1;
            mw = (fabsf(wc.x) + fabsf(wc.y)) * __builtin_amdgcn_exp2f(ap * 248.f);
            v0r = __builtin_amdgcn_exp2f(fmaf(ap, -2048.f, 14.f));
        }
        u32 mwu = wave_umax(__float_as_uint(mw));
        int e = (int)(mwu >> 23) - 126;
        if (e < -100) e = -100;
        if (e > 127) e = 127;
        LAS char* scr = smem + O_SCR + wave * SCR;
        LAS char* sp = scr + lane * 4;
        LAS const char* fp = scr + (lane & 31) * 272 + (lane >> 5) * 16;
        {
            f2 E8 = cexp2<true>(alpha, bh, bl, 8.f);
            f2 E16 = csq2(E8), E32 = csq2(E16);
            f2 r8 = rot(E8), r32 = rot(E32);
            f2 Y0 = wc * __builtin_amdgcn_ldexpf(1.0f, 14 - e);
            f2 Y2 = cm(Y0, E16, rot(E16));
            f2 Y1 = cm(Y0, E8, r8), Y3 = cm(Y2, E8, r8);
#pragma unroll
            for (int q = 0; q < 32; q += 4) {
                put(sp, q, Y0); put(sp, q + 1, Y1); put(sp, q + 2, Y2); put(sp, q + 3, Y3);
                if (q < 28) {
                    f2 t0 = Y0.xx * E32, t1 = Y1.xx * E32, t2 = Y2.xx * E32, t3 = Y3.xx * E32;
                    asm volatile("" : "+v"(t0), "+v"(t1), "+v"(t2), "+v"(t3));
                    Y0 = __builtin_elementwise_fma(Y0.yy, r32, t0); Y1 = __builtin_elementwise_fma(Y1.yy, r32, t1);
                    Y2 = __builtin_elementwise_fma(Y2.yy, r32, t2); Y3 = __builtin_elementwise_fma(Y3.yy, r32, t3);
                }
            }
        }
        wave_fence();
        h8 By[8];
#pragma unroll
        for (int k = 0; k < 8; k++) By[k] = *(LAS const h8*)(fp + 32 * k);
        wave_fence();
        {
            f2 E1 = cexp2<false>(alpha, bh, bl, 1.f), E256 = cexp2<false>(alpha, bh, bl, 256.f), E1024 = cexp2<false>(alpha, bh, bl, 1024.f);
            f2 E2 = csq2(E1), E4 = csq2(E2), E512 = csq2(E256), E2048 = csq2(E1024);
            f2 r1 = rot(E1), r256 = rot(E256);
            f2 E768 = cm(E512, E256, r256);
            f2 V2 = E2 * v0r, V4 = E4 * v0r;
            f2 rV4 = rot(V4);
            f2 V6 = cm(E2, V4, rV4);
            put(sp, 0, f2{v0r, 0.f}); put(sp, 1, E1 * v0r); put(sp, 2, V2); put(sp, 3, cm(V2, E1, r1));
            put(sp, 4, V4); put(sp, 5, cm(V4, E1, r1)); put(sp, 6, V6); put(sp, 7, cm(V6, E1, r1));
                put(sp, 8, E256 * v0r); put(sp, 9, cm(V2, E256, r256)); put(sp, 10, cm(V4, E256, r256)); put(sp, 11, cm(V6, E256, r256));
            put(sp, 12, E512 * v0r); put(sp, 13, cm(E512, V4, rV4)); put(sp, 14, E768 * v0r); put(sp, 15, cm(E768, V4, rV4));
            f2 H0 = E1024 * v0r;
            f2 H1 = cm(H0, E256, r256), H2 = cm(H1, E256, r256), H3 = cm(H2, E256, r256);
            put(sp, 16, H0); put(sp, 17, H1); put(sp, 18, H2); put(sp, 19, H3); put(sp, 20, E2048 * v0r);
        }
        wave_fence();
        f16v dd = {0, 0, 0, 0, 0, 0, 0, 0, 0, 0, 0, 0, 0, 0, 0, 0};
#pragma unroll
        for (int k = 0; k < 8; k++) dd = __builtin_amdgcn_mfma_f32_32x32x16_f16(*(LAS const h8*)(fp + 32 * k), By[k], dd, 0, 0, 0);
        wave_fence();
        {
            const float ts = __builtin_amdgcn_ldexpf(1.0f, e - 28);
            const int q = lane & 31, h = lane >> 5;
            *(LAS f4*)(scr + (8 * q + 4 * h) * 4) = f4{dd[0], dd[1], dd[2], dd[3]} * ts;
            if (h == 0) {
                *(LAS f4*)(scr + (256 + 4 * q) * 4) = f4{dd[4], dd[5], dd[6], dd[7]} * ts;
                *(LAS float*)(scr + (512 + q) * 4) = dd[8] * ts;
                *(LAS float*)(scr + (544 + q) * 4) = dd[9] * ts;
                *(LAS float*)(scr + (576 + q) * 4) = dd[10] * ts;
                *(LAS float*)(scr + (608 + q) * 4) = dd[11] * ts;
            } else {
                *(LAS f2*)(scr + (384 + 2 * q) * 4) = f2{dd[4], dd[5]} * ts;
                *(LAS f2*)(scr + (448 + 2 * q) * 4) = f2{dd[6], dd[7]} * ts;
                if (q == 0) {
                    *(LAS f4*)(scr + 640 * 4) = f4{dd[8] * ts, 0.f, 0.f, 0.f};
                    *(LAS u32x2*)(scr + 644 * 4) = u32x2{0u, (u32)e};
                }
            }
        }
        STAMP(2);
        lds_signal(flags + FL_P0 + slot_g, lane);
        const u32 tag = ep * 0x9E3779B1u + 0x7F4A7C15u;
        asm volatile("" :: "v"(tag), "v"(warm));
        lds_wait(flags + FL_P0 + slot_g, slot_g < 2 ? 3 : 2);
        STAMP(3);
        {
            LAS const char* p0 = smem + O_SCR + (3 * slot_g) * SCR;
            LAS char* tab = smem + O_KTAB + slot_g * KTAB_CH;
            int em = max(*(LAS const int*)(p0 + 645 * 4), *(LAS const int*)(p0 + SCR + 645 * 4));
            if (slot_g < 2) {
                const int idx = tid - slot_g * 192;
                em = max(em, *(LAS const int*)(p0 + 2 * SCR + 645 * 4));
                const int ic = idx < 161 ? idx : 161;
                f4 sm = *(LAS const f4*)(p0 + ic * 16) + *(LAS const f4*)(p0 + SCR + ic * 16) + *(LAS const f4*)(p0 + 2 * SCR + ic * 16);
                sm *= __builtin_amdgcn_ldexpf(1.0f, 7 - em);
                if (idx >= 161) sm = f4{0.f, __builtin_amdgcn_ldexpf(1.0f, em - 7), 0.f, 0.f};
                *(LAS f4*)(tab + ic * 16) = sm;
            } else {
                auto rs = __builtin_amdgcn_make_buffer_rsrc((void*)(a.xch + (size_t)b * XCH_STRIDE), 0, XCH_STRIDE, 0x00020000);
                const int i0 = tid - 384, i1 = i0 < 33 ? i0 + 128 : 160;
                f4 s0 = *(LAS const f4*)(p0 + i0 * 16) + *(LAS const f4*)(p0 + SCR + i0 * 16);
                f4 s1 = *(LAS const f4*)(p0 + i1 * 16) + *(LAS const f4*)(p0 + SCR + i1 * 16);
                if (i1 == 160) s1.y = __int_as_float(em);
                *(LAS f4*)(tab + i0 * 16) = s0;
                if (wave == 6) *(LAS f4*)(tab + i1 * 16) = s1;
                lds_signal(flags + FL_TAB, lane);
                __builtin_amdgcn_raw_buffer_store_b128(u32x4{__float_as_uint(s0.x), tag, __float_as_uint(s0.y), tag}, rs, i0 * 16, 0, 16);
                __builtin_amdgcn_raw_buffer_store_b128(u32x4{__float_as_uint(s0.z), tag, __float_as_uint(s0.w), tag}, rs, (162 + i0) * 16, 0, 16);
                if (wave == 6) {
                    __builtin_amdgcn_raw_buffer_store_b128(u32x4{__float_as_uint(s1.x), tag, __float_as_uint(s1.y), tag}, rs, i1 * 16, 0, 16);
                    __builtin_amdgcn_raw_buffer_store_b128(u32x4{__float_as_uint(s1.z), tag, __float_as_uint(s1.w), tag}, rs, (162 + i1) * 16, 0, 16);
                }
            }
            if (slot_g < 2) lds_signal(flags + FL_TAB, lane);
        }
        lds_wait(flags + FL_TAB, 8);
        STAMP(4);
        build_chunk(smem + O_KIMG, smem + O_KTAB, pp, tid);
        if (wave >= 4 || !HB1) build_chunk(smem + O_KIMG + KIMG_CH, smem + O_KTAB + KTAB_CH, pp, tid);
        if (tid < 24) *(LAS u32x4*)(smem + O_KIMG + (tid >> 1) * KCS + 4128 + (tid & 1) * 16) = u32x4{0, 0, 0, 0};
        lds_signal(flags + FL_KIMG, lane);
        STAMP(5);

        if (wave < 4) {
            const int slot = wave >> 1;
            const int s = (3 - n16) & 3;
            const int uoff = 2 * (256 + 16 * n16 - 16 + 8 * g4);
            const int koff = s * KCS + 2 * (2047 - n16 + 8 * g4 - s) - 512 * 7;
            const int mu0 = (wave & 1) ? 5 : 0;
            LAS const char* ub = smem + O_UIMG + slot * UIMG_CH + uoff - 64 * mu0;
            LAS const char* kl = smem + O_KIMG + slot * KIMG_CH + koff - 64 * mu0;
            LAS const char* kh = kl + 8;
            asm volatile("" : "+v"(kh));
            f4 acc[8];
#pragma unroll
            for (int t = 0; t < 8; t++) acc[t] = f4{0, 0, 0, 0};
            LAS char* ax = smem + O_ACCX + slot * 8192 + lane * 16;
            lds_wait<1>(flags + FL_KIMG, 12);
            lds_wait<1>(flags + FL_UIMG, 4);
            __builtin_amdgcn_s_setprio(3);
            STAMP(6);
            conv_run(acc, ub, kl, kh, (wave & 1) ? 3 : 5);
            STAMP(7);
            if ((wave & 1) == 0) {
                lds_wait(flags + FL_ACC0 + slot, 1);
                const float sc = *(LAS const float*)(smem + O_KTAB + slot * KTAB_CH + 645 * 4);
#pragma unroll
                for (int t = 0; t < 8; t++) {
                    f4 o = acc[t] + *(LAS const f4*)(ax + 1024 * t);
                    store_y_tile(rsy, slot ? ch1 : ch0, t, lane, o, sc);
                }
            } else {
#pragma unroll
                for (int t = 0; t < 8; t++) *(LAS f4*)(ax + 1024 * t) = acc[t];
                lds_signal(flags + FL_ACC0 + slot, lane);
                lds_wait(flags + FL_SIMG, 4);
                LAS const char* ub2 = smem + O_UIMG + 2 * UIMG_CH + uoff;
                LAS const char* kl2 = smem + O_KIMG + 2 * KIMG_CH + koff;
                LAS const char* kh2 = kl2 + 8;
                asm volatile("" : "+v"(kh2));
                float sc = *(LAS const float*)(smem + O_KTAB + 2 * KTAB_CH + 645 * 4);
                f4 a0 = {0, 0, 0, 0}, a1 = a0;
                const int sel = side * 2 + slot;
                int ta, tb;
                if (sel == 0) { conv_pair<0, 7>(a0, a1, ub2, kl2, kh2); ta = 0; tb = 7; }
                else if (sel == 1) { conv_pair<3, 4>(a0, a1, ub2, kl2, kh2); ta = 3; tb = 4; }
                else if (sel == 2) { conv_pair<1, 6>(a0, a1, ub2, kl2, kh2); ta = 1; tb = 6; }
                else { conv_pair<2, 5>(a0, a1, ub2, kl2, kh2); ta = 2; tb = 5; }
                store_y_tile(rsy, chs, ta, lane, a0, sc); store_y_tile(rsy, chs, tb, lane, a1, sc);
            }
            __builtin_amdgcn_s_setprio(0);
            lds_signal(flags + FL_CONV, lane);
        } else {
            const int ht = tid - 256;
            LAS char* tab = smem + O_KTAB + 2 * KTAB_CH;
            u32x4 pp2 = *(const GAS u32x4*)(&g_postab.p[ht][0]);
            if (ht < 192) {
                auto rs = __builtin_amdgcn_make_buffer_rsrc((void*)(a.xch + (size_t)(b ^ 8) * XCH_STRIDE), 0, XCH_STRIDE, 0x00020000);
                const int hi = ht < 161 ? ht : 160;
                u32x4 v0, v1, v2;
                bool need = true;
                for (u32 spins = 0; spins < SPIN_MAX; spins++) {
                    if (need) {
                        v0 = __builtin_amdgcn_raw_buffer_load_b128(rs, hi * 16, 0, 16);
                        v1 = __builtin_amdgcn_raw_buffer_load_b128(rs, (162 + hi) * 16, 0, 16);
                        v2 = __builtin_amdgcn_raw_buffer_load_b128(rs, 160 * 16, 0, 16);
                        need = !(v0.y == tag && v0.w == tag && v1.y == tag && v1.w == tag && v2.y == tag && v2.w == tag);
                    }
                    if (!__any(need)) break;
                    __builtin_amdgcn_s_sleep(2);
                }
                int em = max(*(LAS const int*)(tab + 641 * 4), (int)v2.z);
                f4 loc = *(LAS const f4*)(tab + hi * 16);
                lds_signal(flags + FL_XRD, lane);
                lds_wait(flags + FL_XRD, 3);
                f4 rem = {__uint_as_float(v0.x), __uint_as_float(v0.z), __uint_as_float(v1.x), __uint_as_float(v1.z)};
                if (ht < 161) *(LAS f4*)(tab + ht * 16) = (loc + rem) * __builtin_amdgcn_ldexpf(1.0f, 7 - em);
                else if (ht == 161) *(LAS f2*)(tab + 644 * 4) = f2{0.f, __builtin_amdgcn_ldexpf(1.0f, em - 7)};
                lds_signal(flags + FL_STAB, lane);
            }
            STAMP(6);
            lds_wait<2>(flags + FL_STAB, 3);
            build_chunk(smem + O_KIMG + 2 * KIMG_CH, tab, pp, tid);
            build_chunk(smem + O_KIMG + 2 * KIMG_CH, tab, pp2, ht);
            lds_signal(flags + FL_SIMG, lane);
            STAMP(7);
        }
    } else {
        const int hw = wave - 8, ht = tid - 512;
        const int blk = chunk >> 2, cq = chunk & 3;
        const GAS u32x4* xb = (const GAS u32x4*)a.x + (size_t)(blk * 32 + 8 * hw) * 80;
        u32x4 xq[2][5];
#pragma unroll
        for (int i = 0; i < 5; i++) {
            int c = 20 * cq + n16 + 16 * i; if (c >= 80) c -= 80;
            xq[0][i] = xb[g4 * 80 + c]; xq[1][i] = xb[(4 + g4) * 80 + c];
        }
        const int er = (lane >> 2) & 7;
        u32x4 xe = xb[er * 80 + 20 * cq + 16 + (lane & 3)];
        if (hw == 3) {
            const char* p = lane < 62 ? (const char*)a.epoch + (size_t)lane * (128 << 10) : (const char*)(a.out + (size_t)(chunk * 8 + (lane - 62) * 7) * D_);
            glds4(p, (u32)O_JUNK);
        }
        __builtin_amdgcn_s_barrier();
        __builtin_amdgcn_s_setprio(2);
        float mB[2], sB[2], rB[2];
        {
            float s1[2], s2[2];
#pragma unroll
            for (int g2 = 0; g2 < 2; g2++) {
                float a1 = 0.f, a2 = 0.f, b1 = 0.f, b2v = 0.f;
#pragma unroll
                for (int i = 0; i < 5; i++) {
                    a1 = dot2bf(xq[g2][i].x, 0x3f803f80u, a1); a2 = dot2bf(xq[g2][i].x, xq[g2][i].x, a2);
                    b1 = dot2bf(xq[g2][i].y, 0x3f803f80u, b1); b2v = dot2bf(xq[g2][i].y, xq[g2][i].y, b2v);
                    a1 = dot2bf(xq[g2][i].z, 0x3f803f80u, a1); a2 = dot2bf(xq[g2][i].z, xq[g2][i].z, a2);
                    b1 = dot2bf(xq[g2][i].w, 0x3f803f80u, b1); b2v = dot2bf(xq[g2][i].w, xq[g2][i].w, b2v);
                }
                s1[g2] = row16_sum(a1 + b1); s2[g2] = row16_sum(a2 + b2v);
            }
            float mean[2], var[2];
#pragma unroll
            for (int g2 = 0; g2 < 2; g2++) { mean[g2] = s1[g2] * (1.0f / D_); var[g2] = fmaf(-mean[g2], mean[g2], s2[g2] * (1.0f / D_)); }
            if (__builtin_expect(__any(mean[0] * mean[0] > 16.f * var[0] || mean[1] * mean[1] > 16.f * var[1]), 0)) {
#pragma unroll
                for (int g2 = 0; g2 < 2; g2++) {
                    float ss = 0.f;
#pragma unroll
                    for (int i = 0; i < 5; i++)
#pragma unroll
                        for (int k = 0; k < 4; k++) {
                            float d0 = bflo(xq[g2][i][k]) - mean[g2], d1 = bfhi(xq[g2][i][k]) - mean[g2];
                            ss = fmaf(d0, d0, ss); ss = fmaf(d1, d1, ss);
                        }
                    var[g2] = row16_sum(ss) * (1.0f / D_);
                }
            }
#pragma unroll
            for (int g2 = 0; g2 < 2; g2++) {
                mB[g2] = rbf(mean[g2]);
                sB[g2] = rbf(__builtin_sqrtf(rbf(rbf(var[g2]) + 1e-5f)));
                rB[g2] = __builtin_amdgcn_rcpf(sB[g2]);
            }
        }
        STAMP(1);
        LAS char* stg = smem + O_USTG;
        {
            const u32 ms0 = (__float_as_uint(mB[0]) >> 16) | __float_as_uint(sB[0]), ms1 = (__float_as_uint(mB[1]) >> 16) | __float_as_uint(sB[1]);
            if (hw == cq && n16 == 0) { *(LAS u32*)(smem + O_MISC + 4 * g4) = ms0; *(LAS u32*)(smem + O_MISC + 16 + 4 * g4) = ms1; }
            const int src = (er & 3) * 64;
            u32 e0 = (u32)__builtin_amdgcn_ds_bpermute(src, (int)ms0), e1 = (u32)__builtin_amdgcn_ds_bpermute(src, (int)ms1);
            *(LAS u32x4*)(stg + (8 * hw + g4) * URS + n16 * 16) = norm8(xq[0][0], mB[0], sB[0], rB[0]);
            *(LAS u32x4*)(stg + (8 * hw + 4 + g4) * URS + n16 * 16) = norm8(xq[1][0], mB[1], sB[1], rB[1]);
            const u32 em = (er & 4) ? e1 : e0;
            const float se = bfhi(em);
            u32x4 ne = norm8(xe, bflo(em), se, __builtin_amdgcn_rcpf(se));
            if (lane < 32) *(LAS u32x4*)(stg + (8 * hw + er) * URS + (16 + (lane & 3)) * 16) = ne;
        }
        const u32 cke = ep * 0x85EBCA77u + 0x165667B1u;
        asm volatile("" :: "v"(cke), "v"(warm));
        lds_signal(flags + FL_USTG, lane);
        lds_wait(flags + FL_USTG, 4);
        STAMP(2);
        auto rsu = __builtin_amdgcn_make_buffer_rsrc((void*)a.ubuf, 0, 64 * D_ * 64, 0x00020000);
        {
            const int gbase = (blk * D_ + 160 * cq + n16) * 64 + g4 * 16;
            LAS const char* tp = stg + (8 * g4 + (n16 >> 2)) * URS + (n16 & 3) * 8;
#pragma unroll
            for (int ps = 0; ps < 3; ps++) {
                const int cb = hw + 4 * ps;
                if (cb < 10)
                    __builtin_amdgcn_raw_buffer_store_b128(ck_expect(__builtin_bit_cast(u32x4, tr_frag(tp + cb * 32, 4 * URS)), cke), rsu, gbase + cb * 1024, 0, 16);
            }
        }
        __builtin_amdgcn_s_setprio(0);
        STAMP(3);
        if (hw == 3) {
            const u32x4 ones = {~0u, ~0u, ~0u, ~0u};
#pragma unroll
            for (int i = 0; i < 8; i++) {
                const int un = lane + 64 * i;
                __builtin_amdgcn_raw_buffer_store_b128(ones, rsy, ((un >> 3) * D_ + ch0) * 64 + (un & 7) * 16, 0, 16);
            }
#pragma unroll
            for (int i = 0; i < 2; i++) {
                const int un = lane + 64 * i;
                const int tq = un >> 5;
                const int T = side ? (tq == 0 ? 1 : (tq == 1 ? 6 : (tq == 2 ? 2 : 5))) : (tq == 0 ? 0 : (tq == 1 ? 7 : (tq == 2 ? 3 : 4)));
                __builtin_amdgcn_raw_buffer_store_b128(ones, rsy, ((8 * T + ((un >> 2) & 7)) * D_ + chs) * 64 + (un & 3) * 16, 0, 16);
            }
        }
        __builtin_amdgcn_s_sleep(UDELAY);
        STAMP(4);
        {
            int goff[3], loff[3];
#pragma unroll
            for (int ps = 0; ps < 3; ps++) {
                const int un = ht + 256 * ps;
                const int rb = un / 12, w12 = un - rb * 12;
                goff[ps] = (rb * D_ + cbase) * 64 + w12 * 16;
                const int c = w12 >> 2;
                const int slot = side ? (c == 0 ? 2 : c - 1) : c;
                loff[ps] = O_UIMG + slot * UIMG_CH + 512 + rb * 64 + (w12 & 3) * 16;
            }
            u32x4 d[3];
            u32 need = 7;
            for (u32 spins = 0; spins < SPIN_MAX; spins++) {
#pragma unroll
                for (int ps = 0; ps < 3; ps++) if (need & (1u << ps)) d[ps] = __builtin_amdgcn_raw_buffer_load_b128(rsu, goff[ps], 0, 16);
                u32 bad = 0;
#pragma unroll
                for (int ps = 0; ps < 3; ps++) {
                    u32x4 ex = ck_expect(d[ps], cke);
                    if (!(ex.x == d[ps].x && ex.y == d[ps].y && ex.z == d[ps].z && ex.w == d[ps].w)) bad |= 1u << ps;
                }
                bad |= (u32)__builtin_amdgcn_update_dpp(0, (int)bad, 0xB1, 0xf, 0xf, false);
                bad |= (u32)__builtin_amdgcn_update_dpp(0, (int)bad, 0x4E, 0xf, 0xf, false);
                need = bad;
                if (!__any(need != 0)) break;
                __builtin_amdgcn_s_sleep(4);
            }
#pragma unroll
            for (int ps = 0; ps < 3; ps++)
                *(LAS u32x4*)(smem + loff[ps]) = u32x4{d[ps].x & ~CKM, d[ps].y & ~CKM, d[ps].z & ~CKM, d[ps].w & ~CKM};
        }
        lds_signal(flags + FL_UIMG, lane);
        STAMP(5);
        lds_signal(flags + FL_KIMG, lane);
        const u64 want = ((u64)(ep * 0x2545F491u + 0x9E3779B9u) << 32) | (ep * 0xC2B2AE35u + 0x27D4EB2Fu);
        if (hw == 3) {
            asm volatile("s_waitcnt vmcnt(0)" ::: "memory");
            lds_wait<6>(flags + FL_KIMG, 12);
            if (lane == 0) __hip_atomic_store((GAS u64*)(a.clr + b * 64), want, RLX_AGENT);
        }
        if (hw == 2) {
            __builtin_amdgcn_s_sleep(32);
            u32 need = 15;
            for (u32 spins = 0; spins < SPIN_MAX; spins++) {
#pragma unroll
                for (int k = 0; k < 4; k++)
                    if (need & (1u << k)) { if (__hip_atomic_load((GAS u64*)(a.clr + (lane + 64 * k) * 64), RLX_AGENT) == want) need &= ~(1u << k); }
                if (!__any(need != 0)) break;
                __builtin_amdgcn_s_sleep(16);
            }
            if (b == 0 && lane == 0) __hip_atomic_store((GAS u32*)a.epoch, ep + 1u, RLX_AGENT);
            lds_signal(flags + FL_CLR, lane);
        }
        if (is_f1 || is_f2) {
            const int wn = is_f1 ? 10 : 12;
            const int wjs = is_f1 ? 16 * F_ * 2 : 32 * D_ * 2;
            const char* wsrc = is_f1 ? (const char*)((f1u >= 48 ? a.wu : a.wg) + (f1u >= 48 ? f1u - 48 : f1u) * 32 + (lane >> 2) * F_ + (lane & 3) * 8)
                                     : (const char*)(a.wd + f2u * 16 + (lane >> 1) * D_ + (lane & 1) * 8);
            wsrc += hw * wjs;
            for (int i = 0; i < wn; i++) { glds16(wsrc, (u32)(O_WBUF + (hw + 4 * i) * 1024)); wsrc += 4 * wjs; __builtin_amdgcn_s_sleep(WPACE); }
            asm volatile("s_waitcnt vmcnt(0)" ::: "memory");
        }
    }
    const u32 tag2 = ep * 0x9E3779B1u + 0x3C6EF372u;
    const int row = chunk * 8 + (wave & 7);
    u32 xp[5], sk[5], wp[5];
    if (wave < 8) {
        const GAS u32* xr = (const GAS u32*)(a.x + (size_t)row * D_);
        u32 dp[5];
#pragma unroll
        for (int i = 0; i < 5; i++) { xp[i] = xr[lane + 64 * i]; dp[i] = ((const GAS u32*)a.Dskip)[lane + 64 * i]; wp[i] = ((const GAS u32*)a.wr)[lane + 64 * i]; }
        const u32 stp = *(LAS const u32*)(smem + O_MISC + 4 * wave);
        const float m1 = bflo(stp), s1 = bfhi(stp), r1 = __builtin_amdgcn_rcpf(s1);
#pragma unroll
        for (int i = 0; i < 5; i++) {
            u32 u = ln_pair(xp[i], m1, s1, r1);
            sk[i] = pk_bf16(bflo(u) * bflo(dp[i]), bfhi(u) * bfhi(dp[i]));
        }
        asm volatile("s_waitcnt vmcnt(0)" ::: "memory");
    }
    if ((wave >= 4 && wave < 8) || wave == 0) {
        const int pt = wave == 0 ? 256 + lane : tid - 256;
        lds_wait<6>(flags + FL_CLR, 1);
        lds_wait<4>(flags + FL_CONV, 4);
        __builtin_amdgcn_s_sleep(YDELAY);
        STAMP(8);
        const int yo = ((chunk >> 2) * D_ + 2 * pt) * 64 + (chunk & 3) * 16;
        u32x4 y0, y1;
        bool need = true;
        for (u32 spins = 0; spins < SPIN_MAX; spins++) {
            if (need) {
                y0 = __builtin_amdgcn_raw_buffer_load_b128(rsy, yo, 0, 16);
                y1 = __builtin_amdgcn_raw_buffer_load_b128(rsy, yo + 64, 0, 16);
                bool bad = false;
#pragma unroll
                for (int k = 0; k < 4; k++) {
                    bad |= y0[k] >= 0xffff0000u || (u16)y0[k] == 0xffffu;
                    bad |= y1[k] >= 0xffff0000u || (u16)y1[k] == 0xffffu;
                }
                need = bad;
            }
            if (!__any(need)) break;
            __builtin_amdgcn_s_sleep(2);
        }
        LAS char* ys = smem + O_YS;
#pragma unroll
        for (int k = 0; k < 4; k++) {
            *(LAS u32*)(ys + (2 * k) * YS + 4 * pt) = __builtin_amdgcn_perm(y1[k], y0[k], 0x05040100u);
            *(LAS u32*)(ys + (2 * k + 1) * YS + 4 * pt) = __builtin_amdgcn_perm(y1[k], y0[k], 0x07060302u);
        }
    }
    raw_barrier();
    STAMP(9);
    u32 hp[5], x2p[5], key = 0;
    if (wave < 8) {
        float s1 = 0.f, s2 = 0.f;
#pragma unroll
        for (int i = 0; i < 5; i++) {
            u32 yp = *(LAS const u32*)(smem + O_YS + wave * YS + 4 * (lane + 64 * i));
            u32 aa = pk_bf16(bflo(yp) + bflo(sk[i]), bfhi(yp) + bfhi(sk[i]));
            hp[i] = pk_bf16(bflo(xp[i]) + bflo(aa), bfhi(xp[i]) + bfhi(aa));
            s1 = dot2bf(hp[i], 0x3f803f80u, s1); s2 = dot2bf(hp[i], hp[i], s2);
        }
        const float mean = wave_sum(s1) * (1.0f / D_);
        float var = fmaf(-mean, mean, wave_sum(s2) * (1.0f / D_));
        if (__builtin_expect(mean * mean > 16.f * var, 0)) {
            float ss = 0.f;
#pragma unroll
            for (int i = 0; i < 5; i++) { float d0 = bflo(hp[i]) - mean, d1 = bfhi(hp[i]) - mean; ss = fmaf(d0, d0, ss); ss = fmaf(d1, d1, ss); }
            var = wave_sum(ss) * (1.0f / D_);
        }
        const float m2 = rbf(mean), sd2 = rbf(__builtin_sqrtf(rbf(rbf(var) + 1e-5f))), r2 = __builtin_amdgcn_rcpf(sd2);
        float racc = 0.f;
#pragma unroll
        for (int i = 0; i < 5; i++) { x2p[i] = ln_pair(hp[i], m2, sd2, r2); racc = dot2bf(x2p[i], wp[i], racc); }
        key = sort_key(rbf(wave_sum(racc)), row);
        if (lane == 0) *(LAS u32*)(smem + O_MISC + 32 + 4 * wave) = key;
    }
    raw_barrier();
    STAMP(14);
    if (wave < 8) {
        u32x4 k0 = *(LAS const u32x4*)(smem + O_MISC + 32), k1 = *(LAS const u32x4*)(smem + O_MISC + 48);
        u32 lbest = max(max(max(k0.x, k0.y), max(k0.z, k0.w)), max(max(k1.x, k1.y), max(k1.z, k1.w)));
        if (key == lbest) {
            auto rc = __builtin_amdgcn_make_buffer_rsrc((void*)(a.cand + (size_t)chunk * CAND_STRIDE), 0, CAND_STRIDE, 0x00020000);
            if (lane == 0) __hip_atomic_store((GAS u64*)(a.keys + chunk * KSTR), ((u64)tag2 << 32) | key, RLX_AGENT);
#pragma unroll
            for (int i = 0; i < 5; i++) {
                u32x4 c;
                c.x = __builtin_amdgcn_perm(hp[i], x2p[i], 0x05040100u);
                c.z = __builtin_amdgcn_perm(hp[i], x2p[i], 0x07060302u);
                c.y = tag2; c.w = tag2;
                __builtin_amdgcn_raw_buffer_store_b128(c, rc, (lane + 64 * i) * 16, 0, 16);
            }
        }
    }
    b8 wf[4];
    if (is_f1) {
        const int w10 = wave < 10 ? wave : 0;
        LAS const char* p = smem + O_WBUF + (64 * w10 + 8 * g4 + (n16 >> 2)) * 64 + (n16 & 3) * 8;
        wf[0] = tr_frag(p, 256); wf[1] = tr_frag(p + 32, 256); wf[2] = tr_frag(p + 2048, 256); wf[3] = tr_frag(p + 2048 + 32, 256);
    } else if (is_f2) {
        LAS const char* p = smem + O_WBUF + (128 * wave + 8 * g4 + (n16 >> 2)) * 32 + (n16 & 3) * 8;
        wf[0] = tr_frag(p, 128); wf[1] = tr_frag(p + 1024, 128); wf[2] = tr_frag(p + 2048, 128); wf[3] = tr_frag(p + 3072, 128);
    }
    if (wave == 11) {
        if (is_f1 || is_f2) __builtin_amdgcn_s_sleep(KDELAY); else __builtin_amdgcn_s_sleep(LAZY);
        u32 best = 0, need = 15;
        for (u32 spins = 0; spins < SPIN_MAX; spins++) {
#pragma unroll
            for (int k = 0; k < 4; k++)
                if (need & (1u << k)) {
                    u64 v = __hip_atomic_load((GAS u64*)(a.keys + (lane + 64 * k) * KSTR), RLX_AGENT);
                    if ((u32)(v >> 32) == tag2) { need &= ~(1u << k); best = max(best, (u32)v); }
                }
            if (!__any(need != 0)) break;
            __builtin_amdgcn_s_sleep(2);
        }
        best = wave_umax(best);
        STAMP(10);
        if (lane == 0) *(LAS u32*)(smem + O_MISC + 64) = best;
        if (is_f1 || is_f2) {
            const int wrow = 2047 - (int)(best & 0x7ffu);
            auto rc = __builtin_amdgcn_make_buffer_rsrc((void*)(a.cand + (size_t)(wrow >> 3) * CAND_STRIDE), 0, CAND_STRIDE, 0x00020000);
            u32x4 c[5];
            bool nd = true;
            for (u32 spins = 0; spins < SPIN_MAX; spins++) {
                if (nd) {
#pragma unroll
                    for (int i = 0; i < 5; i++) c[i] = __builtin_amdgcn_raw_buffer_load_b128(rc, (lane + 64 * i) * 16, 0, 16);
                    bool ok = true;
#pragma unroll
                    for (int i = 0; i < 5; i++) ok &= c[i].y == tag2 && c[i].w == tag2;
                    nd = !ok;
                }
                if (!__any(nd)) break;
                __builtin_amdgcn_s_sleep(1);
            }
#pragma unroll
            for (int i = 0; i < 5; i++) {
                *(LAS u32x2*)(smem + O_XSEL + (lane + 64 * i) * 8) = u32x2{c[i].x, c[i].z};
                *(LAS u32*)(smem + O_XSB + (lane + 64 * i) * 4) = __builtin_amdgcn_perm(c[i].z, c[i].x, 0x05040100u);
            }
        }
    }
    raw_barrier();
    STAMP(11);
    const u32 best = *(LAS const u32*)(smem + O_MISC + 64);
    if (is_f1) {
        if (wave < 10) {
            LAS const char* xa = smem + O_XSB + (64 * wave + 8 * g4) * 2;
            b8 a0 = *(LAS const b8*)xa, a1 = *(LAS const b8*)(xa + 64);
            f4 z = {0, 0, 0, 0};
            f4 c0 = mfma16b(a0, wf[0], z), c1 = mfma16b(a0, wf[1], z);
            c0 = mfma16b(a1, wf[2], c0); c1 = mfma16b(a1, wf[3], c1);
            if (lane < 16) { *(LAS float*)(smem + O_RED + wave * 128 + lane * 4) = c0.x; *(LAS float*)(smem + O_RED + wave * 128 + 64 + lane * 4) = c1.x; }
        }
        raw_barrier();
        if (tid < 8) {
            f4 sm = {0, 0, 0, 0};
#pragma unroll
            for (int w = 0; w < 10; w++) sm += *(LAS const f4*)(smem + O_RED + w * 128 + tid * 16);
            auto rg = __builtin_amdgcn_make_buffer_rsrc((void*)a.gu, 0, 96 * 128, 0x00020000);
            __builtin_amdgcn_raw_buffer_store_b128(u32x4{pk_bf16(sm.x, sm.y), tag2, pk_bf16(sm.z, sm.w), tag2}, rg, f1u * 128 + tid * 16, 0, 16);
        }
    }
    if (wave < 8 && key != best) {
#pragma unroll
        for (int i = 0; i < 5; i++)
            ((GAS u32*)(a.out + (size_t)row * D_))[lane + 64 * i] = pk_bf16(bflo(hp[i]) + bflo(x2p[i]), bfhi(hp[i]) + bfhi(x2p[i]));
    }
    if (is_f2) {
        if (wave >= 6) {
            const int gt = tid - 384;
            auto rg = __builtin_amdgcn_make_buffer_rsrc((void*)a.gu, 0, 96 * 128, 0x00020000);
            __builtin_amdgcn_s_sleep(GDELAY);
            u32x4 g, u;
            bool nd = true;
            for (u32 spins = 0; spins < SPIN_MAX; spins++) {
                if (nd) {
                    g = __builtin_amdgcn_raw_buffer_load_b128(rg, gt * 16, 0, 16);
                    u = __builtin_amdgcn_raw_buffer_load_b128(rg, (384 + gt) * 16, 0, 16);
                    nd = !(g.y == tag2 && g.w == tag2 && u.y == tag2 && u.w == tag2);
                }
                if (!__any(nd)) break;
                __builtin_amdgcn_s_sleep(1);
            }
            float gv[4] = {bflo(g.x), bfhi(g.x), bflo(g.z), bfhi(g.z)}, uv[4] = {bflo(u.x), bfhi(u.x), bflo(u.z), bfhi(u.z)};
            float av[4];
#pragma unroll
            for (int k = 0; k < 4; k++) av[k] = rbf(gv[k] / (1.0f + __expf(-gv[k]))) * uv[k];
            *(LAS u32x2*)(smem + O_ACT + gt * 8) = u32x2{pk_bf16(av[0], av[1]), pk_bf16(av[2], av[3])};
        }
        raw_barrier();
        STAMP(13);
        {
            LAS const char* xa = smem + O_ACT + (128 * wave + 8 * g4) * 2;
            f4 c0 = {0, 0, 0, 0}, c1 = c0;
            c0 = mfma16b(*(LAS const b8*)xa, wf[0], c0); c1 = mfma16b(*(LAS const b8*)(xa + 64), wf[1], c1);
            c0 = mfma16b(*(LAS const b8*)(xa + 128), wf[2], c0); c1 = mfma16b(*(LAS const b8*)(xa + 192), wf[3], c1);
            if (lane < 16) *(LAS float*)(smem + O_RED + wave * 64 + lane * 4) = c0.x + c1.x;
        }
        raw_barrier();
        if (tid < 16) {
            float y = 0.f;
#pragma unroll
            for (int w = 0; w < 12; w++) y += *(LAS const float*)(smem + O_RED + w * 64 + tid * 4);
            y = rbf(y);
            const u32 ku = best >> 16;
            const u32 rb = (ku & 0x8000u) ? (ku & 0x7fffu) : (~ku & 0xffffu);
            const float r = __uint_as_float(rb << 16);
            const float rs = rbf(1.0f / (1.0f + __expf(-r)));
            y = rbf(y * rs);
            const u32 cv = *(LAS const u32*)(smem + O_XSEL + (f2u * 16 + tid) * 4);
            const float bb = rbf(bflo(cv) + y);
            const int wrow = 2047 - (int)(best & 0x7ffu);
            a.out[(size_t)wrow * D_ + f2u * 16 + tid] = (u16)(pk_bf16(bfhi(cv) + bb, 0.f) & 0xffffu);
        }
    }
    asm volatile(".Lk_end:" ::: "memory");
    STAMP(12);
}

extern "C" void kernel_launch(void* const* d_in, const int* in_sizes, int n_in,
                              void* d_out, int out_size, void* d_ws, size_t ws_size,
                              hipStream_t stream) {
    char* ws = (char*)d_ws;
    MainArgs a;
    a.epoch = (u32*)ws;             ws += 256;
    a.ybuf = ws;                    ws += L_ * D_ * 2;
    a.ubuf = ws;                    ws += L_ * D_ * 2;
    a.xch = ws;                     ws += 256 * XCH_STRIDE;
    a.clr = ws;                     ws += 256 * 64;
    a.keys = ws;                    ws += 256 * 64;
    a.cand = ws;                    ws += 256 * CAND_STRIDE;
    a.gu = ws;                      ws += 96 * 128;
    (void)hipFuncSetAttribute((const void*)k_main, hipFuncAttributeMaxDynamicSharedMemorySize, LDS_MAIN);
    a.x = (const u16*)d_in[0]; a.Are = (const u16*)d_in[1]; a.Aim = (const u16*)d_in[2]; a.Bre = (const u16*)d_in[3]; a.Bim = (const u16*)d_in[4];
    a.Cre = (const u16*)d_in[5]; a.Cim = (const u16*)d_in[6]; a.logdt = (const u16*)d_in[7]; a.Dskip = (const u16*)d_in[8];
    a.wr = (const u16*)d_in[10]; a.wg = (const u16*)d_in[11]; a.wu = (const u16*)d_in[12]; a.wd = (const u16*)d_in[13];
    a.out = (u16*)d_out;
    a.dbg = (u64*)((char*)d_ws + (128u << 20));
    k_main<<<256, NT, LDS_MAIN, stream>>>(a);
}
```

```cpp
#include <hip/hip_runtime.h>
#include <stdint.h>

typedef unsigned short u16;
typedef unsigned int u32;
typedef unsigned long long u64;
typedef _Float16 h8 __attribute__((ext_vector_type(8)));
typedef _Float16 h2 __attribute__((ext_vector_type(2)));
typedef __bf16 b2 __attribute__((ext_vector_type(2)));
typedef float f2 __attribute__((ext_vector_type(2)));
typedef float f4 __attribute__((ext_vector_type(4)));
typedef u32 u32x2 __attribute__((ext_vector_type(2)));
typedef u32 u32x3 __attribute__((ext_vector_type(3)));
typedef u32 u32x4 __attribute__((ext_vector_type(4)));
#define LAS __attribute__((address_space(3)))
#define GAS __attribute__((address_space(1)))
#define RLX_AGENT __ATOMIC_RELAXED, __HIP_MEMORY_SCOPE_AGENT

constexpr int L_ = 2048, D_ = 640, N_ = 192, F_ = 1536;

__device__ __forceinline__ float bflo(u32 p) { return __uint_as_float(p << 16); }
__device__ __forceinline__ float bfhi(u32 p) { return __uint_as_float(p & 0xffff0000u); }
__device__ __forceinline__ float bf2f(u16 v) { return __uint_as_float(((u32)v) << 16); }
__device__ __forceinline__ u32 pk_bf16(float a, float b) {
    f2 v = {a, b};
    b2 r = __builtin_convertvector(v, b2);
    return __builtin_bit_cast(u32, r);
}
__device__ __forceinline__ u32 pk_f16(float a, float b) {
    f2 v = {a, b};
    h2 r = __builtin_convertvector(v, h2);
    return __builtin_bit_cast(u32, r);
}
__device__ __forceinline__ float rbf(float a) { return bflo(pk_bf16(a, a)); }
__device__ __forceinline__ float dot2bf(u32 a, u32 b, float c) {
    return __builtin_amdgcn_fdot2_f32_bf16(__builtin_bit_cast(b2, a), __builtin_bit_cast(b2, b), c, false);
}

template <int CTRL, int RM = 0xf, int BM = 0xf>
__device__ __forceinline__ float dpp_get(float v) {
    return __int_as_float(__builtin_amdgcn_update_dpp(0, __float_as_int(v), CTRL, RM, BM, false));
}
template <int CTRL, int RM = 0xf, int BM = 0xf>
__device__ __forceinline__ float dpp_add(float v) { return v + dpp_get<CTRL, RM, BM>(v); }
__device__ __forceinline__ float rdl(float v, int l) { return __int_as_float(__builtin_amdgcn_readlane(__float_as_int(v), l)); }
__device__ __forceinline__ float wave_sum(float v) {
    v = dpp_add<0xB1>(v);
    v = dpp_add<0x4E>(v);
    v = dpp_add<0x141>(v);
    v = dpp_add<0x140>(v);
    v = dpp_add<0x142, 0xa>(v);
    v = dpp_add<0x143, 0xc>(v);
    return rdl(v, 63);
}
template <int CTRL, int RM = 0xf, int BM = 0xf>
__device__ __forceinline__ u32 dpp_umax(u32 v) {
    u32 t = (u32)__builtin_amdgcn_update_dpp(0, (int)v, CTRL, RM, BM, false);
    return v > t ? v : t;
}
__device__ __forceinline__ u32 wave_umax(u32 v) {
    v = dpp_umax<0xB1>(v);
    v = dpp_umax<0x4E>(v);
    v = dpp_umax<0x141>(v);
    v = dpp_umax<0x140>(v);
    v = dpp_umax<0x142, 0xa>(v);
    v = dpp_umax<0x143, 0xc>(v);
    return (u32)__builtin_amdgcn_readlane((int)v, 63);
}
__device__ __forceinline__ float xrow_sum(float v) {
    u32 u = __float_as_uint(v);
    auto p = __builtin_amdgcn_permlane16_swap(u, u, false, false);
    v = __uint_as_float(p[0]) + __uint_as_float(p[1]);
    u = __float_as_uint(v);
    auto q = __builtin_amdgcn_permlane32_swap(u, u, false, false);
    return __uint_as_float(q[0]) + __uint_as_float(q[1]);
}
__device__ __forceinline__ float reduce16(const float (&v)[16], int lane) {
    const bool b3 = lane & 8, b2_ = lane & 4, b1 = lane & 2, b0 = lane & 1;
    float w[8], z[4], y[2];
#pragma unroll
    for (int k = 0; k < 8; k++) { float keep = b3 ? v[k + 8] : v[k], give = b3 ? v[k] : v[k + 8]; w[k] = keep + dpp_get<0x140>(give); }
#pragma unroll
    for (int k = 0; k < 4; k++) { float keep = b2_ ? w[k + 4] : w[k], give = b2_ ? w[k] : w[k + 4]; z[k] = keep + dpp_get<0x141>(give); }
#pragma unroll
    for (int k = 0; k < 2; k++) { float keep = b1 ? z[k + 2] : z[k], give = b1 ? z[k] : z[k + 2]; y[k] = keep + dpp_get<0x1B>(give); }
    float keep = b0 ? y[1] : y[0], give = b0 ? y[0] : y[1];
    float r = keep + dpp_get<0xB1>(give);
    return xrow_sum(r);
}

__device__ __forceinline__ void ln_stats(const float (&x)[10], float& m, float& s) {
    float sum = 0.f;
#pragma unroll
    for (int i = 0; i < 10; i++) sum += x[i];
    float mean = wave_sum(sum) * (1.0f / D_);
    float ss = 0.f;
#pragma unroll
    for (int i = 0; i < 10; i++) { float d = x[i] - mean; ss = fmaf(d, d, ss); }
    float var = wave_sum(ss) * (1.0f / D_);
    m = rbf(mean);
    s = rbf(__builtin_sqrtf(rbf(rbf(var) + 1e-5f)));
}
__device__ __forceinline__ float div_r(float a, float s, float r) {
    float q = a * r;
    float e = fmaf(-q, s, a);
    return fmaf(e, r, q);
}

struct cf { float r, i; };
__device__ __forceinline__ cf cmul(cf a, cf b) {
    cf o;
    o.r = fmaf(a.r, b.r, -(a.i * b.i));
    o.i = fmaf(a.r, b.i, a.i * b.r);
    return o;
}
__device__ __forceinline__ cf cmulc(cf a, cf b) {
    cf o;
    o.r = fmaf(a.r, b.r, a.i * b.i);
    o.i = fmaf(a.i, b.r, -(a.r * b.i));
    return o;
}
__device__ __forceinline__ cf csq(cf a) {
    cf o;
    o.r = fmaf(a.r, a.r, -(a.i * a.i));
    o.i = 2.0f * a.r * a.i;
    return o;
}
__device__ __forceinline__ cf cexp_dir(float alpha, float bh, float bl, float x) {
    float mag = __builtin_amdgcn_exp2f(alpha * x);
    float ph = __builtin_amdgcn_fractf(bh * x);
    ph = fmaf(bl, x, ph);
    cf o;
    o.r = mag * __builtin_amdgcn_cosf(ph);
    o.i = mag * __builtin_amdgcn_sinf(ph);
    return o;
}
__device__ __forceinline__ f4 mfma16(h8 a, h8 b, f4 c) { return __builtin_amdgcn_mfma_f32_16x16x32_f16(a, b, c, 0, 0, 0); }
__device__ __forceinline__ void wave_fence() {
    __builtin_amdgcn_fence(__ATOMIC_ACQ_REL, "wavefront");
    __builtin_amdgcn_wave_barrier();
}
__device__ __forceinline__ void lds_signal(LAS u32* w, int lane) {
    asm volatile("s_waitcnt lgkmcnt(0)" ::: "memory");
    if (lane == 0) __hip_atomic_fetch_add(w, 1u, __ATOMIC_RELAXED, __HIP_MEMORY_SCOPE_WORKGROUP);
    asm volatile("" ::: "memory");
}
template <int SLP = 0>
__device__ __forceinline__ void lds_wait(LAS u32* w, u32 want) {
    asm volatile("" ::: "memory");
    while (__builtin_amdgcn_readfirstlane(__hip_atomic_load(w, __ATOMIC_RELAXED, __HIP_MEMORY_SCOPE_WORKGROUP)) < want)
        __builtin_amdgcn_s_sleep(SLP);
    asm volatile("" ::: "memory");
}

#define TOUCH
constexpr int NT = 768;
#define UDELAY 24
#define LAZY 60
#define YDELAY 6
#define KDELAY 8
#define GDELAY 12
#define WDELAY 20
#define WPACE 1
#define HB1 0
#define KSTR 8

constexpr int UIMG_CH = (256 + L_) * 2;
constexpr int O_UIMG = 64;
constexpr int KCS = 4160;
constexpr int KIMG_CH = 4 * KCS;
constexpr int O_KIMG = O_UIMG + 3 * UIMG_CH;
constexpr int SCR = 32 * 272;
constexpr int O_SCR = O_KIMG;
constexpr int O_KTAB = O_SCR + 8 * SCR;
constexpr int KTAB_CH = 656 * 4;
constexpr int O_ACCX = O_KTAB + 3 * KTAB_CH;
constexpr int O_USTG = O_ACCX;
constexpr int URS = 336;
constexpr int O_WBUF = O_ACCX + 2 * 8192;
constexpr int O_FLAG = O_WBUF + 49152;
constexpr int O_MISC = O_FLAG + 64;
constexpr int O_JUNK = O_MISC + 128;
constexpr int O_DBG = O_JUNK + 256;
constexpr int LDS_MAIN = O_DBG;
constexpr int O_ACC2 = O_KIMG + 3 * KIMG_CH;
static_assert(O_ACC2 + 16384 <= O_KTAB, "acc2");
constexpr int YS = 1280 + 16;
constexpr int O_YS = O_KIMG;
constexpr int O_XSEL = O_YS + 8 * YS;
constexpr int O_XSB = O_XSEL + 2560;
constexpr int O_ACT = O_XSB + 1280;
constexpr int O_RED = O_ACT + 3072;
static_assert(8 * SCR >= 3 * KIMG_CH && LDS_MAIN <= 163840, "lds");
static_assert(O_KIMG % 16 == 0 && O_KTAB % 16 == 0 && O_ACCX % 16 == 0 && O_FLAG % 16 == 0 && O_WBUF % 16 == 0 && O_RED % 16 == 0, "align");
enum { FL_P0 = 0, FL_P1, FL_P2, FL_TAB, FL_KIMG, FL_UIMG, FL_ACC0, FL_ACC1, FL_ACC2, FL_ACC3, FL_STAB, FL_SIMG, FL_USTG, FL_SACC, FL_CLR, FL_CONV };

struct PosTab { u16 p[512][8]; };
constexpr int rne_lag(int j) {
    if (j < 256) return j;
    int sh = j < 512 ? 1 : (j < 1024 ? 2 : 3);
    int q = j >> sh, rem = j & ((1 << sh) - 1), half = 1 << (sh - 1);
    if (rem > half || (rem == half && (q & 1))) q++;
    return q << sh;
}
constexpr int ktab_pos_c(int j) {
    if (j < 0 || j >= 2048) return 644;
    int l = rne_lag(j);
    if (l < 256) return l;
    if (l < 512) return 256 + ((l - 256) >> 1);
    if (l < 1024) return 384 + ((l - 512) >> 2);
    return 512 + ((l - 1024) >> 3);
}
constexpr PosTab make_postab() {
    PosTab t{};
    for (int c = 0; c < 512; c++) for (int i = 0; i < 8; i++) t.p[c][i] = (u16)(4 * ktab_pos_c(2047 - 4 * c - i));
    return t;
}
__device__ const __attribute__((aligned(16))) PosTab g_postab = make_postab();

#define STAMP(i)

constexpr int XCH_STRIDE = 5376;
constexpr int CAND_STRIDE = 5120;
struct MainArgs {
    const u16 *x, *Are, *Aim, *Bre, *Bim, *Cre, *Cim, *logdt, *Dskip, *wr, *wg, *wu, *wd;
    u16* out;
    u32* epoch;
    char* xch;
    char* ubuf;
    char* ybuf;
    char* clr;
    char* keys;
    char* cand;
    char* gu;
    u64* dbg;
};
constexpr u32 SPIN_MAX = 1u << 16;

typedef float f16v __attribute__((ext_vector_type(16)));
typedef short s4 __attribute__((ext_vector_type(4)));
typedef __bf16 b8 __attribute__((ext_vector_type(8)));
typedef __amdgpu_buffer_rsrc_t rsrc_t;
__device__ __forceinline__ f2 rot(f2 b) { return f2{-b.y, b.x}; }
__device__ __forceinline__ f2 cm(f2 a, f2 b, f2 rb) { return __builtin_elementwise_fma(a.yy, rb, a.xx * b); }
__device__ __forceinline__ f2 csq2(f2 a) { return f2{fmaf(a.x, a.x, -(a.y * a.y)), 2.0f * a.x * a.y}; }
template <bool CONJ>
__device__ __forceinline__ f2 cexp2(float alpha, float bh, float bl, float x) {
    float mag = __builtin_amdgcn_exp2f(alpha * x);
    float ph = __builtin_amdgcn_fractf(bh * x);
    ph = fmaf(bl, x, ph);
    f2 cs = {__builtin_amdgcn_cosf(ph), __builtin_amdgcn_sinf(CONJ ? -ph : ph)};
    return cs * mag;
}
__device__ __forceinline__ void put(LAS char* sp, int row, f2 v) { *(LAS u32*)(sp + row * 272) = pk_f16(v.x, v.y); }

__device__ __forceinline__ h8 ld_b(LAS const char* ub, int t) { return *(LAS const h8*)(ub + 512 * t); }
__device__ __forceinline__ h8 ld_a(LAS const char* kl, LAS const char* kh, int M) {
    u32x2 lo = *(LAS const u32x2*)(kl + 512 * (7 - M));
    u32x2 hi = *(LAS const u32x2*)(kh + 512 * (7 - M));
    u32x4 a4 = {lo.x, lo.y, hi.x, hi.y};
    return __builtin_bit_cast(h8, a4);
}
#define SB() __builtin_amdgcn_sched_barrier(0)
__device__ __forceinline__ void conv_run(f4 (&acc)[8], LAS const char* ub, LAS const char* kl, LAS const char* kh, int nmu) {
    h8 A[8], B[8], Bn[3];
#pragma unroll
    for (int i = 0; i < 8; i++) { A[i] = ld_a(kl, kh, i); if (i < 5) B[7 - i] = ld_b(ub, 7 - i); if (i < 3) B[i] = ld_b(ub, i); SB(); }
#pragma nounroll
    for (int it = 0; it < nmu; it++) {
        ub -= 64; kl -= 64; kh -= 64;
#pragma unroll
        for (int M = 0; M < 8; M++) {
#pragma unroll
            for (int t = 7 - M; t >= 0; t--) acc[t + M] = mfma16(A[M], B[t], acc[t + M]);
            SB();
            A[M] = ld_a(kl, kh, M);
            if (M < 5) B[7 - M] = ld_b(ub, 7 - M);
            if (M < 3) Bn[M] = ld_b(ub, M);
            if (M >= 5) { B[7 - M] = Bn[7 - M]; asm volatile("" : "+v"(B[7 - M])); }
            SB();
        }
    }
}
template <int T0, int T1, int T2, int T3>
__device__ __forceinline__ void conv_half(f4 (&acc)[4], LAS const char* ub, LAS const char* kl, LAS const char* kh) {
    h8 A[T3 + 1], B[T3 + 1];
#pragma unroll
    for (int i = 0; i <= T3; i++) { A[i] = ld_a(kl, kh, i); B[T3 - i] = ld_b(ub, T3 - i); SB(); }
#pragma unroll
    for (int it = 0; it < 2; it++) {
        ub -= 64; kl -= 64; kh -= 64;
#pragma unroll
        for (int M = 0; M <= T3; M++) {
            acc[3] = mfma16(A[M], B[T3 - M], acc[3]);
            if (M <= T2) acc[2] = mfma16(A[M], B[T2 - M], acc[2]);
            if (M <= T1) acc[1] = mfma16(A[M], B[T1 - M], acc[1]);
            if (M <= T0) acc[0] = mfma16(A[M], B[T0 - M], acc[0]);
            SB();
            if (it == 0) { A[M] = ld_a(kl, kh, M); B[T3 - M] = ld_b(ub, T3 - M); }
            SB();
        }
    }
}

__device__ __forceinline__ void store_y_tile(rsrc_t rsy, int ch, int T, int lane, f4 a, float sc) {
    u32x2 o;
    o.x = pk_bf16(a.x * sc, a.y * sc);
    o.y = pk_bf16(a.z * sc, a.w * sc);
    const int n = lane & 15, g = lane >> 4;
    __builtin_amdgcn_raw_buffer_store_b64(o, rsy, (((n >> 1) * D_ + ch) * 64 + (n & 1) * 32 + g * 8) + T * (8 * D_ * 64), 0, 16);
}
__device__ __forceinline__ void raw_barrier() { asm volatile("s_waitcnt lgkmcnt(0)\n\ts_barrier" ::: "memory"); }
__device__ __forceinline__ u32 sort_key(float r, int row) {
    u32 u = __float_as_uint(r) >> 16;
    u = (u & 0x8000u) ? (~u & 0xffffu) : (u | 0x8000u);
    return (u << 16) | (u32)(2047 - row);
}
__device__ __forceinline__ void build_chunk(LAS char* kimg, LAS const char* tab, u32x4 pp, int c) {
    float v[7];
#pragma unroll
    for (int i = 0; i < 7; i++) v[i] = *(LAS const float*)(tab + ((i & 1) ? (pp[i >> 1] >> 16) : (pp[i >> 1] & 0xffffu)));
    u32 p01 = pk_f16(v[0], v[1]), p23 = pk_f16(v[2], v[3]), p45 = pk_f16(v[4], v[5]);
    u32 p12 = pk_f16(v[1], v[2]), p34 = pk_f16(v[3], v[4]), p56 = pk_f16(v[5], v[6]);
    *(LAS u32x2*)(kimg + 0 * KCS + 32 + 8 * c) = u32x2{p01, p23};
    *(LAS u32x2*)(kimg + 1 * KCS + 32 + 8 * c) = u32x2{p12, p34};
    *(LAS u32x2*)(kimg + 2 * KCS + 32 + 8 * c) = u32x2{p23, p45};
    *(LAS u32x2*)(kimg + 3 * KCS + 32 + 8 * c) = u32x2{p34, p56};
}
constexpr u32 CKM = 0x00070007u;
__device__ __forceinline__ u32x4 ck_expect(u32x4 d, u32 e) {
    u32 m0 = d.x & ~CKM, m1 = d.y & ~CKM, m2 = d.z & ~CKM, m3 = d.w & ~CKM;
    u32 s = (m0 + m1) + (m2 + m3) + e;
    s ^= s >> 9;
    u32x4 o;
    o.x = m0 | (s & CKM);
    o.y = m1 | (__builtin_amdgcn_alignbit(s, s, 3) & CKM);
    o.z = m2 | (__builtin_amdgcn_alignbit(s, s, 6) & CKM);
    o.w = m3 | (__builtin_amdgcn_alignbit(s, s, 9) & CKM);
    return o;
}
__device__ __forceinline__ float row16_sum(float v) {
    v = dpp_add<0xB1>(v); v = dpp_add<0x4E>(v); v = dpp_add<0x141>(v); v = dpp_add<0x140>(v);
    return v;
}
__device__ __forceinline__ u32 ln_pair(u32 x, float m, float s, float r) {
    u32 d = pk_bf16(bflo(x) - m, bfhi(x) - m);
    return pk_bf16(div_r(bflo(d), s, r), div_r(bfhi(d), s, r));
}
__device__ __forceinline__ u32x4 norm8(u32x4 x, float m, float s, float r) {
    u32x4 o;
#pragma unroll
    for (int k = 0; k < 4; k++) { u32 u = ln_pair(x[k], m, s, r); o[k] = pk_f16(bflo(u), bfhi(u)); }
    return o;
}
__device__ __forceinline__ void glds16(const void* gsrc, u32 lds_dst) {
    u32 keep;
    asm volatile("s_mov_b32 %0, m0\n\ts_mov_b32 m0, %2\n\ts_nop 0\n\tglobal_load_lds_dwordx4 %1, off\n\ts_mov_b32 m0, %0" : "=&s"(keep) : "v"(gsrc), "s"(lds_dst) : "memory");
}
__device__ __forceinline__ void glds4(const void* gsrc, u32 lds_dst) {
    u32 keep;
    asm volatile("s_mov_b32 %0, m0\n\ts_mov_b32 m0, %2\n\ts_nop 0\n\tglobal_load_lds_dword %1, off\n\ts_mov_b32 m0, %0" : "=&s"(keep) : "v"(gsrc), "s"(lds_dst) : "memory");
}
__device__ __forceinline__ b8 tr_frag(LAS const char* p, int hi_off) {
    s4 lo = __builtin_amdgcn_ds_read_tr16_b64_v4i16((LAS s4*)p);
    s4 hi = __builtin_amdgcn_ds_read_tr16_b64_v4i16((LAS s4*)(p + hi_off));
    u32x2 l2 = __builtin_bit_cast(u32x2, lo), h2_ = __builtin_bit_cast(u32x2, hi);
    return __builtin_bit_cast(b8, u32x4{l2.x, l2.y, h2_.x, h2_.y});
}
__device__ __forceinline__ f4 mfma16b(b8 a, b8 b, f4 c) { return __builtin_amdgcn_mfma_f32_16x16x32_bf16(a, b, c, 0, 0, 0); }

__global__ void __launch_bounds__(NT, 3) k_main(MainArgs a) {
    extern __shared__ __attribute__((aligned(16))) char smem_[];
    LAS char* smem = (LAS char*)smem_;
    const int tid = threadIdx.x, lane = tid & 63;
    u32 warm;
    {
        u64 pc; u32 csz;
        asm volatile("s_getpc_b64 %0\n.Lk_begin:\n\ts_mov_b32 %1, .Lk_end-.Lk_begin" : "=s"(pc), "=s"(csz));
        if (csz > 49152u || csz < 4096u) csz = 128u;
        warm = *(const GAS u32*)((pc & ~127ull) + min((u32)(tid * 64), csz - 128u));
    }
    const int wave = __builtin_amdgcn_readfirstlane(tid >> 6);
    const int b = blockIdx.x;
    const int pairi = (b & 7) * 16 + (b >> 4), side = (b >> 3) & 1;
    const int cbase = 5 * pairi + 2 * side;
    const int ch0 = side ? cbase + 1 : cbase, ch1 = ch0 + 1, chs = 5 * pairi + 2;
    const int chunk = (b & 7) * 32 + (b >> 3);
    LAS u32* flags = (LAS u32*)(smem + O_FLAG);
    STAMP(0);
    u32 ep;
#define LOAD_EPOCH() asm volatile("s_load_dword %0, %1, 0x0\n\ts_waitcnt lgkmcnt(0)" : "=s"(ep) : "s"(a.epoch) : "memory")
    const rsrc_t rsy = __builtin_amdgcn_make_buffer_rsrc((void*)a.ybuf, 0, 64 * D_ * 64, 0x00020000);
    const int jl = b >> 3;
    const bool is_f1 = jl < 12, is_f2 = jl >= 12 && jl < 17;
    const int f1u = (b & 7) * 12 + jl;
    const int f2u = (b & 7) * 5 + (jl - 12);
    const int n16 = lane & 15, g4 = lane >> 4;
    const int row = chunk * 8 + (wave & 7);
    u32 xp[5], sk[5], wp[5];
    u32 dp_[5];
#define LN2_LOAD() do {                                                                                                       \
        const GAS u32* xr_ = (const GAS u32*)(a.x + (size_t)row * D_);                                                        \
        _Pragma("unroll") for (int i = 0; i < 5; i++) {                                                                       \
            xp[i] = xr_[lane + 64 * i]; dp_[i] = ((const GAS u32*)a.Dskip)[lane + 64 * i]; wp[i] = ((const GAS u32*)a.wr)[lane + 64 * i]; } \
        SB();                                                                                                                 \
    } while (0)
#define LN2_CALC() do {                                                                                                       \
        const u32 stp_ = *(LAS const u32*)(smem + O_MISC + 4 * wave);                                                         \
        const float m1_ = bflo(stp_), s1_ = bfhi(stp_), r1_ = __builtin_amdgcn_rcpf(s1_);                                     \
        _Pragma("unroll") for (int i = 0; i < 5; i++) {                                                                       \
            u32 u_ = ln_pair(xp[i], m1_, s1_, r1_);                                                                           \
            sk[i] = pk_bf16(bflo(u_) * bflo(dp_[i]), bfhi(u_) * bfhi(dp_[i])); }                                              \
        asm volatile("" : "+v"(wp[0]), "+v"(wp[1]), "+v"(wp[2]), "+v"(wp[3]), "+v"(wp[4]));                                    \
    } while (0)

    if (wave < 8) {
        const int slot_g = wave < 3 ? 0 : (wave < 6 ? 1 : 2);
        const int ch_g = slot_g == 0 ? ch0 : (slot_g == 1 ? ch1 : chs);
        int mode; bool act = true;
        if (slot_g < 2) mode = (wave - 3 * slot_g) * 64 + lane;
        else { mode = 96 * side + (wave - 6) * 64 + lane; act = (wave == 6) || (lane < 32); if (!act) mode = 0; }
        const int gi = ch_g * N_ + mode;
        u16 pdt = a.logdt[ch_g];
        u16 pAre = a.Are[gi], pAim = a.Aim[gi], pBre = a.Bre[gi], pBim = a.Bim[gi], pCre = a.Cre[gi], pCim = a.Cim[gi];
        u32x4 pp = *(const GAS u32x4*)(&g_postab.p[tid][0]);
        if (tid < 16) flags[tid] = 0;
        if (tid >= 64 && tid < 160) { int t = tid - 64; *(LAS u32x4*)(smem + O_UIMG + (t >> 5) * UIMG_CH + (t & 31) * 16) = u32x4{0, 0, 0, 0}; }
        LOAD_EPOCH();
        raw_barrier();
        if (wave >= 6) __builtin_amdgcn_s_setprio(1);
        float Are = bf2f(pAre), Aim = bf2f(pAim), Bre = bf2f(pBre), Bim = bf2f(pBim), Cre = bf2f(pCre), Cim = bf2f(pCim);
        float dt = rbf(expf(bf2f(pdt)));
        float are = dt * Are, aim = dt * Aim, br = dt * Bre, bi = dt * Bim;
        f2 wc = {fmaf(Cre, br, -(Cim * bi)), -fmaf(Cre, bi, Cim * br)};
        if (!act) wc = f2{0.f, 0.f};
        asm volatile("" :: "v"(warm), "v"(pp));
        STAMP(1);
        {
            const u32x4 ones = {~0u, ~0u, ~0u, ~0u};
            __builtin_amdgcn_raw_buffer_store_b128(ones, rsy, ((tid >> 3) * D_ + ch0) * 64 + (tid & 7) * 16, 0, 16);
            if (wave < 2) {
                const int tq = tid >> 5;
                const int T = side ? (tq == 0 ? 1 : (tq == 1 ? 6 : (tq == 2 ? 2 : 5))) : (tq == 0 ? 0 : (tq == 1 ? 7 : (tq == 2 ? 3 : 4)));
                __builtin_amdgcn_raw_buffer_store_b128(ones, rsy, ((8 * T + ((tid >> 2) & 7)) * D_ + chs) * 64 + (tid & 3) * 16, 0, 16);
            }
        }
        if (wave == 7) {
            const char* p = lane < 62 ? (const char*)a.epoch + (size_t)lane * (128 << 10) : (const char*)(a.out + (size_t)(chunk * 8 + (lane - 62) * 7) * D_);
            glds4(p, (u32)O_JUNK);
        }
        float alpha = are * 1.44269504088896341f;
        const float CH = 0.15915494f, CL = 6.4206382e-09f;
        float p_ = aim * CH;
        float pe = fmaf(aim, CL, fmaf(aim, CH, -p_));
        float pf = p_ - __builtin_rintf(p_);
        float bh = __uint_as_float(__float_as_uint(pf + pe) & 0xfffff800u);
        float bl = (pf - bh) + pe;
        float mw = fabsf(wc.x) + fabsf(wc.y);
        float v0r = 16384.f;
        if (__builtin_expect(__any(alpha > 0.f), 0)) {
            float ap = fmaxf(alpha, 0.f);
            float g1 = __builtin_amdgcn_exp2f(ap * 1024.f);
            wc = (wc * g1) * g1;
            mw = (fabsf(wc.x) + fabsf(wc.y)) * __builtin_amdgcn_exp2f(ap * 248.f);
            v0r = __builtin_amdgcn_exp2f(fmaf(ap, -2048.f, 14.f));
        }
        u32 mwu = wave_umax(__float_as_uint(mw));
        int e = (int)(mwu >> 23) - 126;
        if (e < -100) e = -100;
        if (e > 127) e = 127;
        LAS char* scr = smem + O_SCR + wave * SCR;
        LAS char* sp = scr + lane * 4;
        LAS const char* fp = scr + (lane & 31) * 272 + (lane >> 5) * 16;
        {
            f2 E8 = cexp2<true>(alpha, bh, bl, 8.f);
            f2 E16 = csq2(E8), E32 = csq2(E16);
            f2 r8 = rot(E8), r32 = rot(E32);
            f2 Y0 = wc * __builtin_amdgcn_ldexpf(1.0f, 14 - e);
            f2 Y2 = cm(Y0, E16, rot(E16));
            f2 Y1 = cm(Y0, E8, r8), Y3 = cm(Y2, E8, r8);
#pragma unroll
            for (int q = 0; q < 32; q += 4) {
                put(sp, q, Y0); put(sp, q + 1, Y1); put(sp, q + 2, Y2); put(sp, q + 3, Y3);
                if (q < 28) {
                    f2 t0 = Y0.xx * E32, t1 = Y1.xx * E32, t2 = Y2.xx * E32, t3 = Y3.xx * E32;
                    asm volatile("" : "+v"(t0), "+v"(t1), "+v"(t2), "+v"(t3));
                    Y0 = __builtin_elementwise_fma(Y0.yy, r32, t0); Y1 = __builtin_elementwise_fma(Y1.yy, r32, t1);
                    Y2 = __builtin_elementwise_fma(Y2.yy, r32, t2); Y3 = __builtin_elementwise_fma(Y3.yy, r32, t3);
                }
            }
        }
        wave_fence();
        h8 By[8];
#pragma unroll
        for (int k = 0; k < 8; k++) By[k] = *(LAS const h8*)(fp + 32 * k);
        wave_fence();
        {
            f2 E1 = cexp2<false>(alpha, bh, bl, 1.f), E256 = cexp2<false>(alpha, bh, bl, 256.f), E1024 = cexp2<false>(alpha, bh, bl, 1024.f);
            f2 E2 = csq2(E1), E4 = csq2(E2), E512 = csq2(E256), E2048 = csq2(E1024);
            f2 r1 = rot(E1), r256 = rot(E256);
            f2 E768 = cm(E512, E256, r256);
            f2 V2 = E2 * v0r, V4 = E4 * v0r;
            f2 rV4 = rot(V4);
            f2 V6 = cm(E2, V4, rV4);
            put(sp, 0, f2{v0r, 0.f}); put(sp, 1, E1 * v0r); put(sp, 2, V2); put(sp, 3, cm(V2, E1, r1));
            put(sp, 4, V4); put(sp, 5, cm(V4, E1, r1)); put(sp, 6, V6); put(sp, 7, cm(V6, E1, r1));
                put(sp, 8, E256 * v0r); put(sp, 9, cm(V2, E256, r256)); put(sp, 10, cm(V4, E256, r256)); put(sp, 11, cm(V6, E256, r256));
            put(sp, 12, E512 * v0r); put(sp, 13, cm(E512, V4, rV4)); put(sp, 14, E768 * v0r); put(sp, 15, cm(E768, V4, rV4));
            f2 H0 = E1024 * v0r;
            f2 H1 = cm(H0, E256, r256), H2 = cm(H1, E256, r256), H3 = cm(H2, E256, r256);
            put(sp, 16, H0); put(sp, 17, H1); put(sp, 18, H2); put(sp, 19, H3); put(sp, 20, E2048 * v0r);
        }
        wave_fence();
        f16v dd = {0, 0, 0, 0, 0, 0, 0, 0, 0, 0, 0, 0, 0, 0, 0, 0};
#pragma unroll
        for (int k = 0; k < 8; k++) dd = __builtin_amdgcn_mfma_f32_32x32x16_f16(*(LAS const h8*)(fp + 32 * k), By[k], dd, 0, 0, 0);
        wave_fence();
        {
            const float ts = __builtin_amdgcn_ldexpf(1.0f, e - 28);
            const int q = lane & 31, h = lane >> 5;
            *(LAS f4*)(scr + (8 * q + 4 * h) * 4) = f4{dd[0], dd[1], dd[2], dd[3]} * ts;
            if (h == 0) {
                *(LAS f4*)(scr + (256 + 4 * q) * 4) = f4{dd[4], dd[5], dd[6], dd[7]} * ts;
                *(LAS float*)(scr + (512 + q) * 4) = dd[8] * ts;
                *(LAS float*)(scr + (544 + q) * 4) = dd[9] * ts;
                *(LAS float*)(scr + (576 + q) * 4) = dd[10] * ts;
                *(LAS float*)(scr + (608 + q) * 4) = dd[11] * ts;
            } else {
                *(LAS f2*)(scr + (384 + 2 * q) * 4) = f2{dd[4], dd[5]} * ts;
                *(LAS f2*)(scr + (448 + 2 * q) * 4) = f2{dd[6], dd[7]} * ts;
                if (q == 0) {
                    *(LAS f4*)(scr + 640 * 4) = f4{dd[8] * ts, 0.f, 0.f, 0.f};
                    *(LAS u32x2*)(scr + 644 * 4) = u32x2{0u, (u32)e};
                }
            }
        }
        STAMP(2);
        asm volatile("s_waitcnt vmcnt(0)" ::: "memory");
        lds_signal(flags + FL_P0 + slot_g, lane);
        const u32 tag = ep * 0x9E3779B1u + 0x7F4A7C15u;
        lds_wait(flags + FL_P0 + slot_g, slot_g < 2 ? 3 : 2);
        STAMP(3);
        {
            LAS const char* p0 = smem + O_SCR + (3 * slot_g) * SCR;
            LAS char* tab = smem + O_KTAB + slot_g * KTAB_CH;
            int em = max(*(LAS const int*)(p0 + 645 * 4), *(LAS const int*)(p0 + SCR + 645 * 4));
            if (slot_g < 2) {
                const int idx = tid - slot_g * 192;
                em = max(em, *(LAS const int*)(p0 + 2 * SCR + 645 * 4));
                const int ic = idx < 161 ? idx : 161;
                f4 sm = *(LAS const f4*)(p0 + ic * 16) + *(LAS const f4*)(p0 + SCR + ic * 16) + *(LAS const f4*)(p0 + 2 * SCR + ic * 16);
                sm *= __builtin_amdgcn_ldexpf(1.0f, 7 - em);
                if (idx >= 161) sm = f4{0.f, __builtin_amdgcn_ldexpf(1.0f, em - 7), 0.f, 0.f};
                *(LAS f4*)(tab + ic * 16) = sm;
            } else {
                auto rs = __builtin_amdgcn_make_buffer_rsrc((void*)(a.xch + (size_t)b * XCH_STRIDE), 0, XCH_STRIDE, 0x00020000);
                const int i0 = tid - 384, i1 = i0 < 33 ? i0 + 128 : 160;
                f4 s0 = *(LAS const f4*)(p0 + i0 * 16) + *(LAS const f4*)(p0 + SCR + i0 * 16);
                f4 s1 = *(LAS const f4*)(p0 + i1 * 16) + *(LAS const f4*)(p0 + SCR + i1 * 16);
                if (i1 == 160) s1.y = __int_as_float(em);
                *(LAS f4*)(tab + i0 * 16) = s0;
                if (wave == 6) { *(LAS f4*)(tab + i1 * 16) = s1; if (lane == 0) *(LAS int*)(tab + 648 * 4) = em; }
                lds_signal(flags + FL_TAB, lane);
                __builtin_amdgcn_raw_buffer_store_b128(u32x4{__float_as_uint(s0.x), tag, __float_as_uint(s0.y), tag}, rs, i0 * 16, 0, 16);
                __builtin_amdgcn_raw_buffer_store_b128(u32x4{__float_as_uint(s0.z), tag, __float_as_uint(s0.w), tag}, rs, (162 + i0) * 16, 0, 16);
                if (wave == 6) {
                    __builtin_amdgcn_raw_buffer_store_b128(u32x4{__float_as_uint(s1.x), tag, __float_as_uint(s1.y), tag}, rs, i1 * 16, 0, 16);
                    __builtin_amdgcn_raw_buffer_store_b128(u32x4{__float_as_uint(s1.z), tag, __float_as_uint(s1.w), tag}, rs, (162 + i1) * 16, 0, 16);
                }
            }
            if (slot_g < 2) lds_signal(flags + FL_TAB, lane);
        }
        __builtin_amdgcn_s_setprio(0);
        lds_wait(flags + FL_TAB, 8);
        STAMP(4);
        if (tid < 8) __hip_atomic_store((GAS u64*)(a.clr + tid * 2048 + b * 8), ((u64)(ep * 0x2545F491u + 0x9E3779B9u) << 32) | (ep * 0xC2B2AE35u + 0x27D4EB2Fu), RLX_AGENT);
        build_chunk(smem + O_KIMG, smem + O_KTAB, pp, tid);
        if (wave >= 4 || !HB1) build_chunk(smem + O_KIMG + KIMG_CH, smem + O_KTAB + KTAB_CH, pp, tid);
        if (tid < 24) *(LAS u32x4*)(smem + O_KIMG + (tid >> 1) * KCS + 4128 + (tid & 1) * 16) = u32x4{0, 0, 0, 0};
        lds_signal(flags + FL_KIMG, lane);
        STAMP(5);

        LN2_LOAD();
        const int sK = (3 - n16) & 3;
        const int uoff = 2 * (256 + 16 * n16 - 16 + 8 * g4);
        const int koff = sK * KCS + 2 * (2047 - n16 + 8 * g4 - sK) - 512 * 7;
        if (wave < 4) {
            const int slot = wave >> 1, odd = wave & 1;
            LAS const char* ub = smem + O_UIMG + slot * UIMG_CH + uoff - 256 * odd;
            LAS const char* kl = smem + O_KIMG + slot * KIMG_CH + koff - 256 * odd;
            LAS const char* kh = kl + 8;
            asm volatile("" : "+v"(kh));
            f4 acc[8];
#pragma unroll
            for (int t = 0; t < 8; t++) acc[t] = f4{0, 0, 0, 0};
            lds_wait<1>(flags + FL_KIMG, 12);
            lds_wait<1>(flags + FL_UIMG, 4);
            __builtin_amdgcn_s_setprio(2);
            STAMP(6);
            conv_run(acc, ub, kl, kh, 4);
            STAMP(7);
            LAS char* axw = smem + O_ACCX + wave * 4096 + lane * 16;
            LAS const char* axr = smem + O_ACCX + (wave ^ 1) * 4096 + lane * 16;
            f4 keep[4];
#pragma unroll
            for (int t = 0; t < 4; t++) {
                f4 give;
#pragma unroll
                for (int k = 0; k < 4; k++) { give[k] = odd ? acc[t][k] : acc[t + 4][k]; keep[t][k] = odd ? acc[t + 4][k] : acc[t][k]; }
                *(LAS f4*)(axw + 1024 * t) = give;
            }
            lds_signal(flags + FL_ACC0 + wave, lane);
            const float sc = *(LAS const float*)(smem + O_KTAB + slot * KTAB_CH + 645 * 4);
            lds_wait(flags + FL_ACC0 + (wave ^ 1), 1);
#pragma unroll
            for (int t = 0; t < 4; t++) {
                f4 o = keep[t] + *(LAS const f4*)(axr + 1024 * t);
                store_y_tile(rsy, slot ? ch1 : ch0, t + 4 * odd, lane, o, sc);
            }
            {
                LAS const char* a2 = smem + O_ACC2 + lane * 16 + wave * 4096;
                lds_wait(flags + FL_SACC, 4);
                const float sc2 = *(LAS const float*)(smem + O_KTAB + 2 * KTAB_CH + 645 * 4);
                f4 o = (*(LAS const f4*)(a2) + *(LAS const f4*)(a2 + 1024)) + (*(LAS const f4*)(a2 + 2048) + *(LAS const f4*)(a2 + 3072));
                const int T = side ? (wave == 0 ? 1 : (wave == 1 ? 2 : (wave == 2 ? 5 : 6))) : (wave == 0 ? 0 : (wave == 1 ? 3 : (wave == 2 ? 4 : 7)));
                store_y_tile(rsy, chs, T, lane, o, sc2);
            }
            __builtin_amdgcn_s_setprio(0);
            lds_signal(flags + FL_CONV, lane);
            STAMP(10);
            LN2_CALC();
            STAMP(8);
        } else {
            const int w4 = wave - 4;
            LAS const char* ub = smem + O_UIMG + 2 * UIMG_CH + uoff - 128 * w4;
            LAS const char* kl = smem + O_KIMG + 2 * KIMG_CH + koff - 128 * w4;
            LAS const char* kh = kl + 8;
            asm volatile("" : "+v"(kh));
            f4 acc[4];
#pragma unroll
            for (int t = 0; t < 4; t++) acc[t] = f4{0, 0, 0, 0};
            lds_wait<1>(flags + FL_SIMG, 4);
            lds_wait<1>(flags + FL_KIMG, 12);
            __builtin_amdgcn_s_setprio(3);
            STAMP(7);
            if (side) conv_half<1, 2, 5, 6>(acc, ub, kl, kh); else conv_half<0, 3, 4, 7>(acc, ub, kl, kh);
            STAMP(6);
            LAS char* a2 = smem + O_ACC2 + lane * 16;
#pragma unroll
            for (int t = 0; t < 4; t++) *(LAS f4*)(a2 + (t * 4 + w4) * 1024) = acc[t];
            lds_signal(flags + FL_SACC, lane);
            __builtin_amdgcn_s_setprio(0);
            STAMP(10);
            LN2_CALC();
        }
    } else {
        const int hw = wave - 8, ht = tid - 512;
        const int blk = chunk >> 2, cq = chunk & 3;
        const GAS u32x4* xb = (const GAS u32x4*)a.x + (size_t)(blk * 32 + 8 * hw) * 80;
        u32x4 xq[2][5];
#pragma unroll
        for (int i = 0; i < 5; i++) {
            int c = 20 * cq + n16 + 16 * i; if (c >= 80) c -= 80;
            xq[0][i] = xb[g4 * 80 + c]; xq[1][i] = xb[(4 + g4) * 80 + c];
        }
        const int er = (lane >> 2) & 7;
        u32x4 xe = xb[er * 80 + 20 * cq + 16 + (lane & 3)];
        LOAD_EPOCH();
        __builtin_amdgcn_s_barrier();
        __builtin_amdgcn_s_setprio(2);
        float mB[2], sB[2], rB[2];
        {
            float s1[2], s2[2];
#pragma unroll
            for (int g2 = 0; g2 < 2; g2++) {
                float a1 = 0.f, a2 = 0.f, b1 = 0.f, b2v = 0.f;
#pragma unroll
                for (int i = 0; i < 5; i++) {
                    a1 = dot2bf(xq[g2][i].x, 0x3f803f80u, a1); a2 = dot2bf(xq[g2][i].x, xq[g2][i].x, a2);
                    b1 = dot2bf(xq[g2][i].y, 0x3f803f80u, b1); b2v = dot2bf(xq[g2][i].y, xq[g2][i].y, b2v);
                    a1 = dot2bf(xq[g2][i].z, 0x3f803f80u, a1); a2 = dot2bf(xq[g2][i].z, xq[g2][i].z, a2);
                    b1 = dot2bf(xq[g2][i].w, 0x3f803f80u, b1); b2v = dot2bf(xq[g2][i].w, xq[g2][i].w, b2v);
                }
                s1[g2] = row16_sum(a1 + b1); s2[g2] = row16_sum(a2 + b2v);
            }
            float mean[2], var[2];
#pragma unroll
            for (int g2 = 0; g2 < 2; g2++) { mean[g2] = s1[g2] * (1.0f / D_); var[g2] = fmaf(-mean[g2], mean[g2], s2[g2] * (1.0f / D_)); }
            if (__builtin_expect(__any(mean[0] * mean[0] > 16.f * var[0] || mean[1] * mean[1] > 16.f * var[1]), 0)) {
#pragma unroll
                for (int g2 = 0; g2 < 2; g2++) {
                    float ss = 0.f;
#pragma unroll
                    for (int i = 0; i < 5; i++)
#pragma unroll
                        for (int k = 0; k < 4; k++) {
                            float d0 = bflo(xq[g2][i][k]) - mean[g2], d1 = bfhi(xq[g2][i][k]) - mean[g2];
                            ss = fmaf(d0, d0, ss); ss = fmaf(d1, d1, ss);
                        }
                    var[g2] = row16_sum(ss) * (1.0f / D_);
                }
            }
#pragma unroll
            for (int g2 = 0; g2 < 2; g2++) {
                mB[g2] = rbf(mean[g2]);
                sB[g2] = rbf(__builtin_sqrtf(rbf(rbf(var[g2]) + 1e-5f)));
                rB[g2] = __builtin_amdgcn_rcpf(sB[g2]);
            }
        }
        STAMP(1);
        LAS char* stg = smem + O_USTG;
        {
            const u32 ms0 = (__float_as_uint(mB[0]) >> 16) | __float_as_uint(sB[0]), ms1 = (__float_as_uint(mB[1]) >> 16) | __float_as_uint(sB[1]);
            if (hw == cq && n16 == 0) { *(LAS u32*)(smem + O_MISC + 4 * g4) = ms0; *(LAS u32*)(smem + O_MISC + 16 + 4 * g4) = ms1; }
            const int src = (er & 3) * 64;
            u32 e0 = (u32)__builtin_amdgcn_ds_bpermute(src, (int)ms0), e1 = (u32)__builtin_amdgcn_ds_bpermute(src, (int)ms1);
            *(LAS u32x4*)(stg + (8 * hw + g4) * URS + n16 * 16) = norm8(xq[0][0], mB[0], sB[0], rB[0]);
            *(LAS u32x4*)(stg + (8 * hw + 4 + g4) * URS + n16 * 16) = norm8(xq[1][0], mB[1], sB[1], rB[1]);
            const u32 em = (er & 4) ? e1 : e0;
            const float se = bfhi(em);
            u32x4 ne = norm8(xe, bflo(em), se, __builtin_amdgcn_rcpf(se));
            if (lane < 32) *(LAS u32x4*)(stg + (8 * hw + er) * URS + (16 + (lane & 3)) * 16) = ne;
        }
        asm volatile("" :: "v"(warm));
        const u32 cke = ep * 0x85EBCA77u + 0x165667B1u;
        lds_signal(flags + FL_USTG, lane);
        lds_wait(flags + FL_USTG, 4);
        STAMP(2);
        auto rsu = __builtin_amdgcn_make_buffer_rsrc((void*)a.ubuf, 0, 64 * D_ * 64, 0x00020000);
        {
            const int gbase = (blk * D_ + 160 * cq + n16) * 64 + g4 * 16;
            LAS const char* tp = stg + (8 * g4 + (n16 >> 2)) * URS + (n16 & 3) * 8;
#pragma unroll
            for (int ps = 0; ps < 3; ps++) {
                const int cb = hw + 4 * ps;
                if (cb < 10)
                    __builtin_amdgcn_raw_buffer_store_b128(ck_expect(__builtin_bit_cast(u32x4, tr_frag(tp + cb * 32, 4 * URS)), cke), rsu, gbase + cb * 1024, 0, 16);
            }
        }
        __builtin_amdgcn_s_setprio(0);
        STAMP(3);
        u32x4 ppa = *(const GAS u32x4*)(&g_postab.p[ht][0]), ppb = *(const GAS u32x4*)(&g_postab.p[ht + 256][0]);
        __builtin_amdgcn_s_sleep(UDELAY);
        STAMP(4);
        {
            int goff[3], loff[3];
#pragma unroll
            for (int ps = 0; ps < 3; ps++) {
                const int un = ht + 256 * ps;
                const int rb = un / 12, w12 = un - rb * 12;
                goff[ps] = (rb * D_ + cbase) * 64 + w12 * 16;
                const int c = w12 >> 2;
                const int slot = side ? (c == 0 ? 2 : c - 1) : c;
                loff[ps] = O_UIMG + slot * UIMG_CH + 512 + rb * 64 + (w12 & 3) * 16;
            }
            u32x4 d[3];
            u32 need = 7;
            for (u32 spins = 0; spins < SPIN_MAX; spins++) {
#pragma unroll
                for (int ps = 0; ps < 3; ps++) if (need & (1u << ps)) d[ps] = __builtin_amdgcn_raw_buffer_load_b128(rsu, goff[ps], 0, 16);
                u32 bad = 0;
#pragma unroll
                for (int ps = 0; ps < 3; ps++) {
                    u32x4 ex = ck_expect(d[ps], cke);
                    if (!(ex.x == d[ps].x && ex.y == d[ps].y && ex.z == d[ps].z && ex.w == d[ps].w)) bad |= 1u << ps;
                }
                bad |= (u32)__builtin_amdgcn_update_dpp(0, (int)bad, 0xB1, 0xf, 0xf, false);
                bad |= (u32)__builtin_amdgcn_update_dpp(0, (int)bad, 0x4E, 0xf, 0xf, false);
                need = bad;
                if (!__any(need != 0)) break;
                __builtin_amdgcn_s_sleep(4);
            }
#pragma unroll
            for (int ps = 0; ps < 3; ps++)
                *(LAS u32x4*)(smem + loff[ps]) = u32x4{d[ps].x & ~CKM, d[ps].y & ~CKM, d[ps].z & ~CKM, d[ps].w & ~CKM};
        }
        lds_signal(flags + FL_UIMG, lane);
        STAMP(5);
        lds_signal(flags + FL_KIMG, lane);
        {
            __builtin_amdgcn_s_setprio(3);
            LAS char* tab = smem + O_KTAB + 2 * KTAB_CH;
            const u32 tag = ep * 0x9E3779B1u + 0x7F4A7C15u;
            if (ht < 192) {
                auto rs = __builtin_amdgcn_make_buffer_rsrc((void*)(a.xch + (size_t)(b ^ 8) * XCH_STRIDE), 0, XCH_STRIDE, 0x00020000);
                const int hi = ht < 161 ? ht : 160;
                u32x4 v0, v1, v2;
                bool need = true;
                for (u32 spins = 0; spins < SPIN_MAX; spins++) {
                    if (need) {
                        v0 = __builtin_amdgcn_raw_buffer_load_b128(rs, hi * 16, 0, 16);
                        v1 = __builtin_amdgcn_raw_buffer_load_b128(rs, (162 + hi) * 16, 0, 16);
                        v2 = __builtin_amdgcn_raw_buffer_load_b128(rs, 160 * 16, 0, 16);
                        need = !(v0.y == tag && v0.w == tag && v1.y == tag && v1.w == tag && v2.y == tag && v2.w == tag);
                    }
                    if (!__any(need)) break;
                    __builtin_amdgcn_s_sleep(1);
                }
                lds_wait<1>(flags + FL_TAB, 8);
                int em = max(*(LAS const int*)(tab + 648 * 4), (int)v2.z);
                f4 loc = *(LAS const f4*)(tab + hi * 16);
                f4 rem = {__uint_as_float(v0.x), __uint_as_float(v0.z), __uint_as_float(v1.x), __uint_as_float(v1.z)};
                if (ht < 161) *(LAS f4*)(tab + ht * 16) = (loc + rem) * __builtin_amdgcn_ldexpf(1.0f, 7 - em);
                else if (ht == 161) *(LAS f2*)(tab + 644 * 4) = f2{0.f, __builtin_amdgcn_ldexpf(1.0f, em - 7)};
                lds_signal(flags + FL_STAB, lane);
            }
            STAMP(6);
            lds_wait<1>(flags + FL_STAB, 3);
            build_chunk(smem + O_KIMG + 2 * KIMG_CH, tab, ppa, ht);
            build_chunk(smem + O_KIMG + 2 * KIMG_CH, tab, ppb, ht + 256);
            lds_signal(flags + FL_SIMG, lane);
            __builtin_amdgcn_s_setprio(0);
            STAMP(7);
        }
        if (hw == 0) {
            const u64 want = ((u64)(ep * 0x2545F491u + 0x9E3779B9u) << 32) | (ep * 0xC2B2AE35u + 0x27D4EB2Fu);
            auto rk = __builtin_amdgcn_make_buffer_rsrc((void*)(a.clr + ((b >> 3) & 7) * 2048), 0, 2048, 0x00020000);
            const u32 wl = (u32)want, wh = (u32)(want >> 32);
            bool nd = true;
            for (u32 spins = 0; spins < SPIN_MAX; spins++) {
                if (nd) {
                    u32x4 k0 = __builtin_amdgcn_raw_buffer_load_b128(rk, lane * 16, 0, 16), k1 = __builtin_amdgcn_raw_buffer_load_b128(rk, 1024 + lane * 16, 0, 16);
                    nd = !(k0.x == wl && k0.y == wh && k0.z == wl && k0.w == wh && k1.x == wl && k1.y == wh && k1.z == wl && k1.w == wh);
                }
                if (!__any(nd)) break;
                __builtin_amdgcn_s_sleep(8);
            }
            lds_signal(flags + FL_CLR, lane);
            STAMP(10);
        }
        if (hw > 0) {
#pragma unroll
          for (int ps = 0; ps < 2; ps++) {
            const int xk = b & 7, li = min(((hw - 1) * 2 + ps) * 40 + (lane < 40 ? lane : 0), 235);
            const char* p;
            if (li < 120) { const int r = li / 6; p = (const char*)(xk < 4 ? a.wg : a.wu) + (size_t)(20 * jl + r) * (F_ * 2) + 768 * (xk & 3) + (li - 6 * r) * 128; }
            else if (li < 216) { const int l2 = li - 120; p = (const char*)a.wd + (size_t)(48 * jl + (l2 >> 1)) * (D_ * 2) + ((160 * xk) & ~127) + (l2 & 1) * 128; }
            else if (li < 226) p = (const char*)a.Dskip + (li - 216) * 128;
            else p = (const char*)a.wr + (li - 226) * 128;
            if (lane < 40) glds4(p, (u32)O_JUNK);
          }
          __builtin_amdgcn_s_sleep(WDELAY);
        }
        if ((is_f1 || is_f2) && hw > 0) {
            const int wn = is_f1 ? 40 : 48;
            const int wjs = is_f1 ? 16 * F_ * 2 : 32 * D_ * 2;
            const char* wsrc = is_f1 ? (const char*)((f1u >= 48 ? a.wu : a.wg) + (f1u >= 48 ? f1u - 48 : f1u) * 32 + (lane >> 2) * F_ + (lane & 3) * 8)
                                     : (const char*)(a.wd + f2u * 16 + (lane >> 1) * D_ + (lane & 1) * 8);
            wsrc += (hw - 1) * wjs;
            for (int i = hw - 1; i < wn; i += 3) { glds16(wsrc, (u32)(O_WBUF + i * 1024)); wsrc += 3 * wjs; __builtin_amdgcn_s_sleep(WPACE); }
            asm volatile("s_waitcnt vmcnt(0)" ::: "memory");
        }
    }
    const u32 tag2 = ep * 0x9E3779B1u + 0x3C6EF372u;
    if (wave >= 4 && wave < 9) {
        const int pt = tid - 256;
        lds_wait<6>(flags + FL_CLR, 1);
        lds_wait<1>(flags + FL_CONV, 4);
        __builtin_amdgcn_s_sleep(YDELAY);
        STAMP(8);
        const int yo = ((chunk >> 2) * D_ + 2 * pt) * 64 + (chunk & 3) * 16;
        u32x4 y0, y1;
        bool need = true;
        for (u32 spins = 0; spins < SPIN_MAX; spins++) {
            if (need) {
                y0 = __builtin_amdgcn_raw_buffer_load_b128(rsy, yo, 0, 16);
                y1 = __builtin_amdgcn_raw_buffer_load_b128(rsy, yo + 64, 0, 16);
                bool bad = false;
#pragma unroll
                for (int k = 0; k < 4; k++) {
                    bad |= y0[k] >= 0xffff0000u || (u16)y0[k] == 0xffffu;
                    bad |= y1[k] >= 0xffff0000u || (u16)y1[k] == 0xffffu;
                }
                need = bad;
            }
            if (!__any(need)) break;
            __builtin_amdgcn_s_sleep(2);
        }
        LAS char* ys = smem + O_YS;
#pragma unroll
        for (int k = 0; k < 4; k++) {
            *(LAS u32*)(ys + (2 * k) * YS + 4 * pt) = __builtin_amdgcn_perm(y1[k], y0[k], 0x05040100u);
            *(LAS u32*)(ys + (2 * k + 1) * YS + 4 * pt) = __builtin_amdgcn_perm(y1[k], y0[k], 0x07060302u);
        }
    }
    raw_barrier();
    STAMP(9);
    if (b == 0 && tid == 704) __hip_atomic_store((GAS u32*)a.epoch, ep + 1u, RLX_AGENT);
    u32 hp[5], x2p[5], key = 0;
    if (wave < 8) {
        float s1 = 0.f, s2 = 0.f;
#pragma unroll
        for (int i = 0; i < 5; i++) {
            u32 yp = *(LAS const u32*)(smem + O_YS + wave * YS + 4 * (lane + 64 * i));
            u32 aa = pk_bf16(bflo(yp) + bflo(sk[i]), bfhi(yp) + bfhi(sk[i]));
            hp[i] = pk_bf16(bflo(xp[i]) + bflo(aa), bfhi(xp[i]) + bfhi(aa));
            s1 = dot2bf(hp[i], 0x3f803f80u, s1); s2 = dot2bf(hp[i], hp[i], s2);
        }
        const float mean = wave_sum(s1) * (1.0f / D_);
        float var = fmaf(-mean, mean, wave_sum(s2) * (1.0f / D_));
        if (__builtin_expect(mean * mean > 16.f * var, 0)) {
            float ss = 0.f;
#pragma unroll
            for (int i = 0; i < 5; i++) { float d0 = bflo(hp[i]) - mean, d1 = bfhi(hp[i]) - mean; ss = fmaf(d0, d0, ss); ss = fmaf(d1, d1, ss); }
            var = wave_sum(ss) * (1.0f / D_);
        }
        const float m2 = rbf(mean), sd2 = rbf(__builtin_sqrtf(rbf(rbf(var) + 1e-5f))), r2 = __builtin_amdgcn_rcpf(sd2);
        float racc = 0.f;
#pragma unroll
        for (int i = 0; i < 5; i++) { x2p[i] = ln_pair(hp[i], m2, sd2, r2); racc = dot2bf(x2p[i], wp[i], racc); }
        key = sort_key(rbf(wave_sum(racc)), row);
        if (lane == 0) *(LAS u32*)(smem + O_MISC + 32 + 4 * wave) = key;
    }
    raw_barrier();
    STAMP(14);
    if (wave < 8) {
        u32x4 k0 = *(LAS const u32x4*)(smem + O_MISC + 32), k1 = *(LAS const u32x4*)(smem + O_MISC + 48);
        u32 lbest = max(max(max(k0.x, k0.y), max(k0.z, k0.w)), max(max(k1.x, k1.y), max(k1.z, k1.w)));
        if (key == lbest) {
            auto rc = __builtin_amdgcn_make_buffer_rsrc((void*)(a.cand + (size_t)chunk * CAND_STRIDE), 0, CAND_STRIDE, 0x00020000);
            if (lane < 8) __hip_atomic_store((GAS u64*)(a.keys + lane * 2048 + chunk * 8), ((u64)tag2 << 32) | key, RLX_AGENT);
#pragma unroll
            for (int i = 0; i < 5; i++) __builtin_amdgcn_raw_buffer_store_b64(u32x2{x2p[i], tag2}, rc, (lane + 64 * i) * 8, 0, 16);
#pragma unroll
            for (int i = 0; i < 5; i++) __builtin_amdgcn_raw_buffer_store_b64(u32x2{hp[i], tag2}, rc, 2560 + (lane + 64 * i) * 8, 0, 16);
        }
    }
    b8 wf[4];
    if (is_f1) {
        const int w10 = wave < 10 ? wave : 0;
        LAS const char* p = smem + O_WBUF + (64 * w10 + 8 * g4 + (n16 >> 2)) * 64 + (n16 & 3) * 8;
        wf[0] = tr_frag(p, 256); wf[1] = tr_frag(p + 32, 256); wf[2] = tr_frag(p + 2048, 256); wf[3] = tr_frag(p + 2048 + 32, 256);
    } else if (is_f2) {
        LAS const char* p = smem + O_WBUF + (128 * wave + 8 * g4 + (n16 >> 2)) * 32 + (n16 & 3) * 8;
        wf[0] = tr_frag(p, 128); wf[1] = tr_frag(p + 1024, 128); wf[2] = tr_frag(p + 2048, 128); wf[3] = tr_frag(p + 3072, 128);
    }
    if (wave == 11) {
        if (is_f1 || is_f2) __builtin_amdgcn_s_sleep(KDELAY); else __builtin_amdgcn_s_sleep(LAZY);
        u32 best = 0;
        {
            auto rk = __builtin_amdgcn_make_buffer_rsrc((void*)(a.keys + ((b >> 3) & 7) * 2048), 0, 2048, 0x00020000);
            bool nd = true;
            for (u32 spins = 0; spins < SPIN_MAX; spins++) {
                if (nd) {
                    u32x4 k0 = __builtin_amdgcn_raw_buffer_load_b128(rk, lane * 16, 0, 16), k1 = __builtin_amdgcn_raw_buffer_load_b128(rk, 1024 + lane * 16, 0, 16);
                    nd = !(k0.y == tag2 && k0.w == tag2 && k1.y == tag2 && k1.w == tag2);
                    best = max(max(k0.x, k0.z), max(k1.x, k1.z));
                }
                if (!__any(nd)) break;
                __builtin_amdgcn_s_sleep(1);
            }
        }
        best = wave_umax(best);
        STAMP(10);
        if (lane == 0) *(LAS u32*)(smem + O_MISC + 64) = best;
        if (is_f1 || is_f2) {
            const int wrow = 2047 - (int)(best & 0x7ffu);
            auto rc = __builtin_amdgcn_make_buffer_rsrc((void*)(a.cand + (size_t)(wrow >> 3) * CAND_STRIDE), 0, CAND_STRIDE, 0x00020000);
            bool nd = true;
            if (is_f1) {
                u32x4 c[3];
                const int o2 = lane < 32 ? 2048 + lane * 16 : 0;
                for (u32 spins = 0; spins < SPIN_MAX; spins++) {
                    if (nd) {
                        c[0] = __builtin_amdgcn_raw_buffer_load_b128(rc, lane * 16, 0, 16);
                        c[1] = __builtin_amdgcn_raw_buffer_load_b128(rc, 1024 + lane * 16, 0, 16);
                        c[2] = __builtin_amdgcn_raw_buffer_load_b128(rc, o2, 0, 16);
                        nd = !(c[0].y == tag2 && c[0].w == tag2 && c[1].y == tag2 && c[1].w == tag2 && c[2].y == tag2 && c[2].w == tag2);
                    }
                    if (!__any(nd)) break;
                    __builtin_amdgcn_s_sleep(1);
                }
                *(LAS u32x2*)(smem + O_XSB + lane * 8) = u32x2{c[0].x, c[0].z};
                *(LAS u32x2*)(smem + O_XSB + 512 + lane * 8) = u32x2{c[1].x, c[1].z};
                if (lane < 32) *(LAS u32x2*)(smem + O_XSB + 1024 + lane * 8) = u32x2{c[2].x, c[2].z};
            } else {
                u32x2 c;
                const int o = ((lane & 8) ? 2560 : 0) + (f2u * 8 + (lane & 7)) * 8;
                for (u32 spins = 0; spins < SPIN_MAX; spins++) {
                    if (nd) { c = __builtin_amdgcn_raw_buffer_load_b64(rc, o, 0, 16); nd = c.y != tag2; }
                    if (!__any(nd)) break;
                    __builtin_amdgcn_s_sleep(1);
                }
                if (lane < 16) *(LAS u32*)(smem + O_XSEL + lane * 4) = c.x;
            }
        }
    }
    raw_barrier();
    STAMP(11);
    const u32 best = *(LAS const u32*)(smem + O_MISC + 64);
    if (is_f1) {
        if (wave < 10) {
            LAS const char* xa = smem + O_XSB + (64 * wave + 8 * g4) * 2;
            b8 a0 = *(LAS const b8*)xa, a1 = *(LAS const b8*)(xa + 64);
            f4 z = {0, 0, 0, 0};
            f4 c0 = mfma16b(a0, wf[0], z), c1 = mfma16b(a0, wf[1], z);
            c0 = mfma16b(a1, wf[2], c0); c1 = mfma16b(a1, wf[3], c1);
            if (lane < 16) { *(LAS float*)(smem + O_RED + wave * 128 + lane * 4) = c0.x; *(LAS float*)(smem + O_RED + wave * 128 + 64 + lane * 4) = c1.x; }
        }
        raw_barrier();
        if (tid < 8) {
            f4 sm = {0, 0, 0, 0};
#pragma unroll
            for (int w = 0; w < 10; w++) sm += *(LAS const f4*)(smem + O_RED + w * 128 + tid * 16);
            auto rg = __builtin_amdgcn_make_buffer_rsrc((void*)a.gu, 0, 96 * 128, 0x00020000);
            __builtin_amdgcn_raw_buffer_store_b128(u32x4{pk_bf16(sm.x, sm.y), tag2, pk_bf16(sm.z, sm.w), tag2}, rg, f1u * 128 + tid * 16, 0, 16);
        }
    }
    if (wave < 8 && key != best) {
#pragma unroll
        for (int i = 0; i < 5; i++)
            ((GAS u32*)(a.out + (size_t)row * D_))[lane + 64 * i] = pk_bf16(bflo(hp[i]) + bflo(x2p[i]), bfhi(hp[i]) + bfhi(x2p[i]));
    }
    if (is_f2) {
        if (wave >= 6) {
            const int gt = tid - 384;
            auto rg = __builtin_amdgcn_make_buffer_rsrc((void*)a.gu, 0, 96 * 128, 0x00020000);
            __builtin_amdgcn_s_sleep(GDELAY);
            u32x4 g, u;
            bool nd = true;
            for (u32 spins = 0; spins < SPIN_MAX; spins++) {
                if (nd) {
                    g = __builtin_amdgcn_raw_buffer_load_b128(rg, gt * 16, 0, 16);
                    u = __builtin_amdgcn_raw_buffer_load_b128(rg, (384 + gt) * 16, 0, 16);
                    nd = !(g.y == tag2 && g.w == tag2 && u.y == tag2 && u.w == tag2);
                }
                if (!__any(nd)) break;
                __builtin_amdgcn_s_sleep(1);
            }
            float gv[4] = {bflo(g.x), bfhi(g.x), bflo(g.z), bfhi(g.z)}, uv[4] = {bflo(u.x), bfhi(u.x), bflo(u.z), bfhi(u.z)};
            float av[4];
#pragma unroll
            for (int k = 0; k < 4; k++) av[k] = rbf(gv[k] / (1.0f + __expf(-gv[k]))) * uv[k];
            *(LAS u32x2*)(smem + O_ACT + gt * 8) = u32x2{pk_bf16(av[0], av[1]), pk_bf16(av[2], av[3])};
        }
        raw_barrier();
        STAMP(13);
        {
            LAS const char* xa = smem + O_ACT + (128 * wave + 8 * g4) * 2;
            f4 c0 = {0, 0, 0, 0}, c1 = c0;
            c0 = mfma16b(*(LAS const b8*)xa, wf[0], c0); c1 = mfma16b(*(LAS const b8*)(xa + 64), wf[1], c1);
            c0 = mfma16b(*(LAS const b8*)(xa + 128), wf[2], c0); c1 = mfma16b(*(LAS const b8*)(xa + 192), wf[3], c1);
            if (lane < 16) *(LAS float*)(smem + O_RED + wave * 64 + lane * 4) = c0.x + c1.x;
        }
        raw_barrier();
        if (tid < 16) {
            float y = 0.f;
#pragma unroll
            for (int w = 0; w < 12; w++) y += *(LAS const float*)(smem + O_RED + w * 64 + tid * 4);
            y = rbf(y);
            const u32 ku = best >> 16;
            const u32 rb = (ku & 0x8000u) ? (ku & 0x7fffu) : (~ku & 0xffffu);
            const float r = __uint_as_float(rb << 16);
            const float rs = rbf(1.0f / (1.0f + __expf(-r)));
            y = rbf(y * rs);
            const float x2v = bf2f(*(LAS const u16*)(smem + O_XSEL + tid * 2)), hv = bf2f(*(LAS const u16*)(smem + O_XSEL + 32 + tid * 2));
            const float bb = rbf(x2v + y);
            const int wrow = 2047 - (int)(best & 0x7ffu);
            a.out[(size_t)wrow * D_ + f2u * 16 + tid] = (u16)(pk_bf16(hv + bb, 0.f) & 0xffffu);
        }
    }
    asm volatile(".Lk_end:" ::: "memory");
    STAMP(12);
}

extern "C" void kernel_launch(void* const* d_in, const int* in_sizes, int n_in,
                              void* d_out, int out_size, void* d_ws, size_t ws_size,
                              hipStream_t stream) {
    char* ws = (char*)d_ws;
    MainArgs a;
    a.epoch = (u32*)ws;             ws += 256;
    a.ybuf = ws;                    ws += L_ * D_ * 2;
    a.clr = ws;                     ws += 256 * 64;
    a.ubuf = ws;                    ws += L_ * D_ * 2;
    a.xch = ws;                     ws += 256 * XCH_STRIDE;
    a.keys = ws;                    ws += 256 * 64;
    a.cand = ws;                    ws += 256 * CAND_STRIDE;
    a.gu = ws;                      ws += 96 * 128;
    (void)hipFuncSetAttribute((const void*)k_main, hipFuncAttributeMaxDynamicSharedMemorySize, LDS_MAIN);
    a.x = (const u16*)d_in[0]; a.Are = (const u16*)d_in[1]; a.Aim = (const u16*)d_in[2]; a.Bre = (const u16*)d_in[3]; a.Bim = (const u16*)d_in[4];
    a.Cre = (const u16*)d_in[5]; a.Cim = (const u16*)d_in[6]; a.logdt = (const u16*)d_in[7]; a.Dskip = (const u16*)d_in[8];
    a.wr = (const u16*)d_in[10]; a.wg = (const u16*)d_in[11]; a.wu = (const u16*)d_in[12]; a.wd = (const u16*)d_in[13];
    a.out = (u16*)d_out;
    a.dbg = (u64*)((char*)d_ws + (128u << 20));
    k_main<<<256, NT, LDS_MAIN, stream>>>(a);
}
```

```cpp
#include <hip/hip_runtime.h>
#include <stdint.h>

typedef unsigned short u16;
typedef unsigned int u32;
typedef unsigned long long u64;
typedef _Float16 h8 __attribute__((ext_vector_type(8)));
typedef _Float16 h2 __attribute__((ext_vector_type(2)));
typedef __bf16 b2 __attribute__((ext_vector_type(2)));
typedef float f2 __attribute__((ext_vector_type(2)));
typedef float f4 __attribute__((ext_vector_type(4)));
typedef u32 u32x2 __attribute__((ext_vector_type(2)));
typedef u32 u32x3 __attribute__((ext_vector_type(3)));
typedef u32 u32x4 __attribute__((ext_vector_type(4)));
#define LAS __attribute__((address_space(3)))
#define GAS __attribute__((address_space(1)))
#define RLX_AGENT __ATOMIC_RELAXED, __HIP_MEMORY_SCOPE_AGENT

constexpr int L_ = 2048, D_ = 640, N_ = 192, F_ = 1536;

__device__ __forceinline__ float bflo(u32 p) { return __uint_as_float(p << 16); }
__device__ __forceinline__ float bfhi(u32 p) { return __uint_as_float(p & 0xffff0000u); }
__device__ __forceinline__ float bf2f(u16 v) { return __uint_as_float(((u32)v) << 16); }
__device__ __forceinline__ u32 pk_bf16(float a, float b) {
    f2 v = {a, b};
    b2 r = __builtin_convertvector(v, b2);
    return __builtin_bit_cast(u32, r);
}
__device__ __forceinline__ u32 pk_f16(float a, float b) {
    f2 v = {a, b};
    h2 r = __builtin_convertvector(v, h2);
    return __builtin_bit_cast(u32, r);
}
__device__ __forceinline__ float rbf(float a) { return bflo(pk_bf16(a, a)); }
__device__ __forceinline__ float dot2bf(u32 a, u32 b, float c) {
    return __builtin_amdgcn_fdot2_f32_bf16(__builtin_bit_cast(b2, a), __builtin_bit_cast(b2, b), c, false);
}

template <int CTRL, int RM = 0xf, int BM = 0xf>
__device__ __forceinline__ float dpp_get(float v) {
    return __int_as_float(__builtin_amdgcn_update_dpp(0, __float_as_int(v), CTRL, RM, BM, false));
}
template <int CTRL, int RM = 0xf, int BM = 0xf>
__device__ __forceinline__ float dpp_add(float v) { return v + dpp_get<CTRL, RM, BM>(v); }
__device__ __forceinline__ float rdl(float v, int l) { return __int_as_float(__builtin_amdgcn_readlane(__float_as_int(v), l)); }
__device__ __forceinline__ float wave_sum(float v) {
    v = dpp_add<0xB1>(v);
    v = dpp_add<0x4E>(v);
    v = dpp_add<0x141>(v);
    v = dpp_add<0x140>(v);
    v = dpp_add<0x142, 0xa>(v);
    v = dpp_add<0x143, 0xc>(v);
    return rdl(v, 63);
}
template <int CTRL, int RM = 0xf, int BM = 0xf>
__device__ __forceinline__ u32 dpp_umax(u32 v) {
    u32 t = (u32)__builtin_amdgcn_update_dpp(0, (int)v, CTRL, RM, BM, false);
    return v > t ? v : t;
}
__device__ __forceinline__ u32 wave_umax(u32 v) {
    v = dpp_umax<0xB1>(v);
    v = dpp_umax<0x4E>(v);
    v = dpp_umax<0x141>(v);
    v = dpp_umax<0x140>(v);
    v = dpp_umax<0x142, 0xa>(v);
    v = dpp_umax<0x143, 0xc>(v);
    return (u32)__builtin_amdgcn_readlane((int)v, 63);
}
__device__ __forceinline__ float xrow_sum(float v) {
    u32 u = __float_as_uint(v);
    auto p = __builtin_amdgcn_permlane16_swap(u, u, false, false);
    v = __uint_as_float(p[0]) + __uint_as_float(p[1]);
    u = __float_as_uint(v);
    auto q = __builtin_amdgcn_permlane32_swap(u, u, false, false);
    return __uint_as_float(q[0]) + __uint_as_float(q[1]);
}
__device__ __forceinline__ float reduce16(const float (&v)[16], int lane) {
    const bool b3 = lane & 8, b2_ = lane & 4, b1 = lane & 2, b0 = lane & 1;
    float w[8], z[4], y[2];
#pragma unroll
    for (int k = 0; k < 8; k++) { float keep = b3 ? v[k + 8] : v[k], give = b3 ? v[k] : v[k + 8]; w[k] = keep + dpp_get<0x140>(give); }
#pragma unroll
    for (int k = 0; k < 4; k++) { float keep = b2_ ? w[k + 4] : w[k], give = b2_ ? w[k] : w[k + 4]; z[k] = keep + dpp_get<0x141>(give); }
#pragma unroll
    for (int k = 0; k < 2; k++) { float keep = b1 ? z[k + 2] : z[k], give = b1 ? z[k] : z[k + 2]; y[k] = keep + dpp_get<0x1B>(give); }
    float keep = b0 ? y[1] : y[0], give = b0 ? y[0] : y[1];
    float r = keep + dpp_get<0xB1>(give);
    return xrow_sum(r);
}

__device__ __forceinline__ void ln_stats(const float (&x)[10], float& m, float& s) {
    float sum = 0.f;
#pragma unroll
    for (int i = 0; i < 10; i++) sum += x[i];
    float mean = wave_sum(sum) * (1.0f / D_);
    float ss = 0.f;
#pragma unroll
    for (int i = 0; i < 10; i++) { float d = x[i] - mean; ss = fmaf(d, d, ss); }
    float var = wave_sum(ss) * (1.0f / D_);
    m = rbf(mean);
    s = rbf(__builtin_sqrtf(rbf(rbf(var) + 1e-5f)));
}
__device__ __forceinline__ float div_r(float a, float s, float r) {
    float q = a * r;
    float e = fmaf(-q, s, a);
    return fmaf(e, r, q);
}

struct cf { float r, i; };
__device__ __forceinline__ cf cmul(cf a, cf b) {
    cf o;
    o.r = fmaf(a.r, b.r, -(a.i * b.i));
    o.i = fmaf(a.r, b.i, a.i * b.r);
    return o;
}
__device__ __forceinline__ cf cmulc(cf a, cf b) {
    cf o;
    o.r = fmaf(a.r, b.r, a.i * b.i);
    o.i = fmaf(a.i, b.r, -(a.r * b.i));
    return o;
}
__device__ __forceinline__ cf csq(cf a) {
    cf o;
    o.r = fmaf(a.r, a.r, -(a.i * a.i));
    o.i = 2.0f * a.r * a.i;
    return o;
}
__device__ __forceinline__ cf cexp_dir(float alpha, float bh, float bl, float x) {
    float mag = __builtin_amdgcn_exp2f(alpha * x);
    float ph = __builtin_amdgcn_fractf(bh * x);
    ph = fmaf(bl, x, ph);
    cf o;
    o.r = mag * __builtin_amdgcn_cosf(ph);
    o.i = mag * __builtin_amdgcn_sinf(ph);
    return o;
}
__device__ __forceinline__ f4 mfma16(h8 a, h8 b, f4 c) { return __builtin_amdgcn_mfma_f32_16x16x32_f16(a, b, c, 0, 0, 0); }
__device__ __forceinline__ void wave_fence() {
    __builtin_amdgcn_fence(__ATOMIC_ACQ_REL, "wavefront");
    __builtin_amdgcn_wave_barrier();
}
__device__ __forceinline__ void lds_signal(LAS u32* w, int lane) {
    asm volatile("s_waitcnt lgkmcnt(0)" ::: "memory");
    if (lane == 0) __hip_atomic_fetch_add(w, 1u, __ATOMIC_RELAXED, __HIP_MEMORY_SCOPE_WORKGROUP);
    asm volatile("" ::: "memory");
}
template <int SLP = 0>
__device__ __forceinline__ void lds_wait(LAS u32* w, u32 want) {
    asm volatile("" ::: "memory");
    while (__builtin_amdgcn_readfirstlane(__hip_atomic_load(w, __ATOMIC_RELAXED, __HIP_MEMORY_SCOPE_WORKGROUP)) < want)
        __builtin_amdgcn_s_sleep(SLP);
    asm volatile("" ::: "memory");
}

#define L2W_EARLY
#define CLR_MODE 0
constexpr int NT = 768;
#define UDELAY 24
#define LAZY 60
#define YDELAY 6
#define KDELAY 8
#define GDELAY 12
#define WDELAY 20
#define WPACE 1
#define HB1 0
#define KSTR 8

constexpr int UIMG_CH = (256 + L_) * 2;
constexpr int O_UIMG = 64;
constexpr int KCS = 4160;
constexpr int KIMG_CH = 4 * KCS;
constexpr int O_KIMG = O_UIMG + 3 * UIMG_CH;
constexpr int SCR = 32 * 272;
constexpr int O_SCR = O_KIMG;
constexpr int O_KTAB = O_SCR + 8 * SCR;
constexpr int KTAB_CH = 656 * 4;
constexpr int O_ACCX = O_KTAB + 3 * KTAB_CH;
constexpr int O_USTG = O_ACCX;
constexpr int URS = 336;
constexpr int O_WBUF = O_ACCX + 2 * 8192;
constexpr int O_FLAG = O_WBUF + 49152;
constexpr int O_MISC = O_FLAG + 96;
constexpr int O_JUNK = O_MISC + 128;
constexpr int O_DBG = O_JUNK + 256;
constexpr int LDS_MAIN = O_DBG;
constexpr int O_ACC2 = O_KIMG + 3 * KIMG_CH;
static_assert(O_ACC2 + 16384 <= O_KTAB, "acc2");
constexpr int YS = 1280 + 16;
constexpr int O_YS = O_KIMG;
constexpr int O_XSEL = O_YS + 8 * YS;
constexpr int O_XSB = O_XSEL + 2560;
constexpr int O_ACT = O_XSB + 1280;
constexpr int O_RED = O_ACT + 3072;
static_assert(8 * SCR >= 3 * KIMG_CH && LDS_MAIN <= 163840, "lds");
static_assert(O_KIMG % 16 == 0 && O_KTAB % 16 == 0 && O_ACCX % 16 == 0 && O_FLAG % 16 == 0 && O_WBUF % 16 == 0 && O_RED % 16 == 0, "align");
enum { FL_P0 = 0, FL_P1, FL_P2, FL_TAB, FL_KIMG, FL_UIMG, FL_ACC0, FL_ACC1, FL_ACC2, FL_ACC3, FL_STAB, FL_SIMG, FL_USTG, FL_SACC, FL_CLR, FL_CONV, FL_CD, FL_W };

struct PosTab { u16 p[512][8]; };
constexpr int rne_lag(int j) {
    if (j < 256) return j;
    int sh = j < 512 ? 1 : (j < 1024 ? 2 : 3);
    int q = j >> sh, rem = j & ((1 << sh) - 1), half = 1 << (sh - 1);
    if (rem > half || (rem == half && (q & 1))) q++;
    return q << sh;
}
constexpr int ktab_pos_c(int j) {
    if (j < 0 || j >= 2048) return 644;
    int l = rne_lag(j);
    if (l < 256) return l;
    if (l < 512) return 256 + ((l - 256) >> 1);
    if (l < 1024) return 384 + ((l - 512) >> 2);
    return 512 + ((l - 1024) >> 3);
}
constexpr PosTab make_postab() {
    PosTab t{};
    for (int c = 0; c < 512; c++) for (int i = 0; i < 8; i++) t.p[c][i] = (u16)(4 * ktab_pos_c(2047 - 4 * c - i));
    return t;
}
__device__ const __attribute__((aligned(16))) PosTab g_postab = make_postab();

#define STAMP(i)

constexpr int XCH_STRIDE = 5376;
constexpr int CAND_STRIDE = 5120;
struct MainArgs {
    const u16 *x, *Are, *Aim, *Bre, *Bim, *Cre, *Cim, *logdt, *Dskip, *wr, *wg, *wu, *wd;
    u16* out;
    u32* epoch;
    char* xch;
    char* ubuf;
    char* ybuf;
    char* clr;
    char* keys;
    char* cand;
    char* gu;
    u64* dbg;
};
constexpr u32 SPIN_MAX = 1u << 16;

typedef float f16v __attribute__((ext_vector_type(16)));
typedef short s4 __attribute__((ext_vector_type(4)));
typedef __bf16 b8 __attribute__((ext_vector_type(8)));
typedef __amdgpu_buffer_rsrc_t rsrc_t;
__device__ __forceinline__ f2 rot(f2 b) { return f2{-b.y, b.x}; }
__device__ __forceinline__ f2 cm(f2 a, f2 b, f2 rb) { return __builtin_elementwise_fma(a.yy, rb, a.xx * b); }
__device__ __forceinline__ f2 csq2(f2 a) { return f2{fmaf(a.x, a.x, -(a.y * a.y)), 2.0f * a.x * a.y}; }
template <bool CONJ>
__device__ __forceinline__ f2 cexp2(float alpha, float bh, float bl, float x) {
    float mag = __builtin_amdgcn_exp2f(alpha * x);
    float ph = __builtin_amdgcn_fractf(bh * x);
    ph = fmaf(bl, x, ph);
    f2 cs = {__builtin_amdgcn_cosf(ph), __builtin_amdgcn_sinf(CONJ ? -ph : ph)};
    return cs * mag;
}
__device__ __forceinline__ void put(LAS char* sp, int row, f2 v) { *(LAS u32*)(sp + row * 272) = pk_f16(v.x, v.y); }

__device__ __forceinline__ h8 ld_b(LAS const char* ub, int t) { return *(LAS const h8*)(ub + 512 * t); }
__device__ __forceinline__ h8 ld_a(LAS const char* kl, LAS const char* kh, int M) {
    u32x2 lo = *(LAS const u32x2*)(kl + 512 * (7 - M));
    u32x2 hi = *(LAS const u32x2*)(kh + 512 * (7 - M));
    u32x4 a4 = {lo.x, lo.y, hi.x, hi.y};
    return __builtin_bit_cast(h8, a4);
}
#define SB() __builtin_amdgcn_sched_barrier(0)
__device__ __forceinline__ void conv_run(f4 (&acc)[8], LAS const char* ub, LAS const char* kl, LAS const char* kh, int nmu) {
    h8 A[8], B[8], Bn[3];
#pragma unroll
    for (int i = 0; i < 8; i++) { A[i] = ld_a(kl, kh, i); if (i < 5) B[7 - i] = ld_b(ub, 7 - i); if (i < 3) B[i] = ld_b(ub, i); SB(); }
#pragma nounroll
    for (int it = 0; it < nmu; it++) {
        ub -= 64; kl -= 64; kh -= 64;
#pragma unroll
        for (int M = 0; M < 8; M++) {
#pragma unroll
            for (int t = 7 - M; t >= 0; t--) acc[t + M] = mfma16(A[M], B[t], acc[t + M]);
            SB();
            A[M] = ld_a(kl, kh, M);
            if (M < 5) B[7 - M] = ld_b(ub, 7 - M);
            if (M < 3) Bn[M] = ld_b(ub, M);
            if (M >= 5) { B[7 - M] = Bn[7 - M]; asm volatile("" : "+v"(B[7 - M])); }
            SB();
        }
    }
}
template <int T0, int T1, int T2, int T3>
__device__ __forceinline__ void conv_half(f4 (&acc)[4], LAS const char* ub, LAS const char* kl, LAS const char* kh) {
    h8 A[T3 + 1], B[T3 + 1];
#pragma unroll
    for (int i = 0; i <= T3; i++) { A[i] = ld_a(kl, kh, i); B[T3 - i] = ld_b(ub, T3 - i); SB(); }
#pragma unroll
    for (int it = 0; it < 2; it++) {
        ub -= 64; kl -= 64; kh -= 64;
#pragma unroll
        for (int M = 0; M <= T3; M++) {
            acc[3] = mfma16(A[M], B[T3 - M], acc[3]);
            if (M <= T2) acc[2] = mfma16(A[M], B[T2 - M], acc[2]);
            if (M <= T1) acc[1] = mfma16(A[M], B[T1 - M], acc[1]);
            if (M <= T0) acc[0] = mfma16(A[M], B[T0 - M], acc[0]);
            SB();
            if (it == 0) { A[M] = ld_a(kl, kh, M); B[T3 - M] = ld_b(ub, T3 - M); }
            SB();
        }
    }
}

__device__ __forceinline__ void store_y_tile(rsrc_t rsy, int ch, int T, int lane, f4 a, float sc) {
    u32x2 o;
    o.x = pk_bf16(a.x * sc, a.y * sc);
    o.y = pk_bf16(a.z * sc, a.w * sc);
    const int n = lane & 15, g = lane >> 4;
    __builtin_amdgcn_raw_buffer_store_b64(o, rsy, (((n >> 1) * D_ + ch) * 64 + (n & 1) * 32 + g * 8) + T * (8 * D_ * 64), 0, 16);
}
__device__ __forceinline__ void raw_barrier() { asm volatile("s_waitcnt lgkmcnt(0)\n\ts_barrier" ::: "memory"); }
__device__ __forceinline__ u32 sort_key(float r, int row) {
    u32 u = __float_as_uint(r) >> 16;
    u = (u & 0x8000u) ? (~u & 0xffffu) : (u | 0x8000u);
    return (u << 16) | (u32)(2047 - row);
}
__device__ __forceinline__ void build_chunk(LAS char* kimg, LAS const char* tab, u32x4 pp, int c) {
    float v[7];
#pragma unroll
    for (int i = 0; i < 7; i++) v[i] = *(LAS const float*)(tab + ((i & 1) ? (pp[i >> 1] >> 16) : (pp[i >> 1] & 0xffffu)));
    u32 p01 = pk_f16(v[0], v[1]), p23 = pk_f16(v[2], v[3]), p45 = pk_f16(v[4], v[5]);
    u32 p12 = pk_f16(v[1], v[2]), p34 = pk_f16(v[3], v[4]), p56 = pk_f16(v[5], v[6]);
    *(LAS u32x2*)(kimg + 0 * KCS + 32 + 8 * c) = u32x2{p01, p23};
    *(LAS u32x2*)(kimg + 1 * KCS + 32 + 8 * c) = u32x2{p12, p34};
    *(LAS u32x2*)(kimg + 2 * KCS + 32 + 8 * c) = u32x2{p23, p45};
    *(LAS u32x2*)(kimg + 3 * KCS + 32 + 8 * c) = u32x2{p34, p56};
}
constexpr u32 CKM = 0x00070007u;
__device__ __forceinline__ u32x4 ck_expect(u32x4 d, u32 e) {
    u32 m0 = d.x & ~CKM, m1 = d.y & ~CKM, m2 = d.z & ~CKM, m3 = d.w & ~CKM;
    u32 s = (m0 + m1) + (m2 + m3) + e;
    s ^= s >> 9;
    u32x4 o;
    o.x = m0 | (s & CKM);
    o.y = m1 | (__builtin_amdgcn_alignbit(s, s, 3) & CKM);
    o.z = m2 | (__builtin_amdgcn_alignbit(s, s, 6) & CKM);
    o.w = m3 | (__builtin_amdgcn_alignbit(s, s, 9) & CKM);
    return o;
}
__device__ __forceinline__ float row16_sum(float v) {
    v = dpp_add<0xB1>(v); v = dpp_add<0x4E>(v); v = dpp_add<0x141>(v); v = dpp_add<0x140>(v);
    return v;
}
__device__ __forceinline__ u32 ln_pair(u32 x, float m, float s, float r) {
    u32 d = pk_bf16(bflo(x) - m, bfhi(x) - m);
    return pk_bf16(div_r(bflo(d), s, r), div_r(bfhi(d), s, r));
}
__device__ __forceinline__ u32x4 norm8(u32x4 x, float m, float s, float r) {
    u32x4 o;
#pragma unroll
    for (int k = 0; k < 4; k++) { u32 u = ln_pair(x[k], m, s, r); o[k] = pk_f16(bflo(u), bfhi(u)); }
    return o;
}
__device__ __forceinline__ void glds16(const void* gsrc, u32 lds_dst) {
    u32 keep;
    asm volatile("s_mov_b32 %0, m0\n\ts_mov_b32 m0, %2\n\ts_nop 0\n\tglobal_load_lds_dwordx4 %1, off\n\ts_mov_b32 m0, %0" : "=&s"(keep) : "v"(gsrc), "s"(lds_dst) : "memory");
}
__device__ __forceinline__ void glds4(const void* gsrc, u32 lds_dst) {
    u32 keep;
    asm volatile("s_mov_b32 %0, m0\n\ts_mov_b32 m0, %2\n\ts_nop 0\n\tglobal_load_lds_dword %1, off\n\ts_mov_b32 m0, %0" : "=&s"(keep) : "v"(gsrc), "s"(lds_dst) : "memory");
}
__device__ __forceinline__ b8 tr_frag(LAS const char* p, int hi_off) {
    s4 lo = __builtin_amdgcn_ds_read_tr16_b64_v4i16((LAS s4*)p);
    s4 hi = __builtin_amdgcn_ds_read_tr16_b64_v4i16((LAS s4*)(p + hi_off));
    u32x2 l2 = __builtin_bit_cast(u32x2, lo), h2_ = __builtin_bit_cast(u32x2, hi);
    return __builtin_bit_cast(b8, u32x4{l2.x, l2.y, h2_.x, h2_.y});
}
__device__ __forceinline__ f4 mfma16b(b8 a, b8 b, f4 c) { return __builtin_amdgcn_mfma_f32_16x16x32_bf16(a, b, c, 0, 0, 0); }

__global__ void __launch_bounds__(NT, 3) k_main(MainArgs a) {
    extern __shared__ __attribute__((aligned(16))) char smem_[];
    LAS char* smem = (LAS char*)smem_;
    const int tid = threadIdx.x, lane = tid & 63;
    u32 warm;
    {
        u64 pc; u32 csz;
        asm volatile("s_getpc_b64 %0\n.Lk_begin:\n\ts_mov_b32 %1, .Lk_end-.Lk_begin" : "=s"(pc), "=s"(csz));
        if (csz > 49152u || csz < 4096u) csz = 128u;
        warm = *(const GAS u32*)((pc & ~127ull) + min((u32)(tid * 64), csz - 128u));
    }
    const int wave = __builtin_amdgcn_readfirstlane(tid >> 6);
    const int b = blockIdx.x;
    const int pairi = (b & 7) * 16 + (b >> 4), side = (b >> 3) & 1;
    const int cbase = 5 * pairi + 2 * side;
    const int ch0 = side ? cbase + 1 : cbase, ch1 = ch0 + 1, chs = 5 * pairi + 2;
    const int chunk = (b & 7) * 32 + (b >> 3);
    LAS u32* flags = (LAS u32*)(smem + O_FLAG);
    STAMP(0);
    u32 ep;
#define LOAD_EPOCH() asm volatile("s_load_dword %0, %1, 0x0\n\ts_waitcnt lgkmcnt(0)" : "=s"(ep) : "s"(a.epoch) : "memory")
    const rsrc_t rsy = __builtin_amdgcn_make_buffer_rsrc((void*)a.ybuf, 0, 64 * D_ * 64, 0x00020000);
    const int jl = b >> 3;
    const bool is_f1 = jl < 12, is_f2 = jl >= 12 && jl < 17;
    const int f1u = (b & 7) * 12 + jl;
    const int f2u = (b & 7) * 5 + (jl - 12);
    const int n16 = lane & 15, g4 = lane >> 4;
    const int row = chunk * 8 + (wave & 7);
    u32 xp[5], sk[5], wp[5];
    u32 dp_[5];
#define LN2_LOAD() do {                                                                                                       \
        const GAS u32* xr_ = (const GAS u32*)(a.x + (size_t)row * D_);                                                        \
        _Pragma("unroll") for (int i = 0; i < 5; i++) {                                                                       \
            xp[i] = xr_[lane + 64 * i]; dp_[i] = ((const GAS u32*)a.Dskip)[lane + 64 * i]; wp[i] = ((const GAS u32*)a.wr)[lane + 64 * i]; } \
        SB();                                                                                                                 \
    } while (0)
#define LN2_CALC() do {                                                                                                       \
        const u32 stp_ = *(LAS const u32*)(smem + O_MISC + 4 * wave);                                                         \
        const float m1_ = bflo(stp_), s1_ = bfhi(stp_), r1_ = __builtin_amdgcn_rcpf(s1_);                                     \
        _Pragma("unroll") for (int i = 0; i < 5; i++) {                                                                       \
            u32 u_ = ln_pair(xp[i], m1_, s1_, r1_);                                                                           \
            sk[i] = pk_bf16(bflo(u_) * bflo(dp_[i]), bfhi(u_) * bfhi(dp_[i])); }                                              \
        asm volatile("" : "+v"(wp[0]), "+v"(wp[1]), "+v"(wp[2]), "+v"(wp[3]), "+v"(wp[4]));                                    \
    } while (0)

    if (wave < 8) {
        const int slot_g = wave < 3 ? 0 : (wave < 6 ? 1 : 2);
        const int ch_g = slot_g == 0 ? ch0 : (slot_g == 1 ? ch1 : chs);
        int mode; bool act = true;
        if (slot_g < 2) mode = (wave - 3 * slot_g) * 64 + lane;
        else { mode = 96 * side + (wave - 6) * 64 + lane; act = (wave == 6) || (lane < 32); if (!act) mode = 0; }
        const int gi = ch_g * N_ + mode;
        u16 pdt = a.logdt[ch_g];
        u16 pAre = a.Are[gi], pAim = a.Aim[gi], pBre = a.Bre[gi], pBim = a.Bim[gi], pCre = a.Cre[gi], pCim = a.Cim[gi];
        u32x4 pp = *(const GAS u32x4*)(&g_postab.p[tid][0]);
        if (tid < 20) flags[tid] = 0;
        if (tid >= 64 && tid < 160) { int t = tid - 64; *(LAS u32x4*)(smem + O_UIMG + (t >> 5) * UIMG_CH + (t & 31) * 16) = u32x4{0, 0, 0, 0}; }
        LOAD_EPOCH();
        raw_barrier();
        if (wave >= 6) __builtin_amdgcn_s_setprio(1);
        float Are = bf2f(pAre), Aim = bf2f(pAim), Bre = bf2f(pBre), Bim = bf2f(pBim), Cre = bf2f(pCre), Cim = bf2f(pCim);
        float dt = rbf(expf(bf2f(pdt)));
        float are = dt * Are, aim = dt * Aim, br = dt * Bre, bi = dt * Bim;
        f2 wc = {fmaf(Cre, br, -(Cim * bi)), -fmaf(Cre, bi, Cim * br)};
        if (!act) wc = f2{0.f, 0.f};
        asm volatile("" :: "v"(warm), "v"(pp));
        STAMP(1);
        {
            const u32x4 ones = {~0u, ~0u, ~0u, ~0u};
            __builtin_amdgcn_raw_buffer_store_b128(ones, rsy, ((tid >> 3) * D_ + ch0) * 64 + (tid & 7) * 16, 0, 16);
            if (wave < 2) {
                const int tq = tid >> 5;
                const int T = side ? (tq == 0 ? 1 : (tq == 1 ? 6 : (tq == 2 ? 2 : 5))) : (tq == 0 ? 0 : (tq == 1 ? 7 : (tq == 2 ? 3 : 4)));
                __builtin_amdgcn_raw_buffer_store_b128(ones, rsy, ((8 * T + ((tid >> 2) & 7)) * D_ + chs) * 64 + (tid & 3) * 16, 0, 16);
            }
        }
        if (lane < 30) {
            const int xk = b & 7, li = min(wave * 30 + lane, 235);
            const char* p;
            if (li < 120) { const int r = li / 6; p = (const char*)(xk < 4 ? a.wg : a.wu) + (size_t)(20 * jl + r) * (F_ * 2) + 768 * (xk & 3) + (li - 6 * r) * 128; }
            else if (li < 216) { const int l2 = li - 120; p = (const char*)a.wd + (size_t)(48 * jl + (l2 >> 1)) * (D_ * 2) + ((160 * xk) & ~127) + (l2 & 1) * 128; }
            else if (li < 226) p = (const char*)a.Dskip + (li - 216) * 128;
            else p = (const char*)a.wr + (li - 226) * 128;
            glds4(p, (u32)O_JUNK);
        }
#define NO_L2W
        float alpha = are * 1.44269504088896341f;
        const float CH = 0.15915494f, CL = 6.4206382e-09f;
        float p_ = aim * CH;
        float pe = fmaf(aim, CL, fmaf(aim, CH, -p_));
        float pf = p_ - __builtin_rintf(p_);
        float bh = __uint_as_float(__float_as_uint(pf + pe) & 0xfffff800u);
        float bl = (pf - bh) + pe;
        float mw = fabsf(wc.x) + fabsf(wc.y);
        float v0r = 16384.f;
        if (__builtin_expect(__any(alpha > 0.f), 0)) {
            float ap = fmaxf(alpha, 0.f);
            float g1 = __builtin_amdgcn_exp2f(ap * 1024.f);
            wc = (wc * g1) * g1;
            mw = (fabsf(wc.x) + fabsf(wc.y)) * __builtin_amdgcn_exp2f(ap * 248.f);
            v0r = __builtin_amdgcn_exp2f(fmaf(ap, -2048.f, 14.f));
        }
        u32 mwu = wave_umax(__float_as_uint(mw));
        int e = (int)(mwu >> 23) - 126;
        if (e < -100) e = -100;
        if (e > 127) e = 127;
        LAS char* scr = smem + O_SCR + wave * SCR;
        LAS char* sp = scr + lane * 4;
        LAS const char* fp = scr + (lane & 31) * 272 + (lane >> 5) * 16;
        {
            f2 E8 = cexp2<true>(alpha, bh, bl, 8.f);
            f2 E16 = csq2(E8), E32 = csq2(E16);
            f2 r8 = rot(E8), r32 = rot(E32);
            f2 Y0 = wc * __builtin_amdgcn_ldexpf(1.0f, 14 - e);
            f2 Y2 = cm(Y0, E16, rot(E16));
            f2 Y1 = cm(Y0, E8, r8), Y3 = cm(Y2, E8, r8);
#pragma unroll
            for (int q = 0; q < 32; q += 4) {
                put(sp, q, Y0); put(sp, q + 1, Y1); put(sp, q + 2, Y2); put(sp, q + 3, Y3);
                if (q < 28) {
                    f2 t0 = Y0.xx * E32, t1 = Y1.xx * E32, t2 = Y2.xx * E32, t3 = Y3.xx * E32;
                    asm volatile("" : "+v"(t0), "+v"(t1), "+v"(t2), "+v"(t3));
                    Y0 = __builtin_elementwise_fma(Y0.yy, r32, t0); Y1 = __builtin_elementwise_fma(Y1.yy, r32, t1);
                    Y2 = __builtin_elementwise_fma(Y2.yy, r32, t2); Y3 = __builtin_elementwise_fma(Y3.yy, r32, t3);
                }
            }
        }
        wave_fence();
        h8 By[8];
#pragma unroll
        for (int k = 0; k < 8; k++) By[k] = *(LAS const h8*)(fp + 32 * k);
        wave_fence();
        {
            f2 E1 = cexp2<false>(alpha, bh, bl, 1.f), E256 = cexp2<false>(alpha, bh, bl, 256.f), E1024 = cexp2<false>(alpha, bh, bl, 1024.f);
            f2 E2 = csq2(E1), E4 = csq2(E2), E512 = csq2(E256), E2048 = csq2(E1024);
            f2 r1 = rot(E1), r256 = rot(E256);
            f2 E768 = cm(E512, E256, r256);
            f2 V2 = E2 * v0r, V4 = E4 * v0r;
            f2 rV4 = rot(V4);
            f2 V6 = cm(E2, V4, rV4);
            put(sp, 0, f2{v0r, 0.f}); put(sp, 1, E1 * v0r); put(sp, 2, V2); put(sp, 3, cm(V2, E1, r1));
            put(sp, 4, V4); put(sp, 5, cm(V4, E1, r1)); put(sp, 6, V6); put(sp, 7, cm(V6, E1, r1));
                put(sp, 8, E256 * v0r); put(sp, 9, cm(V2, E256, r256)); put(sp, 10, cm(V4, E256, r256)); put(sp, 11, cm(V6, E256, r256));
            put(sp, 12, E512 * v0r); put(sp, 13, cm(E512, V4, rV4)); put(sp, 14, E768 * v0r); put(sp, 15, cm(E768, V4, rV4));
            f2 H0 = E1024 * v0r;
            f2 H1 = cm(H0, E256, r256), H2 = cm(H1, E256, r256), H3 = cm(H2, E256, r256);
            put(sp, 16, H0); put(sp, 17, H1); put(sp, 18, H2); put(sp, 19, H3); put(sp, 20, E2048 * v0r);
        }
        wave_fence();
        f16v dd = {0, 0, 0, 0, 0, 0, 0, 0, 0, 0, 0, 0, 0, 0, 0, 0};
#pragma unroll
        for (int k = 0; k < 8; k++) dd = __builtin_amdgcn_mfma_f32_32x32x16_f16(*(LAS const h8*)(fp + 32 * k), By[k], dd, 0, 0, 0);
        wave_fence();
        {
            const float ts = __builtin_amdgcn_ldexpf(1.0f, e - 28);
            const int q = lane & 31, h = lane >> 5;
            *(LAS f4*)(scr + (8 * q + 4 * h) * 4) = f4{dd[0], dd[1], dd[2], dd[3]} * ts;
            if (h == 0) {
                *(LAS f4*)(scr + (256 + 4 * q) * 4) = f4{dd[4], dd[5], dd[6], dd[7]} * ts;
                *(LAS float*)(scr + (512 + q) * 4) = dd[8] * ts;
                *(LAS float*)(scr + (544 + q) * 4) = dd[9] * ts;
                *(LAS float*)(scr + (576 + q) * 4) = dd[10] * ts;
                *(LAS float*)(scr + (608 + q) * 4) = dd[11] * ts;
            } else {
                *(LAS f2*)(scr + (384 + 2 * q) * 4) = f2{dd[4], dd[5]} * ts;
                *(LAS f2*)(scr + (448 + 2 * q) * 4) = f2{dd[6], dd[7]} * ts;
                if (q == 0) {
                    *(LAS f4*)(scr + 640 * 4) = f4{dd[8] * ts, 0.f, 0.f, 0.f};
                    *(LAS u32x2*)(scr + 644 * 4) = u32x2{0u, (u32)e};
                }
            }
        }
        STAMP(2);
        asm volatile("s_waitcnt vmcnt(1)" ::: "memory");
        lds_signal(flags + FL_P0 + slot_g, lane);
        const u32 tag = ep * 0x9E3779B1u + 0x7F4A7C15u;
        lds_wait(flags + FL_P0 + slot_g, slot_g < 2 ? 3 : 2);
        STAMP(3);
        {
            LAS const char* p0 = smem + O_SCR + (3 * slot_g) * SCR;
            LAS char* tab = smem + O_KTAB + slot_g * KTAB_CH;
            int em = max(*(LAS const int*)(p0 + 645 * 4), *(LAS const int*)(p0 + SCR + 645 * 4));
            if (slot_g < 2) {
                const int idx = tid - slot_g * 192;
                em = max(em, *(LAS const int*)(p0 + 2 * SCR + 645 * 4));
                const int ic = idx < 161 ? idx : 161;
                f4 sm = *(LAS const f4*)(p0 + ic * 16) + *(LAS const f4*)(p0 + SCR + ic * 16) + *(LAS const f4*)(p0 + 2 * SCR + ic * 16);
                sm *= __builtin_amdgcn_ldexpf(1.0f, 7 - em);
                if (idx >= 161) sm = f4{0.f, __builtin_amdgcn_ldexpf(1.0f, em - 7), 0.f, 0.f};
                *(LAS f4*)(tab + ic * 16) = sm;
            } else {
                auto rs = __builtin_amdgcn_make_buffer_rsrc((void*)(a.xch + (size_t)b * XCH_STRIDE), 0, XCH_STRIDE, 0x00020000);
                const int i0 = tid - 384, i1 = i0 < 33 ? i0 + 128 : 160;
                f4 s0 = *(LAS const f4*)(p0 + i0 * 16) + *(LAS const f4*)(p0 + SCR + i0 * 16);
                f4 s1 = *(LAS const f4*)(p0 + i1 * 16) + *(LAS const f4*)(p0 + SCR + i1 * 16);
                if (i1 == 160) s1.y = __int_as_float(em);
                *(LAS f4*)(tab + i0 * 16) = s0;
                if (wave == 6) { *(LAS f4*)(tab + i1 * 16) = s1; if (lane == 0) *(LAS int*)(tab + 648 * 4) = em; }
                lds_signal(flags + FL_TAB, lane);
                __builtin_amdgcn_raw_buffer_store_b128(u32x4{__float_as_uint(s0.x), tag, __float_as_uint(s0.y), tag}, rs, i0 * 16, 0, 16);
                __builtin_amdgcn_raw_buffer_store_b128(u32x4{__float_as_uint(s0.z), tag, __float_as_uint(s0.w), tag}, rs, (162 + i0) * 16, 0, 16);
                if (wave == 6) {
                    __builtin_amdgcn_raw_buffer_store_b128(u32x4{__float_as_uint(s1.x), tag, __float_as_uint(s1.y), tag}, rs, i1 * 16, 0, 16);
                    __builtin_amdgcn_raw_buffer_store_b128(u32x4{__float_as_uint(s1.z), tag, __float_as_uint(s1.w), tag}, rs, (162 + i1) * 16, 0, 16);
                }
            }
            if (slot_g < 2) lds_signal(flags + FL_TAB, lane);
        }
        __builtin_amdgcn_s_setprio(0);
        lds_wait(flags + FL_TAB, 8);
        STAMP(4);
        if (tid < 8) __hip_atomic_store((GAS u64*)(a.clr + tid * 2048 + b * 8), ((u64)(ep * 0x2545F491u + 0x9E3779B9u) << 32) | (ep * 0xC2B2AE35u + 0x27D4EB2Fu), RLX_AGENT);
        build_chunk(smem + O_KIMG, smem + O_KTAB, pp, tid);
        if (wave >= 4 || !HB1) build_chunk(smem + O_KIMG + KIMG_CH, smem + O_KTAB + KTAB_CH, pp, tid);
        if (tid < 24) *(LAS u32x4*)(smem + O_KIMG + (tid >> 1) * KCS + 4128 + (tid & 1) * 16) = u32x4{0, 0, 0, 0};
        lds_signal(flags + FL_KIMG, lane);
        STAMP(5);

        LN2_LOAD();
        const int sK = (3 - n16) & 3;
        const int uoff = 2 * (256 + 16 * n16 - 16 + 8 * g4);
        const int koff = sK * KCS + 2 * (2047 - n16 + 8 * g4 - sK) - 512 * 7;
        if (wave < 4) {
            const int slot = wave >> 1, odd = wave & 1;
            LAS const char* ub = smem + O_UIMG + slot * UIMG_CH + uoff - 256 * odd;
            LAS const char* kl = smem + O_KIMG + slot * KIMG_CH + koff - 256 * odd;
            LAS const char* kh = kl + 8;
            asm volatile("" : "+v"(kh));
            f4 acc[8];
#pragma unroll
            for (int t = 0; t < 8; t++) acc[t] = f4{0, 0, 0, 0};
            lds_wait<1>(flags + FL_KIMG, 12);
            lds_wait<1>(flags + FL_UIMG, 4);
            __builtin_amdgcn_s_setprio(2);
            STAMP(6);
            conv_run(acc, ub, kl, kh, 4);
            STAMP(7);
            LAS char* axw = smem + O_ACCX + wave * 4096 + lane * 16;
            LAS const char* axr = smem + O_ACCX + (wave ^ 1) * 4096 + lane * 16;
            f4 keep[4];
#pragma unroll
            for (int t = 0; t < 4; t++) {
                f4 give;
#pragma unroll
                for (int k = 0; k < 4; k++) { give[k] = odd ? acc[t][k] : acc[t + 4][k]; keep[t][k] = odd ? acc[t + 4][k] : acc[t][k]; }
                *(LAS f4*)(axw + 1024 * t) = give;
            }
            lds_signal(flags + FL_ACC0 + wave, lane);
            const float sc = *(LAS const float*)(smem + O_KTAB + slot * KTAB_CH + 645 * 4);
            lds_wait(flags + FL_ACC0 + (wave ^ 1), 1);
#pragma unroll
            for (int t = 0; t < 4; t++) {
                f4 o = keep[t] + *(LAS const f4*)(axr + 1024 * t);
                store_y_tile(rsy, slot ? ch1 : ch0, t + 4 * odd, lane, o, sc);
            }
            {
                LAS const char* a2 = smem + O_ACC2 + lane * 16 + wave * 4096;
                lds_wait(flags + FL_SACC, 4);
                const float sc2 = *(LAS const float*)(smem + O_KTAB + 2 * KTAB_CH + 645 * 4);
                f4 o = (*(LAS const f4*)(a2) + *(LAS const f4*)(a2 + 1024)) + (*(LAS const f4*)(a2 + 2048) + *(LAS const f4*)(a2 + 3072));
                const int T = side ? (wave == 0 ? 1 : (wave == 1 ? 2 : (wave == 2 ? 5 : 6))) : (wave == 0 ? 0 : (wave == 1 ? 3 : (wave == 2 ? 4 : 7)));
                store_y_tile(rsy, chs, T, lane, o, sc2);
            }
            __builtin_amdgcn_s_setprio(0);
            lds_signal(flags + FL_CONV, lane);
            STAMP(10);
            LN2_CALC();
            STAMP(8);
        } else {
            const int w4 = wave - 4;
            LAS const char* ub = smem + O_UIMG + 2 * UIMG_CH + uoff - 128 * w4;
            LAS const char* kl = smem + O_KIMG + 2 * KIMG_CH + koff - 128 * w4;
            LAS const char* kh = kl + 8;
            asm volatile("" : "+v"(kh));
            f4 acc[4];
#pragma unroll
            for (int t = 0; t < 4; t++) acc[t] = f4{0, 0, 0, 0};
            lds_wait<1>(flags + FL_SIMG, 4);
            lds_wait<1>(flags + FL_KIMG, 12);
            __builtin_amdgcn_s_setprio(3);
            STAMP(7);
            if (side) conv_half<1, 2, 5, 6>(acc, ub, kl, kh); else conv_half<0, 3, 4, 7>(acc, ub, kl, kh);
            STAMP(6);
            LAS char* a2 = smem + O_ACC2 + lane * 16;
#pragma unroll
            for (int t = 0; t < 4; t++) *(LAS f4*)(a2 + (t * 4 + w4) * 1024) = acc[t];
            lds_signal(flags + FL_SACC, lane);
            __builtin_amdgcn_s_setprio(0);
            STAMP(10);
            LN2_CALC();
        }
    } else {
        const int hw = wave - 8, ht = tid - 512;
        const int blk = chunk >> 2, cq = chunk & 3;
        const GAS u32x4* xb = (const GAS u32x4*)a.x + (size_t)(blk * 32 + 8 * hw) * 80;
        u32x4 xq[2][5];
#pragma unroll
        for (int i = 0; i < 5; i++) {
            int c = 20 * cq + n16 + 16 * i; if (c >= 80) c -= 80;
            xq[0][i] = xb[g4 * 80 + c]; xq[1][i] = xb[(4 + g4) * 80 + c];
        }
        const int er = (lane >> 2) & 7;
        u32x4 xe = xb[er * 80 + 20 * cq + 16 + (lane & 3)];
        LOAD_EPOCH();
        __builtin_amdgcn_s_barrier();
        __builtin_amdgcn_s_setprio(2);
        float mB[2], sB[2], rB[2];
        {
            float s1[2], s2[2];
#pragma unroll
            for (int g2 = 0; g2 < 2; g2++) {
                float a1 = 0.f, a2 = 0.f, b1 = 0.f, b2v = 0.f;
#pragma unroll
                for (int i = 0; i < 5; i++) {
                    a1 = dot2bf(xq[g2][i].x, 0x3f803f80u, a1); a2 = dot2bf(xq[g2][i].x, xq[g2][i].x, a2);
                    b1 = dot2bf(xq[g2][i].y, 0x3f803f80u, b1); b2v = dot2bf(xq[g2][i].y, xq[g2][i].y, b2v);
                    a1 = dot2bf(xq[g2][i].z, 0x3f803f80u, a1); a2 = dot2bf(xq[g2][i].z, xq[g2][i].z, a2);
                    b1 = dot2bf(xq[g2][i].w, 0x3f803f80u, b1); b2v = dot2bf(xq[g2][i].w, xq[g2][i].w, b2v);
                }
                s1[g2] = row16_sum(a1 + b1); s2[g2] = row16_sum(a2 + b2v);
            }
            float mean[2], var[2];
#pragma unroll
            for (int g2 = 0; g2 < 2; g2++) { mean[g2] = s1[g2] * (1.0f / D_); var[g2] = fmaf(-mean[g2], mean[g2], s2[g2] * (1.0f / D_)); }
            if (__builtin_expect(__any(mean[0] * mean[0] > 16.f * var[0] || mean[1] * mean[1] > 16.f * var[1]), 0)) {
#pragma unroll
                for (int g2 = 0; g2 < 2; g2++) {
                    float ss = 0.f;
#pragma unroll
                    for (int i = 0; i < 5; i++)
#pragma unroll
                        for (int k = 0; k < 4; k++) {
                            float d0 = bflo(xq[g2][i][k]) - mean[g2], d1 = bfhi(xq[g2][i][k]) - mean[g2];
                            ss = fmaf(d0, d0, ss); ss = fmaf(d1, d1, ss);
                        }
                    var[g2] = row16_sum(ss) * (1.0f / D_);
                }
            }
#pragma unroll
            for (int g2 = 0; g2 < 2; g2++) {
                mB[g2] = rbf(mean[g2]);
                sB[g2] = rbf(__builtin_sqrtf(rbf(rbf(var[g2]) + 1e-5f)));
                rB[g2] = __builtin_amdgcn_rcpf(sB[g2]);
            }
        }
        STAMP(1);
        LAS char* stg = smem + O_USTG;
        {
            const u32 ms0 = (__float_as_uint(mB[0]) >> 16) | __float_as_uint(sB[0]), ms1 = (__float_as_uint(mB[1]) >> 16) | __float_as_uint(sB[1]);
            if (hw == cq && n16 == 0) { *(LAS u32*)(smem + O_MISC + 4 * g4) = ms0; *(LAS u32*)(smem + O_MISC + 16 + 4 * g4) = ms1; }
            const int src = (er & 3) * 64;
            u32 e0 = (u32)__builtin_amdgcn_ds_bpermute(src, (int)ms0), e1 = (u32)__builtin_amdgcn_ds_bpermute(src, (int)ms1);
            *(LAS u32x4*)(stg + (8 * hw + g4) * URS + n16 * 16) = norm8(xq[0][0], mB[0], sB[0], rB[0]);
            *(LAS u32x4*)(stg + (8 * hw + 4 + g4) * URS + n16 * 16) = norm8(xq[1][0], mB[1], sB[1], rB[1]);
            const u32 em = (er & 4) ? e1 : e0;
            const float se = bfhi(em);
            u32x4 ne = norm8(xe, bflo(em), se, __builtin_amdgcn_rcpf(se));
            if (lane < 32) *(LAS u32x4*)(stg + (8 * hw + er) * URS + (16 + (lane & 3)) * 16) = ne;
        }
        asm volatile("" :: "v"(warm));
        const u32 cke = ep * 0x85EBCA77u + 0x165667B1u;
        lds_signal(flags + FL_USTG, lane);
        lds_wait(flags + FL_USTG, 4);
        STAMP(2);
        auto rsu = __builtin_amdgcn_make_buffer_rsrc((void*)a.ubuf, 0, 64 * D_ * 64, 0x00020000);
        {
            const int gbase = (blk * D_ + 160 * cq + n16) * 64 + g4 * 16;
            LAS const char* tp = stg + (8 * g4 + (n16 >> 2)) * URS + (n16 & 3) * 8;
#pragma unroll
            for (int ps = 0; ps < 3; ps++) {
                const int cb = hw + 4 * ps;
                if (cb < 10)
                    __builtin_amdgcn_raw_buffer_store_b128(ck_expect(__builtin_bit_cast(u32x4, tr_frag(tp + cb * 32, 4 * URS)), cke), rsu, gbase + cb * 1024, 0, 16);
            }
        }
        __builtin_amdgcn_s_setprio(0);
        STAMP(3);
        u32x4 ppa = *(const GAS u32x4*)(&g_postab.p[ht][0]), ppb = *(const GAS u32x4*)(&g_postab.p[ht + 256][0]);
        __builtin_amdgcn_s_sleep(UDELAY);
        STAMP(4);
        {
            int goff[3], loff[3];
#pragma unroll
            for (int ps = 0; ps < 3; ps++) {
                const int un = ht + 256 * ps;
                const int rb = un / 12, w12 = un - rb * 12;
                goff[ps] = (rb * D_ + cbase) * 64 + w12 * 16;
                const int c = w12 >> 2;
                const int slot = side ? (c == 0 ? 2 : c - 1) : c;
                loff[ps] = O_UIMG + slot * UIMG_CH + 512 + rb * 64 + (w12 & 3) * 16;
            }
            u32x4 d[3];
            u32 need = 7;
            for (u32 spins = 0; spins < SPIN_MAX; spins++) {
#pragma unroll
                for (int ps = 0; ps < 3; ps++) if (need & (1u << ps)) d[ps] = __builtin_amdgcn_raw_buffer_load_b128(rsu, goff[ps], 0, 16);
                u32 bad = 0;
#pragma unroll
                for (int ps = 0; ps < 3; ps++) {
                    u32x4 ex = ck_expect(d[ps], cke);
                    if (!(ex.x == d[ps].x && ex.y == d[ps].y && ex.z == d[ps].z && ex.w == d[ps].w)) bad |= 1u << ps;
                }
                bad |= (u32)__builtin_amdgcn_update_dpp(0, (int)bad, 0xB1, 0xf, 0xf, false);
                bad |= (u32)__builtin_amdgcn_update_dpp(0, (int)bad, 0x4E, 0xf, 0xf, false);
                need = bad;
                if (!__any(need != 0)) break;
                __builtin_amdgcn_s_sleep(4);
            }
#pragma unroll
            for (int ps = 0; ps < 3; ps++)
                *(LAS u32x4*)(smem + loff[ps]) = u32x4{d[ps].x & ~CKM, d[ps].y & ~CKM, d[ps].z & ~CKM, d[ps].w & ~CKM};
        }
        lds_signal(flags + FL_UIMG, lane);
        STAMP(5);
        lds_signal(flags + FL_KIMG, lane);
        {
            __builtin_amdgcn_s_setprio(3);
            LAS char* tab = smem + O_KTAB + 2 * KTAB_CH;
            const u32 tag = ep * 0x9E3779B1u + 0x7F4A7C15u;
            if (ht < 192) {
                auto rs = __builtin_amdgcn_make_buffer_rsrc((void*)(a.xch + (size_t)(b ^ 8) * XCH_STRIDE), 0, XCH_STRIDE, 0x00020000);
                const int hi = ht < 161 ? ht : 160;
                u32x4 v0, v1, v2;
                bool need = true;
                for (u32 spins = 0; spins < SPIN_MAX; spins++) {
                    if (need) {
                        v0 = __builtin_amdgcn_raw_buffer_load_b128(rs, hi * 16, 0, 16);
                        v1 = __builtin_amdgcn_raw_buffer_load_b128(rs, (162 + hi) * 16, 0, 16);
                        v2 = __builtin_amdgcn_raw_buffer_load_b128(rs, 160 * 16, 0, 16);
                        need = !(v0.y == tag && v0.w == tag && v1.y == tag && v1.w == tag && v2.y == tag && v2.w == tag);
                    }
                    if (!__any(need)) break;
                    __builtin_amdgcn_s_sleep(1);
                }
                lds_wait<1>(flags + FL_TAB, 8);
                int em = max(*(LAS const int*)(tab + 648 * 4), (int)v2.z);
                f4 loc = *(LAS const f4*)(tab + hi * 16);
                f4 rem = {__uint_as_float(v0.x), __uint_as_float(v0.z), __uint_as_float(v1.x), __uint_as_float(v1.z)};
                if (ht < 161) *(LAS f4*)(tab + ht * 16) = (loc + rem) * __builtin_amdgcn_ldexpf(1.0f, 7 - em);
                else if (ht == 161) *(LAS f2*)(tab + 644 * 4) = f2{0.f, __builtin_amdgcn_ldexpf(1.0f, em - 7)};
                lds_signal(flags + FL_STAB, lane);
            }
            STAMP(6);
            lds_wait<1>(flags + FL_STAB, 3);
            build_chunk(smem + O_KIMG + 2 * KIMG_CH, tab, ppa, ht);
            build_chunk(smem + O_KIMG + 2 * KIMG_CH, tab, ppb, ht + 256);
            lds_signal(flags + FL_SIMG, lane);
            __builtin_amdgcn_s_setprio(0);
            STAMP(7);
        }
        if (hw == 0) {
            const u64 want = ((u64)(ep * 0x2545F491u + 0x9E3779B9u) << 32) | (ep * 0xC2B2AE35u + 0x27D4EB2Fu);
            auto rk = __builtin_amdgcn_make_buffer_rsrc((void*)(a.clr + ((b >> 3) & 7) * 2048), 0, 2048, 0x00020000);
            const u32 wl = (u32)want, wh = (u32)(want >> 32);
            bool nd = true;
            for (u32 spins = 0; spins < SPIN_MAX; spins++) {
                if (nd) {
                    u32x4 k0 = __builtin_amdgcn_raw_buffer_load_b128(rk, lane * 16, 0, 16), k1 = __builtin_amdgcn_raw_buffer_load_b128(rk, 1024 + lane * 16, 0, 16);
                    nd = !(k0.x == wl && k0.y == wh && k0.z == wl && k0.w == wh && k1.x == wl && k1.y == wh && k1.z == wl && k1.w == wh);
                }
                if (!__any(nd)) break;
                __builtin_amdgcn_s_sleep(8);
            }
            lds_signal(flags + FL_CLR, lane);
            STAMP(10);
        }
        if ((is_f1 || is_f2) && hw > 0) {
            const int wn = is_f1 ? 40 : 48;
            const int wjs = is_f1 ? 16 * F_ * 2 : 32 * D_ * 2;
            const char* wsrc = is_f1 ? (const char*)((f1u >= 48 ? a.wu : a.wg) + (f1u >= 48 ? f1u - 48 : f1u) * 32 + (lane >> 2) * F_ + (lane & 3) * 8)
                                     : (const char*)(a.wd + f2u * 16 + (lane >> 1) * D_ + (lane & 1) * 8);
            wsrc += (hw - 1) * wjs;
            for (int i = hw - 1; i < wn; i += 3) { glds16(wsrc, (u32)(O_WBUF + i * 1024)); wsrc += 3 * wjs; __builtin_amdgcn_s_sleep(WPACE); }
        }
    }
    const u32 tag2 = ep * 0x9E3779B1u + 0x3C6EF372u;
    if (wave >= 4 && wave < 9) {
        const int pt = tid - 256;
        lds_wait<6>(flags + FL_CLR, 1);
        lds_wait<1>(flags + FL_CONV, 4);
        __builtin_amdgcn_s_sleep(YDELAY);
        STAMP(8);
        const int yo = ((chunk >> 2) * D_ + 2 * pt) * 64 + (chunk & 3) * 16;
        u32x4 y0, y1;
        bool need = true;
        for (u32 spins = 0; spins < SPIN_MAX; spins++) {
            if (need) {
                y0 = __builtin_amdgcn_raw_buffer_load_b128(rsy, yo, 0, 16);
                y1 = __builtin_amdgcn_raw_buffer_load_b128(rsy, yo + 64, 0, 16);
                bool bad = false;
#pragma unroll
                for (int k = 0; k < 4; k++) {
                    bad |= y0[k] >= 0xffff0000u || (u16)y0[k] == 0xffffu;
                    bad |= y1[k] >= 0xffff0000u || (u16)y1[k] == 0xffffu;
                }
                need = bad;
            }
            if (!__any(need)) break;
            __builtin_amdgcn_s_sleep(2);
        }
        LAS char* ys = smem + O_YS;
#pragma unroll
        for (int k = 0; k < 4; k++) {
            *(LAS u32*)(ys + (2 * k) * YS + 4 * pt) = __builtin_amdgcn_perm(y1[k], y0[k], 0x05040100u);
            *(LAS u32*)(ys + (2 * k + 1) * YS + 4 * pt) = __builtin_amdgcn_perm(y1[k], y0[k], 0x07060302u);
        }
    }
    raw_barrier();
    STAMP(9);
    if (b == 0 && tid == 704) __hip_atomic_store((GAS u32*)a.epoch, ep + 1u, RLX_AGENT);
    u32 hp[5], x2p[5], key = 0;
    if (wave < 8) {
        float s1 = 0.f, s2 = 0.f;
#pragma unroll
        for (int i = 0; i < 5; i++) {
            u32 yp = *(LAS const u32*)(smem + O_YS + wave * YS + 4 * (lane + 64 * i));
            u32 aa = pk_bf16(bflo(yp) + bflo(sk[i]), bfhi(yp) + bfhi(sk[i]));
            hp[i] = pk_bf16(bflo(xp[i]) + bflo(aa), bfhi(xp[i]) + bfhi(aa));
            s1 = dot2bf(hp[i], 0x3f803f80u, s1); s2 = dot2bf(hp[i], hp[i], s2);
        }
        const float mean = wave_sum(s1) * (1.0f / D_);
        float var = fmaf(-mean, mean, wave_sum(s2) * (1.0f / D_));
        if (__builtin_expect(mean * mean > 16.f * var, 0)) {
            float ss = 0.f;
#pragma unroll
            for (int i = 0; i < 5; i++) { float d0 = bflo(hp[i]) - mean, d1 = bfhi(hp[i]) - mean; ss = fmaf(d0, d0, ss); ss = fmaf(d1, d1, ss); }
            var = wave_sum(ss) * (1.0f / D_);
        }
        const float m2 = rbf(mean), sd2 = rbf(__builtin_sqrtf(rbf(rbf(var) + 1e-5f))), r2 = __builtin_amdgcn_rcpf(sd2);
        float racc = 0.f;
#pragma unroll
        for (int i = 0; i < 5; i++) { x2p[i] = ln_pair(hp[i], m2, sd2, r2); racc = dot2bf(x2p[i], wp[i], racc); }
        key = sort_key(rbf(wave_sum(racc)), row);
        if (lane == 0) *(LAS u32*)(smem + O_MISC + 32 + 4 * wave) = key;
    }
    raw_barrier();
    STAMP(14);
    if (wave < 8) {
        u32x4 k0 = *(LAS const u32x4*)(smem + O_MISC + 32), k1 = *(LAS const u32x4*)(smem + O_MISC + 48);
        u32 lbest = max(max(max(k0.x, k0.y), max(k0.z, k0.w)), max(max(k1.x, k1.y), max(k1.z, k1.w)));
        if (key == lbest) {
            auto rc = __builtin_amdgcn_make_buffer_rsrc((void*)(a.cand + (size_t)chunk * CAND_STRIDE), 0, CAND_STRIDE, 0x00020000);
            if (lane < 8) __hip_atomic_store((GAS u64*)(a.keys + lane * 2048 + chunk * 8), ((u64)tag2 << 32) | key, RLX_AGENT);
#pragma unroll
            for (int i = 0; i < 5; i++) __builtin_amdgcn_raw_buffer_store_b64(u32x2{x2p[i], tag2}, rc, (lane + 64 * i) * 8, 0, 16);
#pragma unroll
            for (int i = 0; i < 5; i++) __builtin_amdgcn_raw_buffer_store_b64(u32x2{hp[i], tag2}, rc, 2560 + (lane + 64 * i) * 8, 0, 16);
        }
    }
    b8 wf[4];
    if (is_f1 || is_f2) {
        if (wave >= 9) { asm volatile("s_waitcnt vmcnt(0)" ::: "memory"); lds_signal(flags + FL_W, lane); }
        lds_wait<1>(flags + FL_W, 3);
    }
    if (is_f1) {
        const int w10 = wave < 10 ? wave : 0;
        LAS const char* p = smem + O_WBUF + (64 * w10 + 8 * g4 + (n16 >> 2)) * 64 + (n16 & 3) * 8;
        wf[0] = tr_frag(p, 256); wf[1] = tr_frag(p + 32, 256); wf[2] = tr_frag(p + 2048, 256); wf[3] = tr_frag(p + 2048 + 32, 256);
    } else if (is_f2) {
        LAS const char* p = smem + O_WBUF + (128 * wave + 8 * g4 + (n16 >> 2)) * 32 + (n16 & 3) * 8;
        wf[0] = tr_frag(p, 128); wf[1] = tr_frag(p + 1024, 128); wf[2] = tr_frag(p + 2048, 128); wf[3] = tr_frag(p + 3072, 128);
    }
    if (wave == 11) {
        if (is_f1 || is_f2) __builtin_amdgcn_s_sleep(KDELAY); else __builtin_amdgcn_s_sleep(LAZY);
        u32 best = 0;
        {
            auto rk = __builtin_amdgcn_make_buffer_rsrc((void*)(a.keys + ((b >> 3) & 7) * 2048), 0, 2048, 0x00020000);
            bool nd = true;
            for (u32 spins = 0; spins < SPIN_MAX; spins++) {
                if (nd) {
                    u32x4 k0 = __builtin_amdgcn_raw_buffer_load_b128(rk, lane * 16, 0, 16), k1 = __builtin_amdgcn_raw_buffer_load_b128(rk, 1024 + lane * 16, 0, 16);
                    nd = !(k0.y == tag2 && k0.w == tag2 && k1.y == tag2 && k1.w == tag2);
                    best = max(max(k0.x, k0.z), max(k1.x, k1.z));
                }
                if (!__any(nd)) break;
                __builtin_amdgcn_s_sleep(1);
            }
        }
        best = wave_umax(best);
        STAMP(10);
        if (lane == 0) *(LAS u32*)(smem + O_MISC + 64) = best;
        if (is_f1 || is_f2) {
            const int wrow = 2047 - (int)(best & 0x7ffu);
            auto rc = __builtin_amdgcn_make_buffer_rsrc((void*)(a.cand + (size_t)(wrow >> 3) * CAND_STRIDE), 0, CAND_STRIDE, 0x00020000);
            bool nd = true;
            if (is_f1) {
                u32x4 c[3];
                const int o2 = lane < 32 ? 2048 + lane * 16 : 0;
                for (u32 spins = 0; spins < SPIN_MAX; spins++) {
                    if (nd) {
                        c[0] = __builtin_amdgcn_raw_buffer_load_b128(rc, lane * 16, 0, 16);
                        c[1] = __builtin_amdgcn_raw_buffer_load_b128(rc, 1024 + lane * 16, 0, 16);
                        c[2] = __builtin_amdgcn_raw_buffer_load_b128(rc, o2, 0, 16);
                        nd = !(c[0].y == tag2 && c[0].w == tag2 && c[1].y == tag2 && c[1].w == tag2 && c[2].y == tag2 && c[2].w == tag2);
                    }
                    if (!__any(nd)) break;
                    __builtin_amdgcn_s_sleep(1);
                }
                *(LAS u32x2*)(smem + O_XSB + lane * 8) = u32x2{c[0].x, c[0].z};
                *(LAS u32x2*)(smem + O_XSB + 512 + lane * 8) = u32x2{c[1].x, c[1].z};
                if (lane < 32) *(LAS u32x2*)(smem + O_XSB + 1024 + lane * 8) = u32x2{c[2].x, c[2].z};
            } else {
                u32x2 c;
                const int o = ((lane & 8) ? 2560 : 0) + (f2u * 8 + (lane & 7)) * 8;
                for (u32 spins = 0; spins < SPIN_MAX; spins++) {
                    if (nd) { c = __builtin_amdgcn_raw_buffer_load_b64(rc, o, 0, 16); nd = c.y != tag2; }
                    if (!__any(nd)) break;
                    __builtin_amdgcn_s_sleep(1);
                }
                if (lane < 16) *(LAS u32*)(smem + O_XSEL + lane * 4) = c.x;
            }
        }
    }
    raw_barrier();
    STAMP(11);
    const u32 best = *(LAS const u32*)(smem + O_MISC + 64);
    if (is_f1) {
        if (wave < 10) {
            LAS const char* xa = smem + O_XSB + (64 * wave + 8 * g4) * 2;
            b8 a0 = *(LAS const b8*)xa, a1 = *(LAS const b8*)(xa + 64);
            f4 z = {0, 0, 0, 0};
            f4 c0 = mfma16b(a0, wf[0], z), c1 = mfma16b(a0, wf[1], z);
            c0 = mfma16b(a1, wf[2], c0); c1 = mfma16b(a1, wf[3], c1);
            if (lane < 16) { *(LAS float*)(smem + O_RED + wave * 128 + lane * 4) = c0.x; *(LAS float*)(smem + O_RED + wave * 128 + 64 + lane * 4) = c1.x; }
        }
        raw_barrier();
        if (tid < 8) {
            f4 sm = {0, 0, 0, 0};
#pragma unroll
            for (int w = 0; w < 10; w++) sm += *(LAS const f4*)(smem + O_RED + w * 128 + tid * 16);
            auto rg = __builtin_amdgcn_make_buffer_rsrc((void*)a.gu, 0, 96 * 128, 0x00020000);
            __builtin_amdgcn_raw_buffer_store_b128(u32x4{pk_bf16(sm.x, sm.y), tag2, pk_bf16(sm.z, sm.w), tag2}, rg, f1u * 128 + tid * 16, 0, 16);
        }
    }
    if (wave < 8 && key != best) {
#pragma unroll
        for (int i = 0; i < 5; i++)
            ((GAS u32*)(a.out + (size_t)row * D_))[lane + 64 * i] = pk_bf16(bflo(hp[i]) + bflo(x2p[i]), bfhi(hp[i]) + bfhi(x2p[i]));
    }
    if (is_f2) {
        if (wave >= 6) {
            const int gt = tid - 384;
            auto rg = __builtin_amdgcn_make_buffer_rsrc((void*)a.gu, 0, 96 * 128, 0x00020000);
            __builtin_amdgcn_s_sleep(GDELAY);
            u32x4 g, u;
            bool nd = true;
            for (u32 spins = 0; spins < SPIN_MAX; spins++) {
                if (nd) {
                    g = __builtin_amdgcn_raw_buffer_load_b128(rg, gt * 16, 0, 16);
                    u = __builtin_amdgcn_raw_buffer_load_b128(rg, (384 + gt) * 16, 0, 16);
                    nd = !(g.y == tag2 && g.w == tag2 && u.y == tag2 && u.w == tag2);
                }
                if (!__any(nd)) break;
                __builtin_amdgcn_s_sleep(1);
            }
            float gv[4] = {bflo(g.x), bfhi(g.x), bflo(g.z), bfhi(g.z)}, uv[4] = {bflo(u.x), bfhi(u.x), bflo(u.z), bfhi(u.z)};
            float av[4];
#pragma unroll
            for (int k = 0; k < 4; k++) av[k] = rbf(gv[k] / (1.0f + __expf(-gv[k]))) * uv[k];
            *(LAS u32x2*)(smem + O_ACT + gt * 8) = u32x2{pk_bf16(av[0], av[1]), pk_bf16(av[2], av[3])};
        }
        raw_barrier();
        STAMP(13);
        {
            LAS const char* xa = smem + O_ACT + (128 * wave + 8 * g4) * 2;
            f4 c0 = {0, 0, 0, 0}, c1 = c0;
            c0 = mfma16b(*(LAS const b8*)xa, wf[0], c0); c1 = mfma16b(*(LAS const b8*)(xa + 64), wf[1], c1);
            c0 = mfma16b(*(LAS const b8*)(xa + 128), wf[2], c0); c1 = mfma16b(*(LAS const b8*)(xa + 192), wf[3], c1);
            if (lane < 16) *(LAS float*)(smem + O_RED + wave * 64 + lane * 4) = c0.x + c1.x;
        }
        raw_barrier();
        if (tid < 16) {
            float y = 0.f;
#pragma unroll
            for (int w = 0; w < 12; w++) y += *(LAS const float*)(smem + O_RED + w * 64 + tid * 4);
            y = rbf(y);
            const u32 ku = best >> 16;
            const u32 rb = (ku & 0x8000u) ? (ku & 0x7fffu) : (~ku & 0xffffu);
            const float r = __uint_as_float(rb << 16);
            const float rs = rbf(1.0f / (1.0f + __expf(-r)));
            y = rbf(y * rs);
            const float x2v = bf2f(*(LAS const u16*)(smem + O_XSEL + tid * 2)), hv = bf2f(*(LAS const u16*)(smem + O_XSEL + 32 + tid * 2));
            const float bb = rbf(x2v + y);
            const int wrow = 2047 - (int)(best & 0x7ffu);
            a.out[(size_t)wrow * D_ + f2u * 16 + tid] = (u16)(pk_bf16(hv + bb, 0.f) & 0xffffu);
        }
    }
    asm volatile(".Lk_end:" ::: "memory");
    STAMP(12);
}

extern "C" void kernel_launch(void* const* d_in, const int* in_sizes, int n_in,
                              void* d_out, int out_size, void* d_ws, size_t ws_size,
                              hipStream_t stream) {
    char* ws = (char*)d_ws;
    MainArgs a;
    a.epoch = (u32*)ws;             ws += 256;
    a.ybuf = ws;                    ws += L_ * D_ * 2;
    a.clr = ws;                     ws += 256 * 64;
    a.ubuf = ws;                    ws += L_ * D_ * 2;
    a.xch = ws;                     ws += 256 * XCH_STRIDE;
    a.keys = ws;                    ws += 256 * 64;
    a.cand = ws;                    ws += 256 * CAND_STRIDE;
    a.gu = ws;                      ws += 96 * 128;
    (void)hipFuncSetAttribute((const void*)k_main, hipFuncAttributeMaxDynamicSharedMemorySize, LDS_MAIN);
    a.x = (const u16*)d_in[0]; a.Are = (const u16*)d_in[1]; a.Aim = (const u16*)d_in[2]; a.Bre = (const u16*)d_in[3]; a.Bim = (const u16*)d_in[4];
    a.Cre = (const u16*)d_in[5]; a.Cim = (const u16*)d_in[6]; a.logdt = (const u16*)d_in[7]; a.Dskip = (const u16*)d_in[8];
    a.wr = (const u16*)d_in[10]; a.wg = (const u16*)d_in[11]; a.wu = (const u16*)d_in[12]; a.wd = (const u16*)d_in[13];
    a.out = (u16*)d_out;
    a.dbg = (u64*)((char*)d_ws + (128u << 20));
    k_main<<<256, NT, LDS_MAIN, stream>>>(a);
}
```

```cpp
#include <hip/hip_runtime.h>
#include <stdint.h>

typedef unsigned short u16;
typedef unsigned int u32;
typedef unsigned long long u64;
typedef _Float16 h8 __attribute__((ext_vector_type(8)));
typedef _Float16 h2 __attribute__((ext_vector_type(2)));
typedef __bf16 b2 __attribute__((ext_vector_type(2)));
typedef float f2 __attribute__((ext_vector_type(2)));
typedef float f4 __attribute__((ext_vector_type(4)));
typedef u32 u32x2 __attribute__((ext_vector_type(2)));
typedef u32 u32x3 __attribute__((ext_vector_type(3)));
typedef u32 u32x4 __attribute__((ext_vector_type(4)));
#define LAS __attribute__((address_space(3)))
#define GAS __attribute__((address_space(1)))
#define RLX_AGENT __ATOMIC_RELAXED, __HIP_MEMORY_SCOPE_AGENT

constexpr int L_ = 2048, D_ = 640, N_ = 192, F_ = 1536;

__device__ __forceinline__ float bflo(u32 p) { return __uint_as_float(p << 16); }
__device__ __forceinline__ float bfhi(u32 p) { return __uint_as_float(p & 0xffff0000u); }
__device__ __forceinline__ float bf2f(u16 v) { return __uint_as_float(((u32)v) << 16); }
__device__ __forceinline__ u32 pk_bf16(float a, float b) {
    f2 v = {a, b};
    b2 r = __builtin_convertvector(v, b2);
    return __builtin_bit_cast(u32, r);
}
__device__ __forceinline__ u32 pk_f16(float a, float b) {
    f2 v = {a, b};
    h2 r = __builtin_convertvector(v, h2);
    return __builtin_bit_cast(u32, r);
}
__device__ __forceinline__ float rbf(float a) { return bflo(pk_bf16(a, a)); }
__device__ __forceinline__ float dot2bf(u32 a, u32 b, float c) {
    return __builtin_amdgcn_fdot2_f32_bf16(__builtin_bit_cast(b2, a), __builtin_bit_cast(b2, b), c, false);
}

template <int CTRL, int RM = 0xf, int BM = 0xf>
__device__ __forceinline__ float dpp_get(float v) {
    return __int_as_float(__builtin_amdgcn_update_dpp(0, __float_as_int(v), CTRL, RM, BM, false));
}
template <int CTRL, int RM = 0xf, int BM = 0xf>
__device__ __forceinline__ float dpp_add(float v) { return v + dpp_get<CTRL, RM, BM>(v); }
__device__ __forceinline__ float rdl(float v, int l) { return __int_as_float(__builtin_amdgcn_readlane(__float_as_int(v), l)); }
__device__ __forceinline__ float wave_sum(float v) {
    v = dpp_add<0xB1>(v);
    v = dpp_add<0x4E>(v);
    v = dpp_add<0x141>(v);
    v = dpp_add<0x140>(v);
    v = dpp_add<0x142, 0xa>(v);
    v = dpp_add<0x143, 0xc>(v);
    return rdl(v, 63);
}
template <int CTRL, int RM = 0xf, int BM = 0xf>
__device__ __forceinline__ u32 dpp_umax(u32 v) {
    u32 t = (u32)__builtin_amdgcn_update_dpp(0, (int)v, CTRL, RM, BM, false);
    return v > t ? v : t;
}
__device__ __forceinline__ u32 wave_umax(u32 v) {
    v = dpp_umax<0xB1>(v);
    v = dpp_umax<0x4E>(v);
    v = dpp_umax<0x141>(v);
    v = dpp_umax<0x140>(v);
    v = dpp_umax<0x142, 0xa>(v);
    v = dpp_umax<0x143, 0xc>(v);
    return (u32)__builtin_amdgcn_readlane((int)v, 63);
}
__device__ __forceinline__ float xrow_sum(float v) {
    u32 u = __float_as_uint(v);
    auto p = __builtin_amdgcn_permlane16_swap(u, u, false, false);
    v = __uint_as_float(p[0]) + __uint_as_float(p[1]);
    u = __float_as_uint(v);
    auto q = __builtin_amdgcn_permlane32_swap(u, u, false, false);
    return __uint_as_float(q[0]) + __uint_as_float(q[1]);
}
__device__ __forceinline__ float reduce16(const float (&v)[16], int lane) {
    const bool b3 = lane & 8, b2_ = lane & 4, b1 = lane & 2, b0 = lane & 1;
    float w[8], z[4], y[2];
#pragma unroll
    for (int k = 0; k < 8; k++) { float keep = b3 ? v[k + 8] : v[k], give = b3 ? v[k] : v[k + 8]; w[k] = keep + dpp_get<0x140>(give); }
#pragma unroll
    for (int k = 0; k < 4; k++) { float keep = b2_ ? w[k + 4] : w[k], give = b2_ ? w[k] : w[k + 4]; z[k] = keep + dpp_get<0x141>(give); }
#pragma unroll
    for (int k = 0; k < 2; k++) { float keep = b1 ? z[k + 2] : z[k], give = b1 ? z[k] : z[k + 2]; y[k] = keep + dpp_get<0x1B>(give); }
    float keep = b0 ? y[1] : y[0], give = b0 ? y[0] : y[1];
    float r = keep + dpp_get<0xB1>(give);
    return xrow_sum(r);
}

__device__ __forceinline__ void ln_stats(const float (&x)[10], float& m, float& s) {
    float sum = 0.f;
#pragma unroll
    for (int i = 0; i < 10; i++) sum += x[i];
    float mean = wave_sum(sum) * (1.0f / D_);
    float ss = 0.f;
#pragma unroll
    for (int i = 0; i < 10; i++) { float d = x[i] - mean; ss = fmaf(d, d, ss); }
    float var = wave_sum(ss) * (1.0f / D_);
    m = rbf(mean);
    s = rbf(__builtin_sqrtf(rbf(rbf(var) + 1e-5f)));
}
__device__ __forceinline__ float div_r(float a, float s, float r) {
    float q = a * r;
    float e = fmaf(-q, s, a);
    return fmaf(e, r, q);
}

struct cf { float r, i; };
__device__ __forceinline__ cf cmul(cf a, cf b) {
    cf o;
    o.r = fmaf(a.r, b.r, -(a.i * b.i));
    o.i = fmaf(a.r, b.i, a.i * b.r);
    return o;
}
__device__ __forceinline__ cf cmulc(cf a, cf b) {
    cf o;
    o.r = fmaf(a.r, b.r, a.i * b.i);
    o.i = fmaf(a.i, b.r, -(a.r * b.i));
    return o;
}
__device__ __forceinline__ cf csq(cf a) {
    cf o;
    o.r = fmaf(a.r, a.r, -(a.i * a.i));
    o.i = 2.0f * a.r * a.i;
    return o;
}
__device__ __forceinline__ cf cexp_dir(float alpha, float bh, float bl, float x) {
    float mag = __builtin_amdgcn_exp2f(alpha * x);
    float ph = __builtin_amdgcn_fractf(bh * x);
    ph = fmaf(bl, x, ph);
    cf o;
    o.r = mag * __builtin_amdgcn_cosf(ph);
    o.i = mag * __builtin_amdgcn_sinf(ph);
    return o;
}
__device__ __forceinline__ f4 mfma16(h8 a, h8 b, f4 c) { return __builtin_amdgcn_mfma_f32_16x16x32_f16(a, b, c, 0, 0, 0); }
__device__ __forceinline__ void wave_fence() {
    __builtin_amdgcn_fence(__ATOMIC_ACQ_REL, "wavefront");
    __builtin_amdgcn_wave_barrier();
}
__device__ __forceinline__ void lds_signal(LAS u32* w, int lane) {
    asm volatile("s_waitcnt lgkmcnt(0)" ::: "memory");
    if (lane == 0) __hip_atomic_fetch_add(w, 1u, __ATOMIC_RELAXED, __HIP_MEMORY_SCOPE_WORKGROUP);
    asm volatile("" ::: "memory");
}
template <int SLP = 0>
__device__ __forceinline__ void lds_wait(LAS u32* w, u32 want) {
    asm volatile("" ::: "memory");
    while (__builtin_amdgcn_readfirstlane(__hip_atomic_load(w, __ATOMIC_RELAXED, __HIP_MEMORY_SCOPE_WORKGROUP)) < want)
        __builtin_amdgcn_s_sleep(SLP);
    asm volatile("" ::: "memory");
}

#define TOUCH
constexpr int NT = 768;
#define UDELAY 24
#define LAZY 60
#define YDELAY 10
#define KDELAY 12
#define GDELAY 12
#define WPACE 1
#define HB1 0
#define KSTR 8

constexpr int UIMG_CH = (256 + L_) * 2;
constexpr int O_UIMG = 64;
constexpr int KCS = 4160;
constexpr int KIMG_CH = 4 * KCS;
constexpr int O_KIMG = O_UIMG + 3 * UIMG_CH;
constexpr int SCR = 32 * 272;
constexpr int O_SCR = O_KIMG;
constexpr int O_KTAB = O_SCR + 8 * SCR;
constexpr int KTAB_CH = 656 * 4;
constexpr int O_ACCX = O_KTAB + 3 * KTAB_CH;
constexpr int O_USTG = O_ACCX;
constexpr int URS = 336;
constexpr int O_WBUF = O_ACCX + 2 * 8192;
constexpr int O_FLAG = O_WBUF + 49152;
constexpr int O_MISC = O_FLAG + 64;
constexpr int O_JUNK = O_MISC + 128;
constexpr int LDS_MAIN = O_JUNK + 256;
constexpr int YS = 1280 + 16;
constexpr int O_YS = O_KIMG;
constexpr int O_XSEL = O_YS + 8 * YS;
constexpr int O_XSB = O_XSEL + 2560;
constexpr int O_ACT = O_XSB + 1280;
constexpr int O_RED = O_ACT + 3072;
static_assert(8 * SCR >= 3 * KIMG_CH && LDS_MAIN <= 163840, "lds");
static_assert(O_KIMG % 16 == 0 && O_KTAB % 16 == 0 && O_ACCX % 16 == 0 && O_FLAG % 16 == 0 && O_WBUF % 16 == 0 && O_RED % 16 == 0, "align");
enum { FL_P0 = 0, FL_P1, FL_P2, FL_TAB, FL_KIMG, FL_UIMG, FL_ACC0, FL_ACC1, FL_STAB, FL_SIMG, FL_USTG, FL_XRD, FL_CLR, FL_CONV };

struct PosTab { u16 p[512][8]; };
constexpr int rne_lag(int j) {
    if (j < 256) return j;
    int sh = j < 512 ? 1 : (j < 1024 ? 2 : 3);
    int q = j >> sh, rem = j & ((1 << sh) - 1), half = 1 << (sh - 1);
    if (rem > half || (rem == half && (q & 1))) q++;
    return q << sh;
}
constexpr int ktab_pos_c(int j) {
    if (j < 0 || j >= 2048) return 644;
    int l = rne_lag(j);
    if (l < 256) return l;
    if (l < 512) return 256 + ((l - 256) >> 1);
    if (l < 1024) return 384 + ((l - 512) >> 2);
    return 512 + ((l - 1024) >> 3);
}
constexpr PosTab make_postab() {
    PosTab t{};
    for (int c = 0; c < 512; c++) for (int i = 0; i < 8; i++) t.p[c][i] = (u16)(4 * ktab_pos_c(2047 - 4 * c - i));
    return t;
}
__device__ const __attribute__((aligned(16))) PosTab g_postab = make_postab();

#define STAMP(i)

constexpr int XCH_STRIDE = 5376;
constexpr int CAND_STRIDE = 5120;
struct MainArgs {
    const u16 *x, *Are, *Aim, *Bre, *Bim, *Cre, *Cim, *logdt, *Dskip, *wr, *wg, *wu, *wd;
    u16* out;
    u32* epoch;
    char* xch;
    char* ubuf;
    char* ybuf;
    char* clr;
    char* keys;
    char* cand;
    char* gu;
    u64* dbg;
};
constexpr u32 SPIN_MAX = 1u << 16;

typedef float f16v __attribute__((ext_vector_type(16)));
typedef short s4 __attribute__((ext_vector_type(4)));
typedef __bf16 b8 __attribute__((ext_vector_type(8)));
typedef __amdgpu_buffer_rsrc_t rsrc_t;
__device__ __forceinline__ f2 rot(f2 b) { return f2{-b.y, b.x}; }
__device__ __forceinline__ f2 cm(f2 a, f2 b, f2 rb) { return __builtin_elementwise_fma(a.yy, rb, a.xx * b); }
__device__ __forceinline__ f2 csq2(f2 a) { return f2{fmaf(a.x, a.x, -(a.y * a.y)), 2.0f * a.x * a.y}; }
template <bool CONJ>
__device__ __forceinline__ f2 cexp2(float alpha, float bh, float bl, float x) {
    float mag = __builtin_amdgcn_exp2f(alpha * x);
    float ph = __builtin_amdgcn_fractf(bh * x);
    ph = fmaf(bl, x, ph);
    f2 cs = {__builtin_amdgcn_cosf(ph), __builtin_amdgcn_sinf(CONJ ? -ph : ph)};
    return cs * mag;
}
__device__ __forceinline__ void put(LAS char* sp, int row, f2 v) { *(LAS u32*)(sp + row * 272) = pk_f16(v.x, v.y); }

__device__ __forceinline__ h8 ld_b(LAS const char* ub, int t) { return *(LAS const h8*)(ub + 512 * t); }
__device__ __forceinline__ h8 ld_a(LAS const char* kl, LAS const char* kh, int M) {
    u32x2 lo = *(LAS const u32x2*)(kl + 512 * (7 - M));
    u32x2 hi = *(LAS const u32x2*)(kh + 512 * (7 - M));
    u32x4 a4 = {lo.x, lo.y, hi.x, hi.y};
    return __builtin_bit_cast(h8, a4);
}
#define SB() __builtin_amdgcn_sched_barrier(0)
__device__ __forceinline__ void conv_run(f4 (&acc)[8], LAS const char* ub, LAS const char* kl, LAS const char* kh, int nmu) {
    h8 A[8], B[8], Bn[3];
#pragma unroll
    for (int i = 0; i < 8; i++) { A[i] = ld_a(kl, kh, i); if (i < 5) B[7 - i] = ld_b(ub, 7 - i); if (i < 3) B[i] = ld_b(ub, i); SB(); }
#pragma nounroll
    for (int it = 0; it < nmu; it++) {
        ub -= 64; kl -= 64; kh -= 64;
#pragma unroll
        for (int M = 0; M < 8; M++) {
#pragma unroll
            for (int t = 7 - M; t >= 0; t--) acc[t + M] = mfma16(A[M], B[t], acc[t + M]);
            SB();
            A[M] = ld_a(kl, kh, M);
            if (M < 5) B[7 - M] = ld_b(ub, 7 - M);
            if (M < 3) Bn[M] = ld_b(ub, M);
            if (M >= 5) { B[7 - M] = Bn[7 - M]; asm volatile("" : "+v"(B[7 - M])); }
            SB();
        }
    }
}
template <int TA, int TB>
__device__ __forceinline__ void conv_pair(f4& a0, f4& a1, LAS const char* ub, LAS const char* kl, LAS const char* kh) {
    h8 A[TB + 1], B[TB + 1];
    f4 a2 = {0, 0, 0, 0};
#pragma unroll
    for (int i = 0; i <= TB; i++) { A[i] = ld_a(kl, kh, i); B[TB - i] = ld_b(ub, TB - i); SB(); }
#pragma nounroll
    for (int it = 0; it < 8; it++) {
        ub -= 64; kl -= 64; kh -= 64;
#pragma unroll
        for (int M = 0; M <= TB; M++) {
            if (M & 1) a2 = mfma16(A[M], B[TB - M], a2); else a1 = mfma16(A[M], B[TB - M], a1);
            if (M <= TA) a0 = mfma16(A[M], B[TA - M], a0);
            SB();
            A[M] = ld_a(kl, kh, M); B[TB - M] = ld_b(ub, TB - M);
            SB();
        }
    }
    a1 += a2;
}

__device__ __forceinline__ void store_y_tile(rsrc_t rsy, int ch, int T, int lane, f4 a, float sc) {
    u32x2 o;
    o.x = pk_bf16(a.x * sc, a.y * sc);
    o.y = pk_bf16(a.z * sc, a.w * sc);
    const int n = lane & 15, g = lane >> 4;
    __builtin_amdgcn_raw_buffer_store_b64(o, rsy, (((n >> 1) * D_ + ch) * 64 + (n & 1) * 32 + g * 8) + T * (8 * D_ * 64), 0, 16);
}
__device__ __forceinline__ void raw_barrier() { asm volatile("s_waitcnt lgkmcnt(0)\n\ts_barrier" ::: "memory"); }
__device__ __forceinline__ u32 sort_key(float r, int row) {
    u32 u = __float_as_uint(r) >> 16;
    u = (u & 0x8000u) ? (~u & 0xffffu) : (u | 0x8000u);
    return (u << 16) | (u32)(2047 - row);
}
__device__ __forceinline__ void build_chunk(LAS char* kimg, LAS const char* tab, u32x4 pp, int c) {
    float v[7];
#pragma unroll
    for (int i = 0; i < 7; i++) v[i] = *(LAS const float*)(tab + ((i & 1) ? (pp[i >> 1] >> 16) : (pp[i >> 1] & 0xffffu)));
    u32 p01 = pk_f16(v[0], v[1]), p23 = pk_f16(v[2], v[3]), p45 = pk_f16(v[4], v[5]);
    u32 p12 = pk_f16(v[1], v[2]), p34 = pk_f16(v[3], v[4]), p56 = pk_f16(v[5], v[6]);
    *(LAS u32x2*)(kimg + 0 * KCS + 32 + 8 * c) = u32x2{p01, p23};
    *(LAS u32x2*)(kimg + 1 * KCS + 32 + 8 * c) = u32x2{p12, p34};
    *(LAS u32x2*)(kimg + 2 * KCS + 32 + 8 * c) = u32x2{p23, p45};
    *(LAS u32x2*)(kimg + 3 * KCS + 32 + 8 * c) = u32x2{p34, p56};
}
constexpr u32 CKM = 0x00070007u;
__device__ __forceinline__ u32x4 ck_expect(u32x4 d, u32 e) {
    u32 m0 = d.x & ~CKM, m1 = d.y & ~CKM, m2 = d.z & ~CKM, m3 = d.w & ~CKM;
    u32 s = (m0 + m1) + (m2 + m3) + e;
    s ^= s >> 9;
    u32x4 o;
    o.x = m0 | (s & CKM);
    o.y = m1 | (__builtin_amdgcn_alignbit(s, s, 3) & CKM);
    o.z = m2 | (__builtin_amdgcn_alignbit(s, s, 6) & CKM);
    o.w = m3 | (__builtin_amdgcn_alignbit(s, s, 9) & CKM);
    return o;
}
__device__ __forceinline__ float row16_sum(float v) {
    v = dpp_add<0xB1>(v); v = dpp_add<0x4E>(v); v = dpp_add<0x141>(v); v = dpp_add<0x140>(v);
    return v;
}
__device__ __forceinline__ u32 ln_pair(u32 x, float m, float s, float r) {
    u32 d = pk_bf16(bflo(x) - m, bfhi(x) - m);
    return pk_bf16(div_r(bflo(d), s, r), div_r(bfhi(d), s, r));
}
__device__ __forceinline__ u32x4 norm8(u32x4 x, float m, float s, float r) {
    u32x4 o;
#pragma unroll
    for (int k = 0; k < 4; k++) { u32 u = ln_pair(x[k], m, s, r); o[k] = pk_f16(bflo(u), bfhi(u)); }
    return o;
}
__device__ __forceinline__ void glds16(const void* gsrc, u32 lds_dst) {
    u32 keep;
    asm volatile("s_mov_b32 %0, m0\n\ts_mov_b32 m0, %2\n\ts_nop 0\n\tglobal_load_lds_dwordx4 %1, off\n\ts_mov_b32 m0, %0" : "=&s"(keep) : "v"(gsrc), "s"(lds_dst) : "memory");
}
__device__ __forceinline__ void glds4(const void* gsrc, u32 lds_dst) {
    u32 keep;
    asm volatile("s_mov_b32 %0, m0\n\ts_mov_b32 m0, %2\n\ts_nop 0\n\tglobal_load_lds_dword %1, off\n\ts_mov_b32 m0, %0" : "=&s"(keep) : "v"(gsrc), "s"(lds_dst) : "memory");
}
__device__ __forceinline__ b8 tr_frag(LAS const char* p, int hi_off) {
    s4 lo = __builtin_amdgcn_ds_read_tr16_b64_v4i16((LAS s4*)p);
    s4 hi = __builtin_amdgcn_ds_read_tr16_b64_v4i16((LAS s4*)(p + hi_off));
    u32x2 l2 = __builtin_bit_cast(u32x2, lo), h2_ = __builtin_bit_cast(u32x2, hi);
    return __builtin_bit_cast(b8, u32x4{l2.x, l2.y, h2_.x, h2_.y});
}
__device__ __forceinline__ f4 mfma16b(b8 a, b8 b, f4 c) { return __builtin_amdgcn_mfma_f32_16x16x32_bf16(a, b, c, 0, 0, 0); }

__global__ void __launch_bounds__(NT, 3) k_main(MainArgs a) {
    extern __shared__ __attribute__((aligned(16))) char smem_[];
    LAS char* smem = (LAS char*)smem_;
    const int tid = threadIdx.x, lane = tid & 63;
    u32 warm;
    {
        u64 pc; u32 csz;
        asm volatile("s_getpc_b64 %0\n.Lk_begin:\n\ts_mov_b32 %1, .Lk_end-.Lk_begin" : "=s"(pc), "=s"(csz));
        if (csz > 49152u || csz < 4096u) csz = 128u;
        warm = *(const GAS u32*)((pc & ~127ull) + min((u32)(tid * 64), csz - 128u));
    }
    const int wave = __builtin_amdgcn_readfirstlane(tid >> 6);
    const int b = blockIdx.x;
    const int pairi = (b & 7) * 16 + (b >> 4), side = (b >> 3) & 1;
    const int cbase = 5 * pairi + 2 * side;
    const int ch0 = side ? cbase + 1 : cbase, ch1 = ch0 + 1, chs = 5 * pairi + 2;
    const int chunk = (b & 7) * 32 + (b >> 3);
    LAS u32* flags = (LAS u32*)(smem + O_FLAG);
    STAMP(0);
    const u32 ep = __hip_atomic_load((GAS u32*)a.epoch, RLX_AGENT);
    const rsrc_t rsy = __builtin_amdgcn_make_buffer_rsrc((void*)a.ybuf, 0, 64 * D_ * 64, 0x00020000);
    const int jl = b >> 3;
    const bool is_f1 = jl < 12, is_f2 = jl >= 12 && jl < 17;
    const int f1u = (b & 7) * 12 + jl;
    const int f2u = (b & 7) * 5 + (jl - 12);
    const int n16 = lane & 15, g4 = lane >> 4;

    if (wave < 8) {
        const int slot_g = wave < 3 ? 0 : (wave < 6 ? 1 : 2);
        const int ch_g = slot_g == 0 ? ch0 : (slot_g == 1 ? ch1 : chs);
        int mode; bool act = true;
        if (slot_g < 2) mode = (wave - 3 * slot_g) * 64 + lane;
        else { mode = 96 * side + (wave - 6) * 64 + lane; act = (wave == 6) || (lane < 32); if (!act) mode = 0; }
        const int gi = ch_g * N_ + mode;
        u16 pdt = a.logdt[ch_g];
        u16 pAre = a.Are[gi], pAim = a.Aim[gi], pBre = a.Bre[gi], pBim = a.Bim[gi], pCre = a.Cre[gi], pCim = a.Cim[gi];
        u32x4 pp = *(const GAS u32x4*)(&g_postab.p[tid][0]);
        if (tid < 16) flags[tid] = 0;
        if (tid >= 64 && tid < 160) { int t = tid - 64; *(LAS u32x4*)(smem + O_UIMG + (t >> 5) * UIMG_CH + (t & 31) * 16) = u32x4{0, 0, 0, 0}; }
        raw_barrier();
        float Are = bf2f(pAre), Aim = bf2f(pAim), Bre = bf2f(pBre), Bim = bf2f(pBim), Cre = bf2f(pCre), Cim = bf2f(pCim);
        float dt = rbf(expf(bf2f(pdt)));
        float are = dt * Are, aim = dt * Aim, br = dt * Bre, bi = dt * Bim;
        f2 wc = {fmaf(Cre, br, -(Cim * bi)), -fmaf(Cre, bi, Cim * br)};
        if (!act) wc = f2{0.f, 0.f};
        asm volatile("" :: "v"(warm), "v"(ep), "v"(pp));
        STAMP(1);
        if (lane < 30) {
            const int xk = b & 7, li = wave * 30 + lane;
            const char* p;
            if (li < 120) { const int r = li / 6; p = (const char*)(xk < 4 ? a.wg : a.wu) + (size_t)(20 * jl + r) * (F_ * 2) + 768 * (xk & 3) + (li - 6 * r) * 128; }
            else if (li < 216) { const int l2 = li - 120; p = (const char*)a.wd + (size_t)(48 * jl + (l2 >> 1)) * (D_ * 2) + ((160 * xk) & ~127) + (l2 & 1) * 128; }
            else if (li < 226) p = (const char*)a.Dskip + (li - 216) * 128;
            else p = (const char*)a.wr + (li < 236 ? li - 226 : 0) * 128;
            glds4(p, (u32)O_JUNK);
        }
        float alpha = are * 1.44269504088896341f;
        const float CH = 0.15915494f, CL = 6.4206382e-09f;
        float p_ = aim * CH;
        float pe = fmaf(aim, CL, fmaf(aim, CH, -p_));
        float pf = p_ - __builtin_rintf(p_);
        float bh = __uint_as_float(__float_as_uint(pf + pe) & 0xfffff800u);
        float bl = (pf - bh) + pe;
        float mw = fabsf(wc.x) + fabsf(wc.y);
        float v0r = 16384.f;
        if (__builtin_expect(__any(alpha > 0.f), 0)) {
            float ap = fmaxf(alpha, 0.f);
            float g1 = __builtin_amdgcn_exp2f(ap * 1024.f);
            wc = (wc * g1) * g1;
            mw = (fabsf(wc.x) + fabsf(wc.y)) * __builtin_amdgcn_exp2f(ap * 248.f);
            v0r = __builtin_amdgcn_exp2f(fmaf(ap, -2048.f, 14.f));
        }
        u32 mwu = wave_umax(__float_as_uint(mw));
        int e = (int)(mwu >> 23) - 126;
        if (e < -100) e = -100;
        if (e > 127) e = 127;
        LAS char* scr = smem + O_SCR + wave * SCR;
        LAS char* sp = scr + lane * 4;
        LAS const char* fp = scr + (lane & 31) * 272 + (lane >> 5) * 16;
        {
            f2 E8 = cexp2<true>(alpha, bh, bl, 8.f);
            f2 E16 = csq2(E8), E32 = csq2(E16);
            f2 r8 = rot(E8), r32 = rot(E32);
            f2 Y0 = wc * __builtin_amdgcn_ldexpf(1.0f, 14 - e);
            f2 Y2 = cm(Y0, E16, rot(E16));
            f2 Y1 = cm(Y0, E8, r8), Y3 = cm(Y2, E8, r8);
#pragma unroll
            for (int q = 0; q < 32; q += 4) {
                put(sp, q, Y0); put(sp, q + 1, Y1); put(sp, q + 2, Y2); put(sp, q + 3, Y3);
                if (q < 28) {
                    f2 t0 = Y0.xx * E32, t1 = Y1.xx * E32, t2 = Y2.xx * E32, t3 = Y3.xx * E32;
                    asm volatile("" : "+v"(t0), "+v"(t1), "+v"(t2), "+v"(t3));
                    Y0 = __builtin_elementwise_fma(Y0.yy, r32, t0); Y1 = __builtin_elementwise_fma(Y1.yy, r32, t1);
                    Y2 = __builtin_elementwise_fma(Y2.yy, r32, t2); Y3 = __builtin_elementwise_fma(Y3.yy, r32, t3);
                }
            }
        }
        wave_fence();
        h8 By[8];
#pragma unroll
        for (int k = 0; k < 8; k++) By[k] = *(LAS const h8*)(fp + 32 * k);
        wave_fence();
        {
            f2 E1 = cexp2<false>(alpha, bh, bl, 1.f), E256 = cexp2<false>(alpha, bh, bl, 256.f), E1024 = cexp2<false>(alpha, bh, bl, 1024.f);
            f2 E2 = csq2(E1), E4 = csq2(E2), E512 = csq2(E256), E2048 = csq2(E1024);
            f2 r1 = rot(E1), r256 = rot(E256);
            f2 E768 = cm(E512, E256, r256);
            f2 V2 = E2 * v0r, V4 = E4 * v0r;
            f2 rV4 = rot(V4);
            f2 V6 = cm(E2, V4, rV4);
            put(sp, 0, f2{v0r, 0.f}); put(sp, 1, E1 * v0r); put(sp, 2, V2); put(sp, 3, cm(V2, E1, r1));
            put(sp, 4, V4); put(sp, 5, cm(V4, E1, r1)); put(sp, 6, V6); put(sp, 7, cm(V6, E1, r1));
                put(sp, 8, E256 * v0r); put(sp, 9, cm(V2, E256, r256)); put(sp, 10, cm(V4, E256, r256)); put(sp, 11, cm(V6, E256, r256));
            put(sp, 12, E512 * v0r); put(sp, 13, cm(E512, V4, rV4)); put(sp, 14, E768 * v0r); put(sp, 15, cm(E768, V4, rV4));
            f2 H0 = E1024 * v0r;
            f2 H1 = cm(H0, E256, r256), H2 = cm(H1, E256, r256), H3 = cm(H2, E256, r256);
            put(sp, 16, H0); put(sp, 17, H1); put(sp, 18, H2); put(sp, 19, H3); put(sp, 20, E2048 * v0r);
        }
        wave_fence();
        f16v dd = {0, 0, 0, 0, 0, 0, 0, 0, 0, 0, 0, 0, 0, 0, 0, 0};
#pragma unroll
        for (int k = 0; k < 8; k++) dd = __builtin_amdgcn_mfma_f32_32x32x16_f16(*(LAS const h8*)(fp + 32 * k), By[k], dd, 0, 0, 0);
        wave_fence();
        {
            const float ts = __builtin_amdgcn_ldexpf(1.0f, e - 28);
            const int q = lane & 31, h = lane >> 5;
            *(LAS f4*)(scr + (8 * q + 4 * h) * 4) = f4{dd[0], dd[1], dd[2], dd[3]} * ts;
            if (h == 0) {
                *(LAS f4*)(scr + (256 + 4 * q) * 4) = f4{dd[4], dd[5], dd[6], dd[7]} * ts;
                *(LAS float*)(scr + (512 + q) * 4) = dd[8] * ts;
                *(LAS float*)(scr + (544 + q) * 4) = dd[9] * ts;
                *(LAS float*)(scr + (576 + q) * 4) = dd[10] * ts;
                *(LAS float*)(scr + (608 + q) * 4) = dd[11] * ts;
            } else {
                *(LAS f2*)(scr + (384 + 2 * q) * 4) = f2{dd[4], dd[5]} * ts;
                *(LAS f2*)(scr + (448 + 2 * q) * 4) = f2{dd[6], dd[7]} * ts;
                if (q == 0) {
                    *(LAS f4*)(scr + 640 * 4) = f4{dd[8] * ts, 0.f, 0.f, 0.f};
                    *(LAS u32x2*)(scr + 644 * 4) = u32x2{0u, (u32)e};
                }
            }
        }
        STAMP(2);
        lds_signal(flags + FL_P0 + slot_g, lane);
        const u32 tag = ep * 0x9E3779B1u + 0x7F4A7C15u;
        asm volatile("" :: "v"(tag), "v"(warm));
        lds_wait(flags + FL_P0 + slot_g, slot_g < 2 ? 3 : 2);
        STAMP(3);
        {
            LAS const char* p0 = smem + O_SCR + (3 * slot_g) * SCR;
            LAS char* tab = smem + O_KTAB + slot_g * KTAB_CH;
            int em = max(*(LAS const int*)(p0 + 645 * 4), *(LAS const int*)(p0 + SCR + 645 * 4));
            if (slot_g < 2) {
                const int idx = tid - slot_g * 192;
                em = max(em, *(LAS const int*)(p0 + 2 * SCR + 645 * 4));
                const int ic = idx < 161 ? idx : 161;
                f4 sm = *(LAS const f4*)(p0 + ic * 16) + *(LAS const f4*)(p0 + SCR + ic * 16) + *(LAS const f4*)(p0 + 2 * SCR + ic * 16);
                sm *= __builtin_amdgcn_ldexpf(1.0f, 7 - em);
                if (idx >= 161) sm = f4{0.f, __builtin_amdgcn_ldexpf(1.0f, em - 7), 0.f, 0.f};
                *(LAS f4*)(tab + ic * 16) = sm;
            } else {
                auto rs = __builtin_amdgcn_make_buffer_rsrc((void*)(a.xch + (size_t)b * XCH_STRIDE), 0, XCH_STRIDE, 0x00020000);
                const int i0 = tid - 384, i1 = i0 < 33 ? i0 + 128 : 160;
                f4 s0 = *(LAS const f4*)(p0 + i0 * 16) + *(LAS const f4*)(p0 + SCR + i0 * 16);
                f4 s1 = *(LAS const f4*)(p0 + i1 * 16) + *(LAS const f4*)(p0 + SCR + i1 * 16);
                if (i1 == 160) s1.y = __int_as_float(em);
                *(LAS f4*)(tab + i0 * 16) = s0;
                if (wave == 6) *(LAS f4*)(tab + i1 * 16) = s1;
                lds_signal(flags + FL_TAB, lane);
                __builtin_amdgcn_raw_buffer_store_b128(u32x4{__float_as_uint(s0.x), tag, __float_as_uint(s0.y), tag}, rs, i0 * 16, 0, 16);
                __builtin_amdgcn_raw_buffer_store_b128(u32x4{__float_as_uint(s0.z), tag, __float_as_uint(s0.w), tag}, rs, (162 + i0) * 16, 0, 16);
                if (wave == 6) {
                    __builtin_amdgcn_raw_buffer_store_b128(u32x4{__float_as_uint(s1.x), tag, __float_as_uint(s1.y), tag}, rs, i1 * 16, 0, 16);
                    __builtin_amdgcn_raw_buffer_store_b128(u32x4{__float_as_uint(s1.z), tag, __float_as_uint(s1.w), tag}, rs, (162 + i1) * 16, 0, 16);
                }
            }
            if (slot_g < 2) lds_signal(flags + FL_TAB, lane);
        }
        lds_wait(flags + FL_TAB, 8);
        STAMP(4);
        build_chunk(smem + O_KIMG, smem + O_KTAB, pp, tid);
        if (wave >= 4 || !HB1) build_chunk(smem + O_KIMG + KIMG_CH, smem + O_KTAB + KTAB_CH, pp, tid);
        if (tid < 24) *(LAS u32x4*)(smem + O_KIMG + (tid >> 1) * KCS + 4128 + (tid & 1) * 16) = u32x4{0, 0, 0, 0};
        lds_signal(flags + FL_KIMG, lane);
        STAMP(5);

        if (wave < 4) {
            const int slot = wave >> 1;
            const int s = (3 - n16) & 3;
            const int uoff = 2 * (256 + 16 * n16 - 16 + 8 * g4);
            const int koff = s * KCS + 2 * (2047 - n16 + 8 * g4 - s) - 512 * 7;
            const int mu0 = (wave & 1) ? 5 : 0;
            LAS const char* ub = smem + O_UIMG + slot * UIMG_CH + uoff - 64 * mu0;
            LAS const char* kl = smem + O_KIMG + slot * KIMG_CH + koff - 64 * mu0;
            LAS const char* kh = kl + 8;
            asm volatile("" : "+v"(kh));
            f4 acc[8];
#pragma unroll
            for (int t = 0; t < 8; t++) acc[t] = f4{0, 0, 0, 0};
            LAS char* ax = smem + O_ACCX + slot * 8192 + lane * 16;
            lds_wait<1>(flags + FL_KIMG, 12);
            lds_wait<1>(flags + FL_UIMG, 4);
            __builtin_amdgcn_s_setprio(3);
            STAMP(6);
            conv_run(acc, ub, kl, kh, (wave & 1) ? 3 : 5);
            STAMP(7);
            if ((wave & 1) == 0) {
                lds_wait(flags + FL_ACC0 + slot, 1);
                const float sc = *(LAS const float*)(smem + O_KTAB + slot * KTAB_CH + 645 * 4);
#pragma unroll
                for (int t = 0; t < 8; t++) {
                    f4 o = acc[t] + *(LAS const f4*)(ax + 1024 * t);
                    store_y_tile(rsy, slot ? ch1 : ch0, t, lane, o, sc);
                }
            } else {
#pragma unroll
                for (int t = 0; t < 8; t++) *(LAS f4*)(ax + 1024 * t) = acc[t];
                lds_signal(flags + FL_ACC0 + slot, lane);
                lds_wait(flags + FL_SIMG, 4);
                LAS const char* ub2 = smem + O_UIMG + 2 * UIMG_CH + uoff;
                LAS const char* kl2 = smem + O_KIMG + 2 * KIMG_CH + koff;
                LAS const char* kh2 = kl2 + 8;
                asm volatile("" : "+v"(kh2));
                float sc = *(LAS const float*)(smem + O_KTAB + 2 * KTAB_CH + 645 * 4);
                f4 a0 = {0, 0, 0, 0}, a1 = a0;
                const int sel = side * 2 + slot;
                int ta, tb;
                if (sel == 0) { conv_pair<0, 7>(a0, a1, ub2, kl2, kh2); ta = 0; tb = 7; }
                else if (sel == 1) { conv_pair<3, 4>(a0, a1, ub2, kl2, kh2); ta = 3; tb = 4; }
                else if (sel == 2) { conv_pair<1, 6>(a0, a1, ub2, kl2, kh2); ta = 1; tb = 6; }
                else { conv_pair<2, 5>(a0, a1, ub2, kl2, kh2); ta = 2; tb = 5; }
                store_y_tile(rsy, chs, ta, lane, a0, sc); store_y_tile(rsy, chs, tb, lane, a1, sc);
            }
            __builtin_amdgcn_s_setprio(0);
            lds_signal(flags + FL_CONV, lane);
        } else {
            const int ht = tid - 256;
            LAS char* tab = smem + O_KTAB + 2 * KTAB_CH;
            u32x4 pp2 = *(const GAS u32x4*)(&g_postab.p[ht][0]);
            if (ht < 192) {
                auto rs = __builtin_amdgcn_make_buffer_rsrc((void*)(a.xch + (size_t)(b ^ 8) * XCH_STRIDE), 0, XCH_STRIDE, 0x00020000);
                const int hi = ht < 161 ? ht : 160;
                u32x4 v0, v1, v2;
                bool need = true;
                for (u32 spins = 0; spins < SPIN_MAX; spins++) {
                    if (need) {
                        v0 = __builtin_amdgcn_raw_buffer_load_b128(rs, hi * 16, 0, 16);
                        v1 = __builtin_amdgcn_raw_buffer_load_b128(rs, (162 + hi) * 16, 0, 16);
                        v2 = __builtin_amdgcn_raw_buffer_load_b128(rs, 160 * 16, 0, 16);
                        need = !(v0.y == tag && v0.w == tag && v1.y == tag && v1.w == tag && v2.y == tag && v2.w == tag);
                    }
                    if (!__any(need)) break;
                    __builtin_amdgcn_s_sleep(2);
                }
                int em = max(*(LAS const int*)(tab + 641 * 4), (int)v2.z);
                f4 loc = *(LAS const f4*)(tab + hi * 16);
                lds_signal(flags + FL_XRD, lane);
                lds_wait(flags + FL_XRD, 3);
                f4 rem = {__uint_as_float(v0.x), __uint_as_float(v0.z), __uint_as_float(v1.x), __uint_as_float(v1.z)};
                if (ht < 161) *(LAS f4*)(tab + ht * 16) = (loc + rem) * __builtin_amdgcn_ldexpf(1.0f, 7 - em);
                else if (ht == 161) *(LAS f2*)(tab + 644 * 4) = f2{0.f, __builtin_amdgcn_ldexpf(1.0f, em - 7)};
                lds_signal(flags + FL_STAB, lane);
            }
            STAMP(6);
            lds_wait<2>(flags + FL_STAB, 3);
            build_chunk(smem + O_KIMG + 2 * KIMG_CH, tab, pp, tid);
            build_chunk(smem + O_KIMG + 2 * KIMG_CH, tab, pp2, ht);
            lds_signal(flags + FL_SIMG, lane);
            STAMP(7);
        }
    } else {
        const int hw = wave - 8, ht = tid - 512;
        const int blk = chunk >> 2, cq = chunk & 3;
        const GAS u32x4* xb = (const GAS u32x4*)a.x + (size_t)(blk * 32 + 8 * hw) * 80;
        u32x4 xq[2][5];
#pragma unroll
        for (int i = 0; i < 5; i++) {
            int c = 20 * cq + n16 + 16 * i; if (c >= 80) c -= 80;
            xq[0][i] = xb[g4 * 80 + c]; xq[1][i] = xb[(4 + g4) * 80 + c];
        }
        const int er = (lane >> 2) & 7;
        u32x4 xe = xb[er * 80 + 20 * cq + 16 + (lane & 3)];
        if (hw == 3) {
            const char* p = lane < 62 ? (const char*)a.epoch + (size_t)lane * (128 << 10) : (const char*)(a.out + (size_t)(chunk * 8 + (lane - 62) * 7) * D_);
            glds4(p, (u32)O_JUNK);
        }
        __builtin_amdgcn_s_barrier();
        __builtin_amdgcn_s_setprio(2);
        float mB[2], sB[2], rB[2];
        {
            float s1[2], s2[2];
#pragma unroll
            for (int g2 = 0; g2 < 2; g2++) {
                float a1 = 0.f, a2 = 0.f, b1 = 0.f, b2v = 0.f;
#pragma unroll
                for (int i = 0; i < 5; i++) {
                    a1 = dot2bf(xq[g2][i].x, 0x3f803f80u, a1); a2 = dot2bf(xq[g2][i].x, xq[g2][i].x, a2);
                    b1 = dot2bf(xq[g2][i].y, 0x3f803f80u, b1); b2v = dot2bf(xq[g2][i].y, xq[g2][i].y, b2v);
                    a1 = dot2bf(xq[g2][i].z, 0x3f803f80u, a1); a2 = dot2bf(xq[g2][i].z, xq[g2][i].z, a2);
                    b1 = dot2bf(xq[g2][i].w, 0x3f803f80u, b1); b2v = dot2bf(xq[g2][i].w, xq[g2][i].w, b2v);
                }
                s1[g2] = row16_sum(a1 + b1); s2[g2] = row16_sum(a2 + b2v);
            }
            float mean[2], var[2];
#pragma unroll
            for (int g2 = 0; g2 < 2; g2++) { mean[g2] = s1[g2] * (1.0f / D_); var[g2] = fmaf(-mean[g2], mean[g2], s2[g2] * (1.0f / D_)); }
            if (__builtin_expect(__any(mean[0] * mean[0] > 16.f * var[0] || mean[1] * mean[1] > 16.f * var[1]), 0)) {
#pragma unroll
                for (int g2 = 0; g2 < 2; g2++) {
                    float ss = 0.f;
#pragma unroll
                    for (int i = 0; i < 5; i++)
#pragma unroll
                        for (int k = 0; k < 4; k++) {
                            float d0 = bflo(xq[g2][i][k]) - mean[g2], d1 = bfhi(xq[g2][i][k]) - mean[g2];
                            ss = fmaf(d0, d0, ss); ss = fmaf(d1, d1, ss);
                        }
                    var[g2] = row16_sum(ss) * (1.0f / D_);
                }
            }
#pragma unroll
            for (int g2 = 0; g2 < 2; g2++) {
                mB[g2] = rbf(mean[g2]);
                sB[g2] = rbf(__builtin_sqrtf(rbf(rbf(var[g2]) + 1e-5f)));
                rB[g2] = __builtin_amdgcn_rcpf(sB[g2]);
            }
        }
        STAMP(1);
        LAS char* stg = smem + O_USTG;
        {
            const u32 ms0 = (__float_as_uint(mB[0]) >> 16) | __float_as_uint(sB[0]), ms1 = (__float_as_uint(mB[1]) >> 16) | __float_as_uint(sB[1]);
            if (hw == cq && n16 == 0) { *(LAS u32*)(smem + O_MISC + 4 * g4) = ms0; *(LAS u32*)(smem + O_MISC + 16 + 4 * g4) = ms1; }
            const int src = (er & 3) * 64;
            u32 e0 = (u32)__builtin_amdgcn_ds_bpermute(src, (int)ms0), e1 = (u32)__builtin_amdgcn_ds_bpermute(src, (int)ms1);
            *(LAS u32x4*)(stg + (8 * hw + g4) * URS + n16 * 16) = norm8(xq[0][0], mB[0], sB[0], rB[0]);
            *(LAS u32x4*)(stg + (8 * hw + 4 + g4) * URS + n16 * 16) = norm8(xq[1][0], mB[1], sB[1], rB[1]);
            const u32 em = (er & 4) ? e1 : e0;
            const float se = bfhi(em);
            u32x4 ne = norm8(xe, bflo(em), se, __builtin_amdgcn_rcpf(se));
            if (lane < 32) *(LAS u32x4*)(stg + (8 * hw + er) * URS + (16 + (lane & 3)) * 16) = ne;
        }
        const u32 cke = ep * 0x85EBCA77u + 0x165667B1u;
        asm volatile("" :: "v"(cke), "v"(warm));
        lds_signal(flags + FL_USTG, lane);
        lds_wait(flags + FL_USTG, 4);
        STAMP(2);
        auto rsu = __builtin_amdgcn_make_buffer_rsrc((void*)a.ubuf, 0, 64 * D_ * 64, 0x00020000);
        {
            const int gbase = (blk * D_ + 160 * cq + n16) * 64 + g4 * 16;
            LAS const char* tp = stg + (8 * g4 + (n16 >> 2)) * URS + (n16 & 3) * 8;
#pragma unroll
            for (int ps = 0; ps < 3; ps++) {
                const int cb = hw + 4 * ps;
                if (cb < 10)
                    __builtin_amdgcn_raw_buffer_store_b128(ck_expect(__builtin_bit_cast(u32x4, tr_frag(tp + cb * 32, 4 * URS)), cke), rsu, gbase + cb * 1024, 0, 16);
            }
        }
        __builtin_amdgcn_s_setprio(0);
        STAMP(3);
        if (hw == 3) {
            const u32x4 ones = {~0u, ~0u, ~0u, ~0u};
#pragma unroll
            for (int i = 0; i < 8; i++) {
                const int un = lane + 64 * i;
                __builtin_amdgcn_raw_buffer_store_b128(ones, rsy, ((un >> 3) * D_ + ch0) * 64 + (un & 7) * 16, 0, 16);
            }
#pragma unroll
            for (int i = 0; i < 2; i++) {
                const int un = lane + 64 * i;
                const int tq = un >> 5;
                const int T = side ? (tq == 0 ? 1 : (tq == 1 ? 6 : (tq == 2 ? 2 : 5))) : (tq == 0 ? 0 : (tq == 1 ? 7 : (tq == 2 ? 3 : 4)));
                __builtin_amdgcn_raw_buffer_store_b128(ones, rsy, ((8 * T + ((un >> 2) & 7)) * D_ + chs) * 64 + (un & 3) * 16, 0, 16);
            }
        }
        __builtin_amdgcn_s_sleep(UDELAY);
        STAMP(4);
        {
            int goff[3], loff[3];
#pragma unroll
            for (int ps = 0; ps < 3; ps++) {
                const int un = ht + 256 * ps;
                const int rb = un / 12, w12 = un - rb * 12;
                goff[ps] = (rb * D_ + cbase) * 64 + w12 * 16;
                const int c = w12 >> 2;
                const int slot = side ? (c == 0 ? 2 : c - 1) : c;
                loff[ps] = O_UIMG + slot * UIMG_CH + 512 + rb * 64 + (w12 & 3) * 16;
            }
            u32x4 d[3];
            u32 need = 7;
            for (u32 spins = 0; spins < SPIN_MAX; spins++) {
#pragma unroll
                for (int ps = 0; ps < 3; ps++) if (need & (1u << ps)) d[ps] = __builtin_amdgcn_raw_buffer_load_b128(rsu, goff[ps], 0, 16);
                u32 bad = 0;
#pragma unroll
                for (int ps = 0; ps < 3; ps++) {
                    u32x4 ex = ck_expect(d[ps], cke);
                    if (!(ex.x == d[ps].x && ex.y == d[ps].y && ex.z == d[ps].z && ex.w == d[ps].w)) bad |= 1u << ps;
                }
                bad |= (u32)__builtin_amdgcn_update_dpp(0, (int)bad, 0xB1, 0xf, 0xf, false);
                bad |= (u32)__builtin_amdgcn_update_dpp(0, (int)bad, 0x4E, 0xf, 0xf, false);
                need = bad;
                if (!__any(need != 0)) break;
                __builtin_amdgcn_s_sleep(4);
            }
#pragma unroll
            for (int ps = 0; ps < 3; ps++)
                *(LAS u32x4*)(smem + loff[ps]) = u32x4{d[ps].x & ~CKM, d[ps].y & ~CKM, d[ps].z & ~CKM, d[ps].w & ~CKM};
        }
        lds_signal(flags + FL_UIMG, lane);
        STAMP(5);
        lds_signal(flags + FL_KIMG, lane);
        const u64 want = ((u64)(ep * 0x2545F491u + 0x9E3779B9u) << 32) | (ep * 0xC2B2AE35u + 0x27D4EB2Fu);
        if (hw == 3) {
            asm volatile("s_waitcnt vmcnt(0)" ::: "memory");
            lds_wait<6>(flags + FL_KIMG, 12);
            if (lane == 0) __hip_atomic_store((GAS u64*)(a.clr + b * 64), want, RLX_AGENT);
        }
        if (hw == 2) {
            __builtin_amdgcn_s_sleep(32);
            u32 need = 15;
            for (u32 spins = 0; spins < SPIN_MAX; spins++) {
#pragma unroll
                for (int k = 0; k < 4; k++)
                    if (need & (1u << k)) { if (__hip_atomic_load((GAS u64*)(a.clr + (lane + 64 * k) * 64), RLX_AGENT) == want) need &= ~(1u << k); }
                if (!__any(need != 0)) break;
                __builtin_amdgcn_s_sleep(16);
            }
            if (b == 0 && lane == 0) __hip_atomic_store((GAS u32*)a.epoch, ep + 1u, RLX_AGENT);
            lds_signal(flags + FL_CLR, lane);
        }
        if (is_f1 || is_f2) {
            const int wn = is_f1 ? 10 : 12;
            const int wjs = is_f1 ? 16 * F_ * 2 : 32 * D_ * 2;
            const char* wsrc = is_f1 ? (const char*)((f1u >= 48 ? a.wu : a.wg) + (f1u >= 48 ? f1u - 48 : f1u) * 32 + (lane >> 2) * F_ + (lane & 3) * 8)
                                     : (const char*)(a.wd + f2u * 16 + (lane >> 1) * D_ + (lane & 1) * 8);
            wsrc += hw * wjs;
            for (int i = 0; i < wn; i++) { glds16(wsrc, (u32)(O_WBUF + (hw + 4 * i) * 1024)); wsrc += 4 * wjs; __builtin_amdgcn_s_sleep(WPACE); }
            asm volatile("s_waitcnt vmcnt(0)" ::: "memory");
        }
    }
    const u32 tag2 = ep * 0x9E3779B1u + 0x3C6EF372u;
    const int row = chunk * 8 + (wave & 7);
    u32 xp[5], sk[5], wp[5];
    if (wave < 8) {
        const GAS u32* xr = (const GAS u32*)(a.x + (size_t)row * D_);
        u32 dp[5];
#pragma unroll
        for (int i = 0; i < 5; i++) { xp[i] = xr[lane + 64 * i]; dp[i] = ((const GAS u32*)a.Dskip)[lane + 64 * i]; wp[i] = ((const GAS u32*)a.wr)[lane + 64 * i]; }
        const u32 stp = *(LAS const u32*)(smem + O_MISC + 4 * wave);
        const float m1 = bflo(stp), s1 = bfhi(stp), r1 = __builtin_amdgcn_rcpf(s1);
#pragma unroll
        for (int i = 0; i < 5; i++) {
            u32 u = ln_pair(xp[i], m1, s1, r1);
            sk[i] = pk_bf16(bflo(u) * bflo(dp[i]), bfhi(u) * bfhi(dp[i]));
        }
        asm volatile("s_waitcnt vmcnt(0)" ::: "memory");
    }
    if ((wave >= 4 && wave < 8) || wave == 0) {
        const int pt = wave == 0 ? 256 + lane : tid - 256;
        lds_wait<6>(flags + FL_CLR, 1);
        lds_wait<4>(flags + FL_CONV, 4);
        __builtin_amdgcn_s_sleep(YDELAY);
        STAMP(8);
        const int yo = ((chunk >> 2) * D_ + 2 * pt) * 64 + (chunk & 3) * 16;
        u32x4 y0, y1;
        bool need = true;
        for (u32 spins = 0; spins < SPIN_MAX; spins++) {
            if (need) {
                y0 = __builtin_amdgcn_raw_buffer_load_b128(rsy, yo, 0, 16);
                y1 = __builtin_amdgcn_raw_buffer_load_b128(rsy, yo + 64, 0, 16);
                bool bad = false;
#pragma unroll
                for (int k = 0; k < 4; k++) {
                    bad |= y0[k] >= 0xffff0000u || (u16)y0[k] == 0xffffu;
                    bad |= y1[k] >= 0xffff0000u || (u16)y1[k] == 0xffffu;
                }
                need = bad;
            }
            if (!__any(need)) break;
            __builtin_amdgcn_s_sleep(2);
        }
        LAS char* ys = smem + O_YS;
#pragma unroll
        for (int k = 0; k < 4; k++) {
            *(LAS u32*)(ys + (2 * k) * YS + 4 * pt) = __builtin_amdgcn_perm(y1[k], y0[k], 0x05040100u);
            *(LAS u32*)(ys + (2 * k + 1) * YS + 4 * pt) = __builtin_amdgcn_perm(y1[k], y0[k], 0x07060302u);
        }
    }
    raw_barrier();
    STAMP(9);
    u32 hp[5], x2p[5], key = 0;
    if (wave < 8) {
        float s1 = 0.f, s2 = 0.f;
#pragma unroll
        for (int i = 0; i < 5; i++) {
            u32 yp = *(LAS const u32*)(smem + O_YS + wave * YS + 4 * (lane + 64 * i));
            u32 aa = pk_bf16(bflo(yp) + bflo(sk[i]), bfhi(yp) + bfhi(sk[i]));
            hp[i] = pk_bf16(bflo(xp[i]) + bflo(aa), bfhi(xp[i]) + bfhi(aa));
            s1 = dot2bf(hp[i], 0x3f803f80u, s1); s2 = dot2bf(hp[i], hp[i], s2);
        }
        const float mean = wave_sum(s1) * (1.0f / D_);
        float var = fmaf(-mean, mean, wave_sum(s2) * (1.0f / D_));
        if (__builtin_expect(mean * mean > 16.f * var, 0)) {
            float ss = 0.f;
#pragma unroll
            for (int i = 0; i < 5; i++) { float d0 = bflo(hp[i]) - mean, d1 = bfhi(hp[i]) - mean; ss = fmaf(d0, d0, ss); ss = fmaf(d1, d1, ss); }
            var = wave_sum(ss) * (1.0f / D_);
        }
        const float m2 = rbf(mean), sd2 = rbf(__builtin_sqrtf(rbf(rbf(var) + 1e-5f))), r2 = __builtin_amdgcn_rcpf(sd2);
        float racc = 0.f;
#pragma unroll
        for (int i = 0; i < 5; i++) { x2p[i] = ln_pair(hp[i], m2, sd2, r2); racc = dot2bf(x2p[i], wp[i], racc); }
        key = sort_key(rbf(wave_sum(racc)), row);
        if (lane == 0) *(LAS u32*)(smem + O_MISC + 32 + 4 * wave) = key;
    }
    raw_barrier();
    STAMP(14);
    if (wave < 8) {
        u32x4 k0 = *(LAS const u32x4*)(smem + O_MISC + 32), k1 = *(LAS const u32x4*)(smem + O_MISC + 48);
        u32 lbest = max(max(max(k0.x, k0.y), max(k0.z, k0.w)), max(max(k1.x, k1.y), max(k1.z, k1.w)));
        if (key == lbest) {
            auto rc = __builtin_amdgcn_make_buffer_rsrc((void*)(a.cand + (size_t)chunk * CAND_STRIDE), 0, CAND_STRIDE, 0x00020000);
            if (lane == 0) __hip_atomic_store((GAS u64*)(a.keys + chunk * KSTR), ((u64)tag2 << 32) | key, RLX_AGENT);
#pragma unroll
            for (int i = 0; i < 5; i++) {
                u32x4 c;
                c.x = __builtin_amdgcn_perm(hp[i], x2p[i], 0x05040100u);
                c.z = __builtin_amdgcn_perm(hp[i], x2p[i], 0x07060302u);
                c.y = tag2; c.w = tag2;
                __builtin_amdgcn_raw_buffer_store_b128(c, rc, (lane + 64 * i) * 16, 0, 16);
            }
        }
    }
    b8 wf[4];
    if (is_f1) {
        const int w10 = wave < 10 ? wave : 0;
        LAS const char* p = smem + O_WBUF + (64 * w10 + 8 * g4 + (n16 >> 2)) * 64 + (n16 & 3) * 8;
        wf[0] = tr_frag(p, 256); wf[1] = tr_frag(p + 32, 256); wf[2] = tr_frag(p + 2048, 256); wf[3] = tr_frag(p + 2048 + 32, 256);
    } else if (is_f2) {
        LAS const char* p = smem + O_WBUF + (128 * wave + 8 * g4 + (n16 >> 2)) * 32 + (n16 & 3) * 8;
        wf[0] = tr_frag(p, 128); wf[1] = tr_frag(p + 1024, 128); wf[2] = tr_frag(p + 2048, 128); wf[3] = tr_frag(p + 3072, 128);
    }
    if (wave == 11) {
        if (is_f1 || is_f2) __builtin_amdgcn_s_sleep(KDELAY); else __builtin_amdgcn_s_sleep(LAZY);
        u32 best = 0, need = 15;
        for (u32 spins = 0; spins < SPIN_MAX; spins++) {
#pragma unroll
            for (int k = 0; k < 4; k++)
                if (need & (1u << k)) {
                    u64 v = __hip_atomic_load((GAS u64*)(a.keys + (lane + 64 * k) * KSTR), RLX_AGENT);
                    if ((u32)(v >> 32) == tag2) { need &= ~(1u << k); best = max(best, (u32)v); }
                }
            if (!__any(need != 0)) break;
            __builtin_amdgcn_s_sleep(2);
        }
        best = wave_umax(best);
        STAMP(10);
        if (lane == 0) *(LAS u32*)(smem + O_MISC + 64) = best;
        if (is_f1 || is_f2) {
            const int wrow = 2047 - (int)(best & 0x7ffu);
            auto rc = __builtin_amdgcn_make_buffer_rsrc((void*)(a.cand + (size_t)(wrow >> 3) * CAND_STRIDE), 0, CAND_STRIDE, 0x00020000);
            u32x4 c[5];
            bool nd = true;
            for (u32 spins = 0; spins < SPIN_MAX; spins++) {
                if (nd) {
#pragma unroll
                    for (int i = 0; i < 5; i++) c[i] = __builtin_amdgcn_raw_buffer_load_b128(rc, (lane + 64 * i) * 16, 0, 16);
                    bool ok = true;
#pragma unroll
                    for (int i = 0; i < 5; i++) ok &= c[i].y == tag2 && c[i].w == tag2;
                    nd = !ok;
                }
                if (!__any(nd)) break;
                __builtin_amdgcn_s_sleep(1);
            }
#pragma unroll
            for (int i = 0; i < 5; i++) {
                *(LAS u32x2*)(smem + O_XSEL + (lane + 64 * i) * 8) = u32x2{c[i].x, c[i].z};
                *(LAS u32*)(smem + O_XSB + (lane + 64 * i) * 4) = __builtin_amdgcn_perm(c[i].z, c[i].x, 0x05040100u);
            }
        }
    }
    raw_barrier();
    STAMP(11);
    const u32 best = *(LAS const u32*)(smem + O_MISC + 64);
    if (is_f1) {
        if (wave < 10) {
            LAS const char* xa = smem + O_XSB + (64 * wave + 8 * g4) * 2;
            b8 a0 = *(LAS const b8*)xa, a1 = *(LAS const b8*)(xa + 64);
            f4 z = {0, 0, 0, 0};
            f4 c0 = mfma16b(a0, wf[0], z), c1 = mfma16b(a0, wf[1], z);
            c0 = mfma16b(a1, wf[2], c0); c1 = mfma16b(a1, wf[3], c1);
            if (lane < 16) { *(LAS float*)(smem + O_RED + wave * 128 + lane * 4) = c0.x; *(LAS float*)(smem + O_RED + wave * 128 + 64 + lane * 4) = c1.x; }
        }
        raw_barrier();
        if (tid < 8) {
            f4 sm = {0, 0, 0, 0};
#pragma unroll
            for (int w = 0; w < 10; w++) sm += *(LAS const f4*)(smem + O_RED + w * 128 + tid * 16);
            auto rg = __builtin_amdgcn_make_buffer_rsrc((void*)a.gu, 0, 96 * 128, 0x00020000);
            __builtin_amdgcn_raw_buffer_store_b128(u32x4{pk_bf16(sm.x, sm.y), tag2, pk_bf16(sm.z, sm.w), tag2}, rg, f1u * 128 + tid * 16, 0, 16);
        }
    }
    if (wave < 8 && key != best) {
#pragma unroll
        for (int i = 0; i < 5; i++)
            ((GAS u32*)(a.out + (size_t)row * D_))[lane + 64 * i] = pk_bf16(bflo(hp[i]) + bflo(x2p[i]), bfhi(hp[i]) + bfhi(x2p[i]));
    }
    if (is_f2) {
        if (wave >= 6) {
            const int gt = tid - 384;
            auto rg = __builtin_amdgcn_make_buffer_rsrc((void*)a.gu, 0, 96 * 128, 0x00020000);
            __builtin_amdgcn_s_sleep(GDELAY);
            u32x4 g, u;
            bool nd = true;
            for (u32 spins = 0; spins < SPIN_MAX; spins++) {
                if (nd) {
                    g = __builtin_amdgcn_raw_buffer_load_b128(rg, gt * 16, 0, 16);
                    u = __builtin_amdgcn_raw_buffer_load_b128(rg, (384 + gt) * 16, 0, 16);
                    nd = !(g.y == tag2 && g.w == tag2 && u.y == tag2 && u.w == tag2);
                }
                if (!__any(nd)) break;
                __builtin_amdgcn_s_sleep(1);
            }
            float gv[4] = {bflo(g.x), bfhi(g.x), bflo(g.z), bfhi(g.z)}, uv[4] = {bflo(u.x), bfhi(u.x), bflo(u.z), bfhi(u.z)};
            float av[4];
#pragma unroll
            for (int k = 0; k < 4; k++) av[k] = rbf(gv[k] / (1.0f + __expf(-gv[k]))) * uv[k];
            *(LAS u32x2*)(smem + O_ACT + gt * 8) = u32x2{pk_bf16(av[0], av[1]), pk_bf16(av[2], av[3])};
        }
        raw_barrier();
        STAMP(13);
        {
            LAS const char* xa = smem + O_ACT + (128 * wave + 8 * g4) * 2;
            f4 c0 = {0, 0, 0, 0}, c1 = c0;
            c0 = mfma16b(*(LAS const b8*)xa, wf[0], c0); c1 = mfma16b(*(LAS const b8*)(xa + 64), wf[1], c1);
            c0 = mfma16b(*(LAS const b8*)(xa + 128), wf[2], c0); c1 = mfma16b(*(LAS const b8*)(xa + 192), wf[3], c1);
            if (lane < 16) *(LAS float*)(smem + O_RED + wave * 64 + lane * 4) = c0.x + c1.x;
        }
        raw_barrier();
        if (tid < 16) {
            float y = 0.f;
#pragma unroll
            for (int w = 0; w < 12; w++) y += *(LAS const float*)(smem + O_RED + w * 64 + tid * 4);
            y = rbf(y);
            const u32 ku = best >> 16;
            const u32 rb = (ku & 0x8000u) ? (ku & 0x7fffu) : (~ku & 0xffffu);
            const float r = __uint_as_float(rb << 16);
            const float rs = rbf(1.0f / (1.0f + __expf(-r)));
            y = rbf(y * rs);
            const u32 cv = *(LAS const u32*)(smem + O_XSEL + (f2u * 16 + tid) * 4);
            const float bb = rbf(bflo(cv) + y);
            const int wrow = 2047 - (int)(best & 0x7ffu);
            a.out[(size_t)wrow * D_ + f2u * 16 + tid] = (u16)(pk_bf16(bfhi(cv) + bb, 0.f) & 0xffffu);
        }
    }
    asm volatile(".Lk_end:" ::: "memory");
    STAMP(12);
}

__global__ void __launch_bounds__(768) k_nop(u32* p) { if (p == nullptr) *p = 0; }
extern "C" void kernel_launch(void* const* d_in, const int* in_sizes, int n_in,
                              void* d_out, int out_size, void* d_ws, size_t ws_size,
                              hipStream_t stream) {
    char* ws = (char*)d_ws;
    MainArgs a;
    a.epoch = (u32*)ws;             ws += 256;
    a.ybuf = ws;                    ws += L_ * D_ * 2;
    a.ubuf = ws;                    ws += L_ * D_ * 2;
    a.xch = ws;                     ws += 256 * XCH_STRIDE;
    a.clr = ws;                     ws += 256 * 64;
    a.keys = ws;                    ws += 256 * 64;
    a.cand = ws;                    ws += 256 * CAND_STRIDE;
    a.gu = ws;                      ws += 96 * 128;
    (void)hipFuncSetAttribute((const void*)k_nop, hipFuncAttributeMaxDynamicSharedMemorySize, LDS_MAIN);
    (void)hipFuncSetAttribute((const void*)k_main, hipFuncAttributeMaxDynamicSharedMemorySize, LDS_MAIN);
    a.x = (const u16*)d_in[0]; a.Are = (const u16*)d_in[1]; a.Aim = (const u16*)d_in[2]; a.Bre = (const u16*)d_in[3]; a.Bim = (const u16*)d_in[4];
    a.Cre = (const u16*)d_in[5]; a.Cim = (const u16*)d_in[6]; a.logdt = (const u16*)d_in[7]; a.Dskip = (const u16*)d_in[8];
    a.wr = (const u16*)d_in[10]; a.wg = (const u16*)d_in[11]; a.wu = (const u16*)d_in[12]; a.wd = (const u16*)d_in[13];
    a.out = (u16*)d_out;
    a.dbg = (u64*)((char*)d_ws + (128u << 20));
    k_nop<<<256, 768, LDS_MAIN, stream>>>(a.epoch);
    k_main<<<256, NT, LDS_MAIN, stream>>>(a);
}
```

```cpp
#include <hip/hip_runtime.h>
#include <stdint.h>

typedef unsigned short u16;
typedef unsigned int u32;
typedef unsigned long long u64;
typedef _Float16 h8 __attribute__((ext_vector_type(8)));
typedef _Float16 h2 __attribute__((ext_vector_type(2)));
typedef __bf16 b2 __attribute__((ext_vector_type(2)));
typedef float f2 __attribute__((ext_vector_type(2)));
typedef float f4 __attribute__((ext_vector_type(4)));
typedef u32 u32x2 __attribute__((ext_vector_type(2)));
typedef u32 u32x3 __attribute__((ext_vector_type(3)));
typedef u32 u32x4 __attribute__((ext_vector_type(4)));
#define LAS __attribute__((address_space(3)))
#define GAS __attribute__((address_space(1)))
#define RLX_AGENT __ATOMIC_RELAXED, __HIP_MEMORY_SCOPE_AGENT

constexpr int L_ = 2048, D_ = 640, N_ = 192, F_ = 1536;

__device__ __forceinline__ float bflo(u32 p) { return __uint_as_float(p << 16); }
__device__ __forceinline__ float bfhi(u32 p) { return __uint_as_float(p & 0xffff0000u); }
__device__ __forceinline__ float bf2f(u16 v) { return __uint_as_float(((u32)v) << 16); }
__device__ __forceinline__ u32 pk_bf16(float a, float b) {
    f2 v = {a, b};
    b2 r = __builtin_convertvector(v, b2);
    return __builtin_bit_cast(u32, r);
}
__device__ __forceinline__ u32 pk_f16(float a, float b) {
    f2 v = {a, b};
    h2 r = __builtin_convertvector(v, h2);
    return __builtin_bit_cast(u32, r);
}
__device__ __forceinline__ float rbf(float a) { return bflo(pk_bf16(a, a)); }
__device__ __forceinline__ float dot2bf(u32 a, u32 b, float c) {
    return __builtin_amdgcn_fdot2_f32_bf16(__builtin_bit_cast(b2, a), __builtin_bit_cast(b2, b), c, false);
}

template <int CTRL, int RM = 0xf, int BM = 0xf>
__device__ __forceinline__ float dpp_get(float v) {
    return __int_as_float(__builtin_amdgcn_update_dpp(0, __float_as_int(v), CTRL, RM, BM, false));
}
template <int CTRL, int RM = 0xf, int BM = 0xf>
__device__ __forceinline__ float dpp_add(float v) { return v + dpp_get<CTRL, RM, BM>(v); }
__device__ __forceinline__ float rdl(float v, int l) { return __int_as_float(__builtin_amdgcn_readlane(__float_as_int(v), l)); }
__device__ __forceinline__ float wave_sum(float v) {
    v = dpp_add<0xB1>(v);
    v = dpp_add<0x4E>(v);
    v = dpp_add<0x141>(v);
    v = dpp_add<0x140>(v);
    v = dpp_add<0x142, 0xa>(v);
    v = dpp_add<0x143, 0xc>(v);
    return rdl(v, 63);
}
template <int CTRL, int RM = 0xf, int BM = 0xf>
__device__ __forceinline__ u32 dpp_umax(u32 v) {
    u32 t = (u32)__builtin_amdgcn_update_dpp(0, (int)v, CTRL, RM, BM, false);
    return v > t ? v : t;
}
__device__ __forceinline__ u32 wave_umax(u32 v) {
    v = dpp_umax<0xB1>(v);
    v = dpp_umax<0x4E>(v);
    v = dpp_umax<0x141>(v);
    v = dpp_umax<0x140>(v);
    v = dpp_umax<0x142, 0xa>(v);
    v = dpp_umax<0x143, 0xc>(v);
    return (u32)__builtin_amdgcn_readlane((int)v, 63);
}
__device__ __forceinline__ float div_r(float a, float s, float r) {
    float q = a * r;
    float e = fmaf(-q, s, a);
    return fmaf(e, r, q);
}

__device__ __forceinline__ f4 mfma16(h8 a, h8 b, f4 c) { return __builtin_amdgcn_mfma_f32_16x16x32_f16(a, b, c, 0, 0, 0); }
__device__ __forceinline__ void wave_fence() {
    __builtin_amdgcn_fence(__ATOMIC_ACQ_REL, "wavefront");
    __builtin_amdgcn_wave_barrier();
}
__device__ __forceinline__ void lds_signal(LAS u32* w, int lane) {
    u64 sv;
    asm volatile("s_waitcnt lgkmcnt(0)\n\ts_mov_b64 %0, exec\n\ts_mov_b64 exec, 1\n\tds_add_u32 %1, %2\n\ts_mov_b64 exec, %0"
                 : "=&s"(sv) : "v"((u32)(uintptr_t)w), "v"(1u) : "memory");
}
template <int SLP = 0>
__device__ __forceinline__ void lds_wait(LAS u32* w, u32 want) {
    asm volatile("" ::: "memory");
    while (__builtin_amdgcn_readfirstlane(__hip_atomic_load(w, __ATOMIC_RELAXED, __HIP_MEMORY_SCOPE_WORKGROUP)) < want)
        __builtin_amdgcn_s_sleep(SLP);
    asm volatile("" ::: "memory");
}

constexpr int NT = 768;
constexpr int UDELAY = 24;
constexpr int LAZY = 60;
constexpr int YDELAY = 6;
constexpr int KDELAY = 5;
constexpr int KSTAG = 4;
constexpr int GDELAY = 8;
constexpr int WPACE = 1;

constexpr int UIMG_CH = (256 + L_) * 2;
constexpr int O_UIMG = 64;
constexpr int KCS = 4160;
constexpr int KIMG_CH = 4 * KCS;
constexpr int O_KIMG = O_UIMG + 3 * UIMG_CH;
constexpr int SCR = 32 * 272;
constexpr int O_SCR = O_KIMG;
constexpr int O_KTAB = O_SCR + 8 * SCR;
constexpr int KTAB_CH = 656 * 4;
constexpr int O_ACCX = O_KTAB + 3 * KTAB_CH;
constexpr int O_USTG = O_ACCX;
constexpr int URS = 336;
constexpr int O_WBUF = O_ACCX + 2 * 8192;
constexpr int O_FLAG = O_WBUF + 49152;
constexpr int O_MISC = O_FLAG + 96;
constexpr int O_JUNK = O_MISC + 128;
constexpr int LDS_MAIN = O_JUNK + 256;
constexpr int O_ACC2 = O_KIMG + 3 * KIMG_CH;
static_assert(O_ACC2 + 16384 <= O_KTAB, "acc2");
constexpr int YS = 1280 + 16;
constexpr int O_YS = O_KIMG;
constexpr int O_XSEL = O_YS + 8 * YS;
constexpr int O_XSB = O_XSEL + 2560;
constexpr int O_ACT = O_XSB + 1280;
constexpr int O_RED = O_ACT + 3072;
static_assert(8 * SCR >= 3 * KIMG_CH && LDS_MAIN <= 163840, "lds");
static_assert(O_KIMG % 16 == 0 && O_KTAB % 16 == 0 && O_ACCX % 16 == 0 && O_FLAG % 16 == 0 && O_WBUF % 16 == 0 && O_RED % 16 == 0, "align");
enum { FL_P0 = 0, FL_P1, FL_P2, FL_TAB, FL_KIMG, FL_UIMG, FL_ACC0, FL_ACC1, FL_ACC2, FL_ACC3, FL_STAB, FL_SIMG, FL_USTG, FL_SACC, FL_CLR, FL_CONV, FL_W, FL_BEST };

struct PosTab { u16 p[512][8]; };
constexpr int rne_lag(int j) {
    if (j < 256) return j;
    int sh = j < 512 ? 1 : (j < 1024 ? 2 : 3);
    int q = j >> sh, rem = j & ((1 << sh) - 1), half = 1 << (sh - 1);
    if (rem > half || (rem == half && (q & 1))) q++;
    return q << sh;
}
constexpr int ktab_pos_c(int j) {
    if (j < 0 || j >= 2048) return 644;
    int l = rne_lag(j);
    if (l < 256) return l;
    if (l < 512) return 256 + ((l - 256) >> 1);
    if (l < 1024) return 384 + ((l - 512) >> 2);
    return 512 + ((l - 1024) >> 3);
}
constexpr PosTab make_postab() {
    PosTab t{};
    for (int c = 0; c < 512; c++) for (int i = 0; i < 8; i++) t.p[c][i] = (u16)(4 * ktab_pos_c(2047 - 4 * c - i));
    return t;
}
__device__ const __attribute__((aligned(16))) PosTab g_postab = make_postab();

constexpr int XCH_STRIDE = 5376;
constexpr int CAND_STRIDE = 5120;
struct MainArgs {
    const u16 *x, *Are, *Aim, *Bre, *Bim, *Cre, *Cim, *logdt, *Dskip, *wr, *wg, *wu, *wd;
    u16* out;
    u32* epoch;
    char* xch;
    char* ubuf;
    char* ybuf;
    char* clr;
    char* keys;
    char* cand;
    char* gu;
};
constexpr u32 SPIN_MAX = 1u << 16;

typedef float f16v __attribute__((ext_vector_type(16)));
typedef short s4 __attribute__((ext_vector_type(4)));
typedef __bf16 b8 __attribute__((ext_vector_type(8)));
typedef __amdgpu_buffer_rsrc_t rsrc_t;
__device__ __forceinline__ f2 rot(f2 b) { return f2{-b.y, b.x}; }
__device__ __forceinline__ f2 cm(f2 a, f2 b, f2 rb) { return __builtin_elementwise_fma(a.yy, rb, a.xx * b); }
__device__ __forceinline__ f2 csq2(f2 a) { return f2{fmaf(a.x, a.x, -(a.y * a.y)), 2.0f * a.x * a.y}; }
template <bool CONJ>
__device__ __forceinline__ f2 cexp2(float alpha, float bh, float bl, float x) {
    float mag = __builtin_amdgcn_exp2f(alpha * x);
    float ph = __builtin_amdgcn_fractf(bh * x);
    ph = fmaf(bl, x, ph);
    f2 cs = {__builtin_amdgcn_cosf(ph), __builtin_amdgcn_sinf(CONJ ? -ph : ph)};
    return cs * mag;
}
__device__ __forceinline__ void put(LAS char* sp, int row, f2 v) { *(LAS u32*)(sp + row * 272) = pk_f16(v.x, v.y); }

__device__ __forceinline__ h8 ld_b(LAS const char* ub, int t) { return *(LAS const h8*)(ub + 512 * t); }
__device__ __forceinline__ h8 ld_a(LAS const char* kl, LAS const char* kh, int M) {
    u32x2 lo = *(LAS const u32x2*)(kl + 512 * (7 - M));
    u32x2 hi = *(LAS const u32x2*)(kh + 512 * (7 - M));
    u32x4 a4 = {lo.x, lo.y, hi.x, hi.y};
    return __builtin_bit_cast(h8, a4);
}
#define SB() __builtin_amdgcn_sched_barrier(0)
__device__ __forceinline__ void conv_run(f4 (&acc)[8], LAS const char* ub, LAS const char* kl, LAS const char* kh, int nmu) {
    h8 A[8], B[8], Bn[3];
#pragma unroll
    for (int i = 0; i < 8; i++) { A[i] = ld_a(kl, kh, i); if (i < 5) B[7 - i] = ld_b(ub, 7 - i); if (i < 3) B[i] = ld_b(ub, i); SB(); }
#pragma nounroll
    for (int it = 0; it < nmu; it++) {
        ub -= 64; kl -= 64; kh -= 64;
#pragma unroll
        for (int M = 0; M < 8; M++) {
#pragma unroll
            for (int t = 7 - M; t >= 0; t--) acc[t + M] = mfma16(A[M], B[t], acc[t + M]);
            SB();
            A[M] = ld_a(kl, kh, M);
            if (M < 5) B[7 - M] = ld_b(ub, 7 - M);
            if (M < 3) Bn[M] = ld_b(ub, M);
            if (M >= 5) { B[7 - M] = Bn[7 - M]; asm volatile("" : "+v"(B[7 - M])); }
            SB();
        }
    }
}
template <int T0, int T1, int T2, int T3>
__device__ __forceinline__ void conv_half(f4 (&acc)[4], LAS const char* ub, LAS const char* kl, LAS const char* kh) {
    h8 A[T3 + 1], B[T3 + 1];
#pragma unroll
    for (int i = 0; i <= T3; i++) { A[i] = ld_a(kl, kh, i); B[T3 - i] = ld_b(ub, T3 - i); SB(); }
#pragma unroll
    for (int it = 0; it < 2; it++) {
        ub -= 64; kl -= 64; kh -= 64;
#pragma unroll
        for (int M = 0; M <= T3; M++) {
            acc[3] = mfma16(A[M], B[T3 - M], acc[3]);
            if (M <= T2) acc[2] = mfma16(A[M], B[T2 - M], acc[2]);
            if (M <= T1) acc[1] = mfma16(A[M], B[T1 - M], acc[1]);
            if (M <= T0) acc[0] = mfma16(A[M], B[T0 - M], acc[0]);
            SB();
            if (it == 0) { A[M] = ld_a(kl, kh, M); B[T3 - M] = ld_b(ub, T3 - M); }
            SB();
        }
    }
}

__device__ __forceinline__ void store_y_tile(rsrc_t rsy, int ch, int T, int lane, f4 a, float sc) {
    u32x2 o;
    o.x = pk_bf16(a.x * sc, a.y * sc);
    o.y = pk_bf16(a.z * sc, a.w * sc);
    const int n = lane & 15, g = lane >> 4;
    __builtin_amdgcn_raw_buffer_store_b64(o, rsy, (((n >> 1) * D_ + ch) * 64 + (n & 1) * 32 + g * 8) + T * (8 * D_ * 64), 0, 16);
}
__device__ __forceinline__ void raw_barrier() { asm volatile("s_waitcnt lgkmcnt(0)\n\ts_barrier" ::: "memory"); }
__device__ __forceinline__ u32 sort_key(float r, int row) {
    u32 u = __float_as_uint(r) >> 16;
    u = (u & 0x8000u) ? (~u & 0xffffu) : (u | 0x8000u);
    return (u << 16) | (u32)(2047 - row);
}
__device__ __forceinline__ void build_chunk(LAS char* kimg, LAS const char* tab, u32x4 pp, int c) {
    float v[7];
#pragma unroll
    for (int i = 0; i < 7; i++) v[i] = *(LAS const float*)(tab + ((i & 1) ? (pp[i >> 1] >> 16) : (pp[i >> 1] & 0xffffu)));
    u32 p01 = pk_f16(v[0], v[1]), p23 = pk_f16(v[2], v[3]), p45 = pk_f16(v[4], v[5]);
    u32 p12 = pk_f16(v[1], v[2]), p34 = pk_f16(v[3], v[4]), p56 = pk_f16(v[5], v[6]);
    *(LAS u32x2*)(kimg + 0 * KCS + 32 + 8 * c) = u32x2{p01, p23};
    *(LAS u32x2*)(kimg + 1 * KCS + 32 + 8 * c) = u32x2{p12, p34};
    *(LAS u32x2*)(kimg + 2 * KCS + 32 + 8 * c) = u32x2{p23, p45};
    *(LAS u32x2*)(kimg + 3 * KCS + 32 + 8 * c) = u32x2{p34, p56};
}
constexpr u32 CKM = 0x00070007u;
__device__ __forceinline__ u32x4 ck_expect(u32x4 d, u32 e) {
    u32 m0 = d.x & ~CKM, m1 = d.y & ~CKM, m2 = d.z & ~CKM, m3 = d.w & ~CKM;
    u32 s = (m0 + m1) + (m2 + m3) + e;
    s ^= s >> 9;
    u32x4 o;
    o.x = m0 | (s & CKM);
    o.y = m1 | (__builtin_amdgcn_alignbit(s, s, 3) & CKM);
    o.z = m2 | (__builtin_amdgcn_alignbit(s, s, 6) & CKM);
    o.w = m3 | (__builtin_amdgcn_alignbit(s, s, 9) & CKM);
    return o;
}
__device__ __forceinline__ float row16_sum(float v) {
    v = dpp_add<0xB1>(v); v = dpp_add<0x4E>(v); v = dpp_add<0x141>(v); v = dpp_add<0x140>(v);
    return v;
}
__device__ __forceinline__ u32 ln_pair(u32 x, float m, float s, float r) {
    u32 d = pk_bf16(bflo(x) - m, bfhi(x) - m);
    return pk_bf16(div_r(bflo(d), s, r), div_r(bfhi(d), s, r));
}
__device__ __forceinline__ u32x4 norm8(u32x4 x, float m, float s, float r) {
    u32x4 o;
#pragma unroll
    for (int k = 0; k < 4; k++) { u32 u = ln_pair(x[k], m, s, r); o[k] = pk_f16(bflo(u), bfhi(u)); }
    return o;
}
__device__ __forceinline__ void glds16(const void* gsrc, u32 lds_dst) {
    u32 keep;
    asm volatile("s_mov_b32 %0, m0\n\ts_mov_b32 m0, %2\n\ts_nop 0\n\tglobal_load_lds_dwordx4 %1, off\n\ts_mov_b32 m0, %0" : "=&s"(keep) : "v"(gsrc), "s"(lds_dst) : "memory");
}
__device__ __forceinline__ void glds4(const void* gsrc, u32 lds_dst) {
    u32 keep;
    asm volatile("s_mov_b32 %0, m0\n\ts_mov_b32 m0, %2\n\ts_nop 0\n\tglobal_load_lds_dword %1, off\n\ts_mov_b32 m0, %0" : "=&s"(keep) : "v"(gsrc), "s"(lds_dst) : "memory");
}
__device__ __forceinline__ b8 tr_frag(LAS const char* p, int hi_off) {
    s4 lo = __builtin_amdgcn_ds_read_tr16_b64_v4i16((LAS s4*)p);
    s4 hi = __builtin_amdgcn_ds_read_tr16_b64_v4i16((LAS s4*)(p + hi_off));
    u32x2 l2 = __builtin_bit_cast(u32x2, lo), h2_ = __builtin_bit_cast(u32x2, hi);
    return __builtin_bit_cast(b8, u32x4{l2.x, l2.y, h2_.x, h2_.y});
}
__device__ __forceinline__ f4 mfma16b(b8 a, b8 b, f4 c) { return __builtin_amdgcn_mfma_f32_16x16x32_bf16(a, b, c, 0, 0, 0); }

__global__ void __launch_bounds__(NT, 3) k_main(MainArgs a) {
    extern __shared__ __attribute__((aligned(16))) char smem_[];
    LAS char* smem = (LAS char*)smem_;
    const int tid = threadIdx.x, lane = tid & 63;
    u32 warm;
    {
        u64 pc; u32 csz;
        asm volatile("s_getpc_b64 %0\n.Lk_begin:\n\ts_mov_b32 %1, .Lk_end-.Lk_begin" : "=s"(pc), "=s"(csz));
        if (csz > 49152u || csz < 4096u) csz = 128u;
        warm = *(const GAS u32*)((pc & ~127ull) + min((u32)(tid * 64), csz - 128u));
    }
    const int wave = __builtin_amdgcn_readfirstlane(tid >> 6);
    const int b = blockIdx.x;
    const int pairi = (b & 7) * 16 + (b >> 4), side = (b >> 3) & 1;
    const int cbase = 5 * pairi + 2 * side;
    const int ch0 = side ? cbase + 1 : cbase, ch1 = ch0 + 1, chs = 5 * pairi + 2;
    const int chunk = (b & 7) * 32 + (b >> 3);
    LAS u32* flags = (LAS u32*)(smem + O_FLAG);
    u32 ep;
#define LOAD_EPOCH() asm volatile("s_load_dword %0, %1, 0x0\n\ts_waitcnt lgkmcnt(0)" : "=s"(ep) : "s"(a.epoch) : "memory")
    const rsrc_t rsy = __builtin_amdgcn_make_buffer_rsrc((void*)a.ybuf, 0, 64 * D_ * 64, 0x00020000);
    const int jl = b >> 3;
    const bool is_f1 = jl < 12, is_f2 = jl >= 12 && jl < 17;
    const int f1u = (b & 7) * 12 + jl;
    const int f2u = (b & 7) * 5 + (jl - 12);
    const int n16 = lane & 15, g4 = lane >> 4;
    const int row = chunk * 8 + (wave & 7);
    u32 xp[5], sk[5], wp[5];
    u32 dp_[5];
#define LN2_LOAD() do {                                                                                                       \
        const GAS u32* xr_ = (const GAS u32*)(a.x + (size_t)row * D_);                                                        \
        _Pragma("unroll") for (int i = 0; i < 5; i++) {                                                                       \
            xp[i] = xr_[lane + 64 * i]; dp_[i] = ((const GAS u32*)a.Dskip)[lane + 64 * i]; wp[i] = ((const GAS u32*)a.wr)[lane + 64 * i]; } \
        SB();                                                                                                                 \
    } while (0)
#define LN2_CALC() do {                                                                                                       \
        const u32 stp_ = *(LAS const u32*)(smem + O_MISC + 4 * wave);                                                         \
        const float m1_ = bflo(stp_), s1_ = bfhi(stp_), r1_ = __builtin_amdgcn_rcpf(s1_);                                     \
        _Pragma("unroll") for (int i = 0; i < 5; i++) {                                                                       \
            u32 u_ = ln_pair(xp[i], m1_, s1_, r1_);                                                                           \
            sk[i] = pk_bf16(bflo(u_) * bflo(dp_[i]), bfhi(u_) * bfhi(dp_[i])); }                                              \
        asm volatile("" : "+v"(wp[0]), "+v"(wp[1]), "+v"(wp[2]), "+v"(wp[3]), "+v"(wp[4]));                                    \
    } while (0)

    if (wave < 8) {
        const int slot_g = wave < 3 ? 0 : (wave < 6 ? 1 : 2);
        const int ch_g = slot_g == 0 ? ch0 : (slot_g == 1 ? ch1 : chs);
        int mode; bool act = true;
        if (slot_g < 2) mode = (wave - 3 * slot_g) * 64 + lane;
        else { mode = 96 * side + (wave - 6) * 64 + lane; act = (wave == 6) || (lane < 32); if (!act) mode = 0; }
        const int gi = ch_g * N_ + mode;
        u16 pdt = a.logdt[ch_g];
        u16 pAre = a.Are[gi], pAim = a.Aim[gi], pBre = a.Bre[gi], pBim = a.Bim[gi], pCre = a.Cre[gi], pCim = a.Cim[gi];
        u32x4 pp = *(const GAS u32x4*)(&g_postab.p[tid][0]);
        if (tid < 20) flags[tid] = 0;
        if (tid >= 64 && tid < 160) { int t = tid - 64; *(LAS u32x4*)(smem + O_UIMG + (t >> 5) * UIMG_CH + (t & 31) * 16) = u32x4{0, 0, 0, 0}; }
        LOAD_EPOCH();
        raw_barrier();
        if (wave >= 6) __builtin_amdgcn_s_setprio(1);
        float Are = bf2f(pAre), Aim = bf2f(pAim), Bre = bf2f(pBre), Bim = bf2f(pBim), Cre = bf2f(pCre), Cim = bf2f(pCim);
        float dt;
        {
            const float xl = bf2f(pdt), LH = 1.44269502f, LL = 1.92596303e-08f;
            const float t = xl * LH;
            const float er = fmaf(xl, LL, fmaf(xl, LH, -t));
            const float ex = __builtin_amdgcn_exp2f(t);
            dt = rbf(fmaf(ex, er * 0.693147181f, ex));
        }
        float are = dt * Are, aim = dt * Aim, br = dt * Bre, bi = dt * Bim;
        f2 wc = {fmaf(Cre, br, -(Cim * bi)), -fmaf(Cre, bi, Cim * br)};
        if (!act) wc = f2{0.f, 0.f};
        asm volatile("" :: "v"(warm), "v"(pp));
        {
            const u32x4 ones = {~0u, ~0u, ~0u, ~0u};
            __builtin_amdgcn_raw_buffer_store_b128(ones, rsy, ((tid >> 3) * D_ + ch0) * 64 + (tid & 7) * 16, 0, 16);
            if (wave < 2) {
                const int tq = tid >> 5;
                const int T = side ? (tq == 0 ? 1 : (tq == 1 ? 6 : (tq == 2 ? 2 : 5))) : (tq == 0 ? 0 : (tq == 1 ? 7 : (tq == 2 ? 3 : 4)));
                __builtin_amdgcn_raw_buffer_store_b128(ones, rsy, ((8 * T + ((tid >> 2) & 7)) * D_ + chs) * 64 + (tid & 3) * 16, 0, 16);
            }
        }
        if (lane < 30) {
            const int xk = b & 7, li = min(wave * 30 + lane, 235);
            const char* p;
            if (li < 120) { const int r = li / 6; p = (const char*)(xk < 4 ? a.wg : a.wu) + (size_t)(20 * jl + r) * (F_ * 2) + 768 * (xk & 3) + (li - 6 * r) * 128; }
            else if (li < 216) { const int l2 = li - 120; p = (const char*)a.wd + (size_t)(48 * jl + (l2 >> 1)) * (D_ * 2) + ((160 * xk) & ~127) + (l2 & 1) * 128; }
            else if (li < 226) p = (const char*)a.Dskip + (li - 216) * 128;
            else p = (const char*)a.wr + (li - 226) * 128;
            glds4(p, (u32)O_JUNK);
        }
        float alpha = are * 1.44269504088896341f;
        const float CH = 0.15915494f, CL = 6.4206382e-09f;
        float p_ = aim * CH;
        float pe = fmaf(aim, CL, fmaf(aim, CH, -p_));
        float pf = p_ - __builtin_rintf(p_);
        float bh = __uint_as_float(__float_as_uint(pf + pe) & 0xfffff800u);
        float bl = (pf - bh) + pe;
        float mw = fabsf(wc.x) + fabsf(wc.y);
        float v0r = 16384.f;
        if (__builtin_expect(__any(alpha > 0.f), 0)) {
            float ap = fmaxf(alpha, 0.f);
            float g1 = __builtin_amdgcn_exp2f(ap * 1024.f);
            wc = (wc * g1) * g1;
            mw = (fabsf(wc.x) + fabsf(wc.y)) * __builtin_amdgcn_exp2f(ap * 248.f);
            v0r = __builtin_amdgcn_exp2f(fmaf(ap, -2048.f, 14.f));
        }
        u32 mwu = wave_umax(__float_as_uint(mw));
        int e = (int)(mwu >> 23) - 126;
        if (e < -100) e = -100;
        if (e > 127) e = 127;
        LAS char* scr = smem + O_SCR + wave * SCR;
        LAS char* sp = scr + lane * 4;
        LAS const char* fp = scr + (lane & 31) * 272 + (lane >> 5) * 16;
        {
            f2 E8 = cexp2<true>(alpha, bh, bl, 8.f);
            f2 E16 = csq2(E8), E32 = csq2(E16);
            f2 r8 = rot(E8), r32 = rot(E32);
            f2 Y0 = wc * __builtin_amdgcn_ldexpf(1.0f, 14 - e);
            f2 Y2 = cm(Y0, E16, rot(E16));
            f2 Y1 = cm(Y0, E8, r8), Y3 = cm(Y2, E8, r8);
#pragma unroll
            for (int q = 0; q < 32; q += 4) {
                put(sp, q, Y0); put(sp, q + 1, Y1); put(sp, q + 2, Y2); put(sp, q + 3, Y3);
                if (q < 28) {
                    f2 t0 = Y0.xx * E32, t1 = Y1.xx * E32, t2 = Y2.xx * E32, t3 = Y3.xx * E32;
                    asm volatile("" : "+v"(t0), "+v"(t1), "+v"(t2), "+v"(t3));
                    Y0 = __builtin_elementwise_fma(Y0.yy, r32, t0); Y1 = __builtin_elementwise_fma(Y1.yy, r32, t1);
                    Y2 = __builtin_elementwise_fma(Y2.yy, r32, t2); Y3 = __builtin_elementwise_fma(Y3.yy, r32, t3);
                }
            }
        }
        wave_fence();
        h8 By[8];
#pragma unroll
        for (int k = 0; k < 8; k++) By[k] = *(LAS const h8*)(fp + 32 * k);
        wave_fence();
        {
            f2 E1 = cexp2<false>(alpha, bh, bl, 1.f), E256 = cexp2<false>(alpha, bh, bl, 256.f), E1024 = cexp2<false>(alpha, bh, bl, 1024.f);
            f2 E2 = csq2(E1), E4 = csq2(E2), E512 = csq2(E256), E2048 = csq2(E1024);
            f2 r1 = rot(E1), r256 = rot(E256);
            f2 E768 = cm(E512, E256, r256);
            f2 V2 = E2 * v0r, V4 = E4 * v0r;
            f2 rV4 = rot(V4);
            f2 V6 = cm(E2, V4, rV4);
            put(sp, 0, f2{v0r, 0.f}); put(sp, 1, E1 * v0r); put(sp, 2, V2); put(sp, 3, cm(V2, E1, r1));
            put(sp, 4, V4); put(sp, 5, cm(V4, E1, r1)); put(sp, 6, V6); put(sp, 7, cm(V6, E1, r1));
            put(sp, 8, E256 * v0r); put(sp, 9, cm(V2, E256, r256)); put(sp, 10, cm(V4, E256, r256)); put(sp, 11, cm(V6, E256, r256));
            put(sp, 12, E512 * v0r); put(sp, 13, cm(E512, V4, rV4)); put(sp, 14, E768 * v0r); put(sp, 15, cm(E768, V4, rV4));
            f2 H0 = E1024 * v0r;
            f2 H1 = cm(H0, E256, r256), H2 = cm(H1, E256, r256), H3 = cm(H2, E256, r256);
            put(sp, 16, H0); put(sp, 17, H1); put(sp, 18, H2); put(sp, 19, H3); put(sp, 20, E2048 * v0r);
        }
        wave_fence();
        f16v dd = {0, 0, 0, 0, 0, 0, 0, 0, 0, 0, 0, 0, 0, 0, 0, 0};
#pragma unroll
        for (int k = 0; k < 8; k++) dd = __builtin_amdgcn_mfma_f32_32x32x16_f16(*(LAS const h8*)(fp + 32 * k), By[k], dd, 0, 0, 0);
        wave_fence();
        {
            const float ts = __builtin_amdgcn_ldexpf(1.0f, e - 28);
            const int q = lane & 31, h = lane >> 5;
            *(LAS f4*)(scr + (8 * q + 4 * h) * 4) = f4{dd[0], dd[1], dd[2], dd[3]} * ts;
            if (h == 0) {
                *(LAS f4*)(scr + (256 + 4 * q) * 4) = f4{dd[4], dd[5], dd[6], dd[7]} * ts;
                *(LAS float*)(scr + (512 + q) * 4) = dd[8] * ts;
                *(LAS float*)(scr + (544 + q) * 4) = dd[9] * ts;
                *(LAS float*)(scr + (576 + q) * 4) = dd[10] * ts;
                *(LAS float*)(scr + (608 + q) * 4) = dd[11] * ts;
            } else {
                *(LAS f2*)(scr + (384 + 2 * q) * 4) = f2{dd[4], dd[5]} * ts;
                *(LAS f2*)(scr + (448 + 2 * q) * 4) = f2{dd[6], dd[7]} * ts;
                if (q == 0) {
                    *(LAS f4*)(scr + 640 * 4) = f4{dd[8] * ts, 0.f, 0.f, 0.f};
                    *(LAS u32x2*)(scr + 644 * 4) = u32x2{0u, (u32)e};
                }
            }
        }
        asm volatile("s_waitcnt vmcnt(1)" ::: "memory");
        lds_signal(flags + FL_P0 + slot_g, lane);
        const u32 tag = ep * 0x9E3779B1u + 0x7F4A7C15u;
        lds_wait(flags + FL_P0 + slot_g, slot_g < 2 ? 3 : 2);
        {
            LAS const char* p0 = smem + O_SCR + (3 * slot_g) * SCR;
            LAS char* tab = smem + O_KTAB + slot_g * KTAB_CH;
            int em = max(*(LAS const int*)(p0 + 645 * 4), *(LAS const int*)(p0 + SCR + 645 * 4));
            if (slot_g < 2) {
                const int idx = tid - slot_g * 192;
                em = max(em, *(LAS const int*)(p0 + 2 * SCR + 645 * 4));
                const int ic = idx < 161 ? idx : 161;
                f4 sm = *(LAS const f4*)(p0 + ic * 16) + *(LAS const f4*)(p0 + SCR + ic * 16) + *(LAS const f4*)(p0 + 2 * SCR + ic * 16);
                sm *= __builtin_amdgcn_ldexpf(1.0f, 7 - em);
                if (idx >= 161) sm = f4{0.f, __builtin_amdgcn_ldexpf(1.0f, em - 7), 0.f, 0.f};
                *(LAS f4*)(tab + ic * 16) = sm;
            } else {
                auto rs = __builtin_amdgcn_make_buffer_rsrc((void*)(a.xch + (size_t)b * XCH_STRIDE), 0, XCH_STRIDE, 0x00020000);
                const int i0 = tid - 384, i1 = i0 < 33 ? i0 + 128 : 160;
                f4 s0 = *(LAS const f4*)(p0 + i0 * 16) + *(LAS const f4*)(p0 + SCR + i0 * 16);
                f4 s1 = *(LAS const f4*)(p0 + i1 * 16) + *(LAS const f4*)(p0 + SCR + i1 * 16);
                if (i1 == 160) s1.y = __int_as_float(em);
                *(LAS f4*)(tab + i0 * 16) = s0;
                if (wave == 6) { *(LAS f4*)(tab + i1 * 16) = s1; if (lane == 0) *(LAS int*)(tab + 648 * 4) = em; }
                lds_signal(flags + FL_TAB, lane);
                __builtin_amdgcn_raw_buffer_store_b128(u32x4{__float_as_uint(s0.x), tag, __float_as_uint(s0.y), tag}, rs, i0 * 16, 0, 16);
                __builtin_amdgcn_raw_buffer_store_b128(u32x4{__float_as_uint(s0.z), tag, __float_as_uint(s0.w), tag}, rs, (162 + i0) * 16, 0, 16);
                if (wave == 6) {
                    __builtin_amdgcn_raw_buffer_store_b128(u32x4{__float_as_uint(s1.x), tag, __float_as_uint(s1.y), tag}, rs, i1 * 16, 0, 16);
                    __builtin_amdgcn_raw_buffer_store_b128(u32x4{__float_as_uint(s1.z), tag, __float_as_uint(s1.w), tag}, rs, (162 + i1) * 16, 0, 16);
                }
            }
            if (slot_g < 2) lds_signal(flags + FL_TAB, lane);
        }
        __builtin_amdgcn_s_setprio(0);
        lds_wait(flags + FL_TAB, 8);
        if (tid < 8) __hip_atomic_store((GAS u64*)(a.clr + tid * 2048 + b * 8), ((u64)(ep * 0x2545F491u + 0x9E3779B9u) << 32) | (ep * 0xC2B2AE35u + 0x27D4EB2Fu), RLX_AGENT);
        build_chunk(smem + O_KIMG, smem + O_KTAB, pp, tid);
        build_chunk(smem + O_KIMG + KIMG_CH, smem + O_KTAB + KTAB_CH, pp, tid);
        if (tid < 24) *(LAS u32x4*)(smem + O_KIMG + (tid >> 1) * KCS + 4128 + (tid & 1) * 16) = u32x4{0, 0, 0, 0};
        lds_signal(flags + FL_KIMG, lane);

        LN2_LOAD();
        const int sK = (3 - n16) & 3;
        const int uoff = 2 * (256 + 16 * n16 - 16 + 8 * g4);
        const int koff = sK * KCS + 2 * (2047 - n16 + 8 * g4 - sK) - 512 * 7;
        if (wave < 4) {
            const int slot = wave >> 1, odd = wave & 1;
            LAS const char* ub = smem + O_UIMG + slot * UIMG_CH + uoff - 256 * odd;
            LAS const char* kl = smem + O_KIMG + slot * KIMG_CH + koff - 256 * odd;
            LAS const char* kh = kl + 8;
            asm volatile("" : "+v"(kh));
            f4 acc[8];
#pragma unroll
            for (int t = 0; t < 8; t++) acc[t] = f4{0, 0, 0, 0};
            int yoff = ((n16 >> 1) * D_ + (slot ? ch1 : ch0)) * 64 + (n16 & 1) * 32 + g4 * 8 + odd * (4 * 8 * D_ * 64);
            asm volatile("" : "+v"(yoff));
            lds_wait<1>(flags + FL_KIMG, 12);
            lds_wait<1>(flags + FL_UIMG, 4);
            __builtin_amdgcn_s_setprio(2);
            conv_run(acc, ub, kl, kh, 4);
            LAS char* axw = smem + O_ACCX + wave * 4096 + lane * 16;
            LAS const char* axr = smem + O_ACCX + (wave ^ 1) * 4096 + lane * 16;
            if (odd) {
#pragma unroll
                for (int t = 0; t < 4; t++) { *(LAS f4*)(axw + 1024 * t) = acc[t]; acc[t] = acc[t + 4]; }
            } else {
#pragma unroll
                for (int t = 0; t < 4; t++) *(LAS f4*)(axw + 1024 * t) = acc[t + 4];
            }
            lds_signal(flags + FL_ACC0 + wave, lane);
            const float sc = *(LAS const float*)(smem + O_KTAB + slot * KTAB_CH + 645 * 4);
            lds_wait(flags + FL_ACC0 + (wave ^ 1), 1);
#pragma unroll
            for (int t = 0; t < 4; t++) {
                f4 o = (acc[t] + *(LAS const f4*)(axr + 1024 * t)) * sc;
                __builtin_amdgcn_raw_buffer_store_b64(u32x2{pk_bf16(o.x, o.y), pk_bf16(o.z, o.w)}, rsy, yoff, t * (8 * D_ * 64), 16);
            }
            {
                LAS const char* a2 = smem + O_ACC2 + lane * 16 + wave * 4096;
                lds_wait(flags + FL_SACC, 4);
                const float sc2 = *(LAS const float*)(smem + O_KTAB + 2 * KTAB_CH + 645 * 4);
                f4 o = (*(LAS const f4*)(a2) + *(LAS const f4*)(a2 + 1024)) + (*(LAS const f4*)(a2 + 2048) + *(LAS const f4*)(a2 + 3072));
                const int T = side ? (wave == 0 ? 1 : (wave == 1 ? 2 : (wave == 2 ? 5 : 6))) : (wave == 0 ? 0 : (wave == 1 ? 3 : (wave == 2 ? 4 : 7)));
                store_y_tile(rsy, chs, T, lane, o, sc2);
            }
            __builtin_amdgcn_s_setprio(0);
            lds_signal(flags + FL_CONV, lane);
            LN2_CALC();
        } else {
            const int w4 = wave - 4;
            LAS const char* ub = smem + O_UIMG + 2 * UIMG_CH + uoff - 128 * w4;
            LAS const char* kl = smem + O_KIMG + 2 * KIMG_CH + koff - 128 * w4;
            LAS const char* kh = kl + 8;
            asm volatile("" : "+v"(kh));
            f4 acc[4];
#pragma unroll
            for (int t = 0; t < 4; t++) acc[t] = f4{0, 0, 0, 0};
            lds_wait<1>(flags + FL_SIMG, 4);
            lds_wait<1>(flags + FL_KIMG, 12);
            __builtin_amdgcn_s_setprio(3);
            if (side) conv_half<1, 2, 5, 6>(acc, ub, kl, kh); else conv_half<0, 3, 4, 7>(acc, ub, kl, kh);
            LAS char* a2 = smem + O_ACC2 + lane * 16;
#pragma unroll
            for (int t = 0; t < 4; t++) *(LAS f4*)(a2 + (t * 4 + w4) * 1024) = acc[t];
            lds_signal(flags + FL_SACC, lane);
            __builtin_amdgcn_s_setprio(0);
            LN2_CALC();
        }
    } else {
        const int hw = wave - 8, ht = tid - 512;
        const int blk = chunk >> 2, cq = chunk & 3;
        const GAS u32x4* xb = (const GAS u32x4*)a.x + (size_t)(blk * 32 + 8 * hw) * 80;
        u32x4 xq[2][5];
#pragma unroll
        for (int i = 0; i < 5; i++) {
            int c = 20 * cq + n16 + 16 * i; if (c >= 80) c -= 80;
            xq[0][i] = xb[g4 * 80 + c]; xq[1][i] = xb[(4 + g4) * 80 + c];
        }
        const int er = (lane >> 2) & 7;
        u32x4 xe = xb[er * 80 + 20 * cq + 16 + (lane & 3)];
        LOAD_EPOCH();
        __builtin_amdgcn_s_barrier();
        __builtin_amdgcn_s_setprio(2);
        float mB[2], sB[2], rB[2];
        {
            float s1[2], s2[2];
#pragma unroll
            for (int g2 = 0; g2 < 2; g2++) {
                float a1 = 0.f, a2 = 0.f, b1 = 0.f, b2v = 0.f;
#pragma unroll
                for (int i = 0; i < 5; i++) {
                    a1 = dot2bf(xq[g2][i].x, 0x3f803f80u, a1); a2 = dot2bf(xq[g2][i].x, xq[g2][i].x, a2);
                    b1 = dot2bf(xq[g2][i].y, 0x3f803f80u, b1); b2v = dot2bf(xq[g2][i].y, xq[g2][i].y, b2v);
                    a1 = dot2bf(xq[g2][i].z, 0x3f803f80u, a1); a2 = dot2bf(xq[g2][i].z, xq[g2][i].z, a2);
                    b1 = dot2bf(xq[g2][i].w, 0x3f803f80u, b1); b2v = dot2bf(xq[g2][i].w, xq[g2][i].w, b2v);
                }
                s1[g2] = row16_sum(a1 + b1); s2[g2] = row16_sum(a2 + b2v);
            }
            float mean[2], var[2];
#pragma unroll
            for (int g2 = 0; g2 < 2; g2++) { mean[g2] = s1[g2] * (1.0f / D_); var[g2] = fmaf(-mean[g2], mean[g2], s2[g2] * (1.0f / D_)); }
            if (__builtin_expect(__any(mean[0] * mean[0] > 16.f * var[0] || mean[1] * mean[1] > 16.f * var[1]), 0)) {
#pragma unroll
                for (int g2 = 0; g2 < 2; g2++) {
                    float ss = 0.f;
#pragma unroll
                    for (int i = 0; i < 5; i++)
#pragma unroll
                        for (int k = 0; k < 4; k++) {
                            float d0 = bflo(xq[g2][i][k]) - mean[g2], d1 = bfhi(xq[g2][i][k]) - mean[g2];
                            ss = fmaf(d0, d0, ss); ss = fmaf(d1, d1, ss);
                        }
                    var[g2] = row16_sum(ss) * (1.0f / D_);
                }
            }
#pragma unroll
            for (int g2 = 0; g2 < 2; g2++) {
                mB[g2] = rbf(mean[g2]);
                sB[g2] = rbf(__builtin_sqrtf(rbf(rbf(var[g2]) + 1e-5f)));
                rB[g2] = __builtin_amdgcn_rcpf(sB[g2]);
            }
        }
        LAS char* stg = smem + O_USTG;
        {
            const u32 ms0 = (__float_as_uint(mB[0]) >> 16) | __float_as_uint(sB[0]), ms1 = (__float_as_uint(mB[1]) >> 16) | __float_as_uint(sB[1]);
            if (hw == cq && n16 == 0) { *(LAS u32*)(smem + O_MISC + 4 * g4) = ms0; *(LAS u32*)(smem + O_MISC + 16 + 4 * g4) = ms1; }
            const int src = (er & 3) * 64;
            u32 e0 = (u32)__builtin_amdgcn_ds_bpermute(src, (int)ms0), e1 = (u32)__builtin_amdgcn_ds_bpermute(src, (int)ms1);
            *(LAS u32x4*)(stg + (8 * hw + g4) * URS + n16 * 16) = norm8(xq[0][0], mB[0], sB[0], rB[0]);
            *(LAS u32x4*)(stg + (8 * hw + 4 + g4) * URS + n16 * 16) = norm8(xq[1][0], mB[1], sB[1], rB[1]);
            const u32 em = (er & 4) ? e1 : e0;
            const float se = bfhi(em);
            u32x4 ne = norm8(xe, bflo(em), se, __builtin_amdgcn_rcpf(se));
            if (lane < 32) *(LAS u32x4*)(stg + (8 * hw + er) * URS + (16 + (lane & 3)) * 16) = ne;
        }
        asm volatile("" :: "v"(warm));
        const u32 cke = ep * 0x85EBCA77u + 0x165667B1u;
        lds_signal(flags + FL_USTG, lane);
        lds_wait(flags + FL_USTG, 4);
        auto rsu = __builtin_amdgcn_make_buffer_rsrc((void*)a.ubuf, 0, 64 * D_ * 64, 0x00020000);
        {
            const int gbase = (blk * D_ + 160 * cq + n16) * 64 + g4 * 16;
            LAS const char* tp = stg + (8 * g4 + (n16 >> 2)) * URS + (n16 & 3) * 8;
#pragma unroll
            for (int ps = 0; ps < 3; ps++) {
                const int cb = hw + 4 * ps;
                if (cb < 10)
                    __builtin_amdgcn_raw_buffer_store_b128(ck_expect(__builtin_bit_cast(u32x4, tr_frag(tp + cb * 32, 4 * URS)), cke), rsu, gbase + cb * 1024, 0, 16);
            }
        }
        __builtin_amdgcn_s_setprio(0);
        u32x4 ppa = *(const GAS u32x4*)(&g_postab.p[ht][0]), ppb = *(const GAS u32x4*)(&g_postab.p[ht + 256][0]);
        __builtin_amdgcn_s_sleep(UDELAY);
        {
            int goff[3], loff[3];
#pragma unroll
            for (int ps = 0; ps < 3; ps++) {
                const int un = ht + 256 * ps;
                const int rb = un / 12, w12 = un - rb * 12;
                goff[ps] = (rb * D_ + cbase) * 64 + w12 * 16;
                const int c = w12 >> 2;
                const int slot = side ? (c == 0 ? 2 : c - 1) : c;
                loff[ps] = O_UIMG + slot * UIMG_CH + 512 + rb * 64 + (w12 & 3) * 16;
            }
            u32x4 d[3];
            u32 need = 7;
            for (u32 spins = 0; spins < SPIN_MAX; spins++) {
#pragma unroll
                for (int ps = 0; ps < 3; ps++) if (need & (1u << ps)) d[ps] = __builtin_amdgcn_raw_buffer_load_b128(rsu, goff[ps], 0, 16);
                u32 bad = 0;
#pragma unroll
                for (int ps = 0; ps < 3; ps++) {
                    u32x4 ex = ck_expect(d[ps], cke);
                    if (!(ex.x == d[ps].x && ex.y == d[ps].y && ex.z == d[ps].z && ex.w == d[ps].w)) bad |= 1u << ps;
                }
                bad |= (u32)__builtin_amdgcn_update_dpp(0, (int)bad, 0xB1, 0xf, 0xf, false);
                bad |= (u32)__builtin_amdgcn_update_dpp(0, (int)bad, 0x4E, 0xf, 0xf, false);
                need = bad;
                if (!__any(need != 0)) break;
                __builtin_amdgcn_s_sleep(4);
            }
#pragma unroll
            for (int ps = 0; ps < 3; ps++)
                *(LAS u32x4*)(smem + loff[ps]) = u32x4{d[ps].x & ~CKM, d[ps].y & ~CKM, d[ps].z & ~CKM, d[ps].w & ~CKM};
        }
        lds_signal(flags + FL_UIMG, lane);
        lds_signal(flags + FL_KIMG, lane);
        {
            __builtin_amdgcn_s_setprio(3);
            LAS char* tab = smem + O_KTAB + 2 * KTAB_CH;
            const u32 tag = ep * 0x9E3779B1u + 0x7F4A7C15u;
            if (ht < 192) {
                auto rs = __builtin_amdgcn_make_buffer_rsrc((void*)(a.xch + (size_t)(b ^ 8) * XCH_STRIDE), 0, XCH_STRIDE, 0x00020000);
                const int hi = ht < 161 ? ht : 160;
                u32x4 v0, v1, v2;
                bool need = true;
                for (u32 spins = 0; spins < SPIN_MAX; spins++) {
                    if (need) {
                        v0 = __builtin_amdgcn_raw_buffer_load_b128(rs, hi * 16, 0, 16);
                        v1 = __builtin_amdgcn_raw_buffer_load_b128(rs, (162 + hi) * 16, 0, 16);
                        v2 = __builtin_amdgcn_raw_buffer_load_b128(rs, 160 * 16, 0, 16);
                        need = !(v0.y == tag && v0.w == tag && v1.y == tag && v1.w == tag && v2.y == tag && v2.w == tag);
                    }
                    if (!__any(need)) break;
                    __builtin_amdgcn_s_sleep(1);
                }
                lds_wait<1>(flags + FL_TAB, 8);
                int em = max(*(LAS const int*)(tab + 648 * 4), (int)v2.z);
                f4 loc = *(LAS const f4*)(tab + hi * 16);
                f4 rem = {__uint_as_float(v0.x), __uint_as_float(v0.z), __uint_as_float(v1.x), __uint_as_float(v1.z)};
                if (ht < 161) *(LAS f4*)(tab + ht * 16) = (loc + rem) * __builtin_amdgcn_ldexpf(1.0f, 7 - em);
                else if (ht == 161) *(LAS f2*)(tab + 644 * 4) = f2{0.f, __builtin_amdgcn_ldexpf(1.0f, em - 7)};
                lds_signal(flags + FL_STAB, lane);
            }
            lds_wait<1>(flags + FL_STAB, 3);
            build_chunk(smem + O_KIMG + 2 * KIMG_CH, tab, ppa, ht);
            build_chunk(smem + O_KIMG + 2 * KIMG_CH, tab, ppb, ht + 256);
            lds_signal(flags + FL_SIMG, lane);
            __builtin_amdgcn_s_setprio(0);
        }
        if (hw == 0) {
            const u64 want = ((u64)(ep * 0x2545F491u + 0x9E3779B9u) << 32) | (ep * 0xC2B2AE35u + 0x27D4EB2Fu);
            auto rk = __builtin_amdgcn_make_buffer_rsrc((void*)(a.clr + ((b >> 3) & 7) * 2048), 0, 2048, 0x00020000);
            const u32 wl = (u32)want, wh = (u32)(want >> 32);
            bool nd = true;
            for (u32 spins = 0; spins < SPIN_MAX; spins++) {
                if (nd) {
                    u32x4 k0 = __builtin_amdgcn_raw_buffer_load_b128(rk, lane * 16, 0, 16), k1 = __builtin_amdgcn_raw_buffer_load_b128(rk, 1024 + lane * 16, 0, 16);
                    nd = !(k0.x == wl && k0.y == wh && k0.z == wl && k0.w == wh && k1.x == wl && k1.y == wh && k1.z == wl && k1.w == wh);
                }
                if (!__any(nd)) break;
                __builtin_amdgcn_s_sleep(8);
            }
            lds_signal(flags + FL_CLR, lane);
        }
        if ((is_f1 || is_f2) && hw > 0) {
            const int wn = is_f1 ? 40 : 48;
            const int wjs = is_f1 ? 16 * F_ * 2 : 32 * D_ * 2;
            const char* wsrc = is_f1 ? (const char*)((f1u >= 48 ? a.wu : a.wg) + (f1u >= 48 ? f1u - 48 : f1u) * 32 + (lane >> 2) * F_ + (lane & 3) * 8)
                                     : (const char*)(a.wd + f2u * 16 + (lane >> 1) * D_ + (lane & 1) * 8);
            wsrc += (hw - 1) * wjs;
            for (int i = hw - 1; i < wn; i += 3) { glds16(wsrc, (u32)(O_WBUF + i * 1024)); wsrc += 3 * wjs; __builtin_amdgcn_s_sleep(WPACE); }
        }
    }
    const u32 tag2 = ep * 0x9E3779B1u + 0x3C6EF372u;
    if (wave >= 4 && wave < 9) {
        const int pt = tid - 256;
        lds_wait<6>(flags + FL_CLR, 1);
        lds_wait<1>(flags + FL_CONV, 4);
        __builtin_amdgcn_s_sleep(YDELAY);
        const int yo = ((chunk >> 2) * D_ + 2 * pt) * 64 + (chunk & 3) * 16;
        u32x4 y0, y1;
        bool need = true;
        for (u32 spins = 0; spins < SPIN_MAX; spins++) {
            if (need) {
                y0 = __builtin_amdgcn_raw_buffer_load_b128(rsy, yo, 0, 16);
                y1 = __builtin_amdgcn_raw_buffer_load_b128(rsy, yo + 64, 0, 16);
                bool bad = false;
#pragma unroll
                for (int k = 0; k < 4; k++) {
                    bad |= y0[k] >= 0xffff0000u || (u16)y0[k] == 0xffffu;
                    bad |= y1[k] >= 0xffff0000u || (u16)y1[k] == 0xffffu;
                }
                need = bad;
            }
            if (!__any(need)) break;
            __builtin_amdgcn_s_sleep(2);
        }
        LAS char* ys = smem + O_YS;
#pragma unroll
        for (int k = 0; k < 4; k++) {
            *(LAS u32*)(ys + (2 * k) * YS + 4 * pt) = __builtin_amdgcn_perm(y1[k], y0[k], 0x05040100u);
            *(LAS u32*)(ys + (2 * k + 1) * YS + 4 * pt) = __builtin_amdgcn_perm(y1[k], y0[k], 0x07060302u);
        }
    }
    raw_barrier();
    if (b == 0 && tid == 704) __hip_atomic_store((GAS u32*)a.epoch, ep + 1u, RLX_AGENT);
    u32 hp[5], x2p[5], key = 0;
    if (wave < 8) {
        float s1 = 0.f, s2 = 0.f;
#pragma unroll
        for (int i = 0; i < 5; i++) {
            u32 yp = *(LAS const u32*)(smem + O_YS + wave * YS + 4 * (lane + 64 * i));
            u32 aa = pk_bf16(bflo(yp) + bflo(sk[i]), bfhi(yp) + bfhi(sk[i]));
            hp[i] = pk_bf16(bflo(xp[i]) + bflo(aa), bfhi(xp[i]) + bfhi(aa));
            s1 = dot2bf(hp[i], 0x3f803f80u, s1); s2 = dot2bf(hp[i], hp[i], s2);
        }
        const float mean = wave_sum(s1) * (1.0f / D_);
        float var = fmaf(-mean, mean, wave_sum(s2) * (1.0f / D_));
        if (__builtin_expect(mean * mean > 16.f * var, 0)) {
            float ss = 0.f;
#pragma unroll
            for (int i = 0; i < 5; i++) { float d0 = bflo(hp[i]) - mean, d1 = bfhi(hp[i]) - mean; ss = fmaf(d0, d0, ss); ss = fmaf(d1, d1, ss); }
            var = wave_sum(ss) * (1.0f / D_);
        }
        const float m2 = rbf(mean), sd2 = rbf(__builtin_sqrtf(rbf(rbf(var) + 1e-5f))), r2 = __builtin_amdgcn_rcpf(sd2);
        float racc = 0.f;
#pragma unroll
        for (int i = 0; i < 5; i++) { x2p[i] = ln_pair(hp[i], m2, sd2, r2); racc = dot2bf(x2p[i], wp[i], racc); }
        key = sort_key(rbf(wave_sum(racc)), row);
        if (lane == 0) *(LAS u32*)(smem + O_MISC + 32 + 4 * wave) = key;
    }
    raw_barrier();
    if (wave < 8) {
        u32x4 k0 = *(LAS const u32x4*)(smem + O_MISC + 32), k1 = *(LAS const u32x4*)(smem + O_MISC + 48);
        u32 lbest = max(max(max(k0.x, k0.y), max(k0.z, k0.w)), max(max(k1.x, k1.y), max(k1.z, k1.w)));
        if (key == lbest) {
            auto rc = __builtin_amdgcn_make_buffer_rsrc((void*)(a.cand + (size_t)chunk * CAND_STRIDE), 0, CAND_STRIDE, 0x00020000);
            if (lane < 8) __hip_atomic_store((GAS u64*)(a.keys + lane * 2048 + chunk * 8), ((u64)tag2 << 32) | key, RLX_AGENT);
#pragma unroll
            for (int i = 0; i < 5; i++) __builtin_amdgcn_raw_buffer_store_b64(u32x2{x2p[i], tag2}, rc, (lane + 64 * i) * 8, 0, 16);
#pragma unroll
            for (int i = 0; i < 5; i++) __builtin_amdgcn_raw_buffer_store_b64(u32x2{hp[i], tag2}, rc, 2560 + (lane + 64 * i) * 8, 0, 16);
        }
    }
    b8 wf[4];
    if (is_f1 || is_f2) {
        if (wave >= 9) { asm volatile("s_waitcnt vmcnt(0)" ::: "memory"); lds_signal(flags + FL_W, lane); }
        lds_wait<1>(flags + FL_W, 3);
    }
    if (is_f1) {
        const int w10 = wave < 10 ? wave : 0;
        LAS const char* p = smem + O_WBUF + (64 * w10 + 8 * g4 + (n16 >> 2)) * 64 + (n16 & 3) * 8;
        wf[0] = tr_frag(p, 256); wf[1] = tr_frag(p + 32, 256); wf[2] = tr_frag(p + 2048, 256); wf[3] = tr_frag(p + 2048 + 32, 256);
    } else if (is_f2) {
        LAS const char* p = smem + O_WBUF + (128 * wave + 8 * g4 + (n16 >> 2)) * 32 + (n16 & 3) * 8;
        wf[0] = tr_frag(p, 128); wf[1] = tr_frag(p + 1024, 128); wf[2] = tr_frag(p + 2048, 128); wf[3] = tr_frag(p + 3072, 128);
    }
    if (wave >= 10) {
        if (is_f1 || is_f2) { __builtin_amdgcn_s_sleep(KDELAY); if (wave == 11) __builtin_amdgcn_s_sleep(KSTAG); } else __builtin_amdgcn_s_sleep(LAZY);
        u32 best = 0;
        bool mine = false;
        if (wave == 10 || is_f1 || is_f2) {
            auto rk = __builtin_amdgcn_make_buffer_rsrc((void*)(a.keys + ((b >> 3) & 7) * 2048), 0, 2048, 0x00020000);
            bool nd = true;
            for (u32 spins = 0; spins < SPIN_MAX; spins++) {
                if (nd) {
                    u32x4 k0 = __builtin_amdgcn_raw_buffer_load_b128(rk, lane * 16, 0, 16), k1 = __builtin_amdgcn_raw_buffer_load_b128(rk, 1024 + lane * 16, 0, 16);
                    nd = !(k0.y == tag2 && k0.w == tag2 && k1.y == tag2 && k1.w == tag2);
                    best = max(max(k0.x, k0.z), max(k1.x, k1.z));
                }
                if (!__any(nd)) break;
                if (__builtin_amdgcn_readfirstlane(__hip_atomic_load(flags + FL_BEST, __ATOMIC_RELAXED, __HIP_MEMORY_SCOPE_WORKGROUP))) break;
            }
            {
                u32 tk = 1;
                if (lane == 0) tk = __hip_atomic_fetch_add(flags + FL_BEST, 1u, __ATOMIC_RELAXED, __HIP_MEMORY_SCOPE_WORKGROUP);
                mine = __builtin_amdgcn_readfirstlane(tk) == 0;
            }
        }
      if (mine) {
        best = wave_umax(best);
        if (lane == 0) *(LAS u32*)(smem + O_MISC + 64) = best;
        if (is_f1 || is_f2) {
            const int wrow = 2047 - (int)(best & 0x7ffu);
            auto rc = __builtin_amdgcn_make_buffer_rsrc((void*)(a.cand + (size_t)(wrow >> 3) * CAND_STRIDE), 0, CAND_STRIDE, 0x00020000);
            bool nd = true;
            if (is_f1) {
                u32x4 c[3];
                const int o2 = lane < 32 ? 2048 + lane * 16 : 0;
                for (u32 spins = 0; spins < SPIN_MAX; spins++) {
                    if (nd) {
                        c[0] = __builtin_amdgcn_raw_buffer_load_b128(rc, lane * 16, 0, 16);
                        c[1] = __builtin_amdgcn_raw_buffer_load_b128(rc, 1024 + lane * 16, 0, 16);
                        c[2] = __builtin_amdgcn_raw_buffer_load_b128(rc, o2, 0, 16);
                        nd = !(c[0].y == tag2 && c[0].w == tag2 && c[1].y == tag2 && c[1].w == tag2 && c[2].y == tag2 && c[2].w == tag2);
                    }
                    if (!__any(nd)) break;
                    __builtin_amdgcn_s_sleep(1);
                }
                *(LAS u32x2*)(smem + O_XSB + lane * 8) = u32x2{c[0].x, c[0].z};
                *(LAS u32x2*)(smem + O_XSB + 512 + lane * 8) = u32x2{c[1].x, c[1].z};
                if (lane < 32) *(LAS u32x2*)(smem + O_XSB + 1024 + lane * 8) = u32x2{c[2].x, c[2].z};
            } else {
                u32x2 c;
                const int o = ((lane & 8) ? 2560 : 0) + (f2u * 8 + (lane & 7)) * 8;
                for (u32 spins = 0; spins < SPIN_MAX; spins++) {
                    if (nd) { c = __builtin_amdgcn_raw_buffer_load_b64(rc, o, 0, 16); nd = c.y != tag2; }
                    if (!__any(nd)) break;
                    __builtin_amdgcn_s_sleep(1);
                }
                if (lane < 16) *(LAS u32*)(smem + O_XSEL + lane * 4) = c.x;
            }
        }
      }
    }
    raw_barrier();
    const u32 best = *(LAS const u32*)(smem + O_MISC + 64);
    if (is_f1) {
        if (wave < 10) {
            LAS const char* xa = smem + O_XSB + (64 * wave + 8 * g4) * 2;
            b8 a0 = *(LAS const b8*)xa, a1 = *(LAS const b8*)(xa + 64);
            f4 z = {0, 0, 0, 0};
            f4 c0 = mfma16b(a0, wf[0], z), c1 = mfma16b(a0, wf[1], z);
            c0 = mfma16b(a1, wf[2], c0); c1 = mfma16b(a1, wf[3], c1);
            if (lane < 16) { *(LAS float*)(smem + O_RED + wave * 128 + lane * 4) = c0.x; *(LAS float*)(smem + O_RED + wave * 128 + 64 + lane * 4) = c1.x; }
        }
        raw_barrier();
        if (tid < 8) {
            f4 sm = {0, 0, 0, 0};
#pragma unroll
            for (int w = 0; w < 10; w++) sm += *(LAS const f4*)(smem + O_RED + w * 128 + tid * 16);
            auto rg = __builtin_amdgcn_make_buffer_rsrc((void*)a.gu, 0, 96 * 128, 0x00020000);
            __builtin_amdgcn_raw_buffer_store_b128(u32x4{pk_bf16(sm.x, sm.y), tag2, pk_bf16(sm.z, sm.w), tag2}, rg, f1u * 128 + tid * 16, 0, 16);
        }
    }
    if (wave < 8 && key != best) {
#pragma unroll
        for (int i = 0; i < 5; i++)
            ((GAS u32*)(a.out + (size_t)row * D_))[lane + 64 * i] = pk_bf16(bflo(hp[i]) + bflo(x2p[i]), bfhi(hp[i]) + bfhi(x2p[i]));
    }
    if (is_f2) {
        if (wave >= 6) {
            const int gt = tid - 384;
            auto rg = __builtin_amdgcn_make_buffer_rsrc((void*)a.gu, 0, 96 * 128, 0x00020000);
            __builtin_amdgcn_s_sleep(GDELAY);
            u32x4 g, u;
            bool nd = true;
            for (u32 spins = 0; spins < SPIN_MAX; spins++) {
                if (nd) {
                    g = __builtin_amdgcn_raw_buffer_load_b128(rg, gt * 16, 0, 16);
                    u = __builtin_amdgcn_raw_buffer_load_b128(rg, (384 + gt) * 16, 0, 16);
                    nd = !(g.y == tag2 && g.w == tag2 && u.y == tag2 && u.w == tag2);
                }
                if (!__any(nd)) break;
                __builtin_amdgcn_s_sleep(1);
            }
            float gv[4] = {bflo(g.x), bfhi(g.x), bflo(g.z), bfhi(g.z)}, uv[4] = {bflo(u.x), bfhi(u.x), bflo(u.z), bfhi(u.z)};
            float av[4];
#pragma unroll
            for (int k = 0; k < 4; k++) av[k] = rbf(gv[k] / (1.0f + __expf(-gv[k]))) * uv[k];
            *(LAS u32x2*)(smem + O_ACT + gt * 8) = u32x2{pk_bf16(av[0], av[1]), pk_bf16(av[2], av[3])};
        }
        raw_barrier();
        {
            LAS const char* xa = smem + O_ACT + (128 * wave + 8 * g4) * 2;
            f4 c0 = {0, 0, 0, 0}, c1 = c0;
            c0 = mfma16b(*(LAS const b8*)xa, wf[0], c0); c1 = mfma16b(*(LAS const b8*)(xa + 64), wf[1], c1);
            c0 = mfma16b(*(LAS const b8*)(xa + 128), wf[2], c0); c1 = mfma16b(*(LAS const b8*)(xa + 192), wf[3], c1);
            if (lane < 16) *(LAS float*)(smem + O_RED + wave * 64 + lane * 4) = c0.x + c1.x;
        }
        raw_barrier();
        if (tid < 16) {
            float y = 0.f;
#pragma unroll
            for (int w = 0; w < 12; w++) y += *(LAS const float*)(smem + O_RED + w * 64 + tid * 4);
            y = rbf(y);
            const u32 ku = best >> 16;
            const u32 rb = (ku & 0x8000u) ? (ku & 0x7fffu) : (~ku & 0xffffu);
            const float r = __uint_as_float(rb << 16);
            const float rs = rbf(1.0f / (1.0f + __expf(-r)));
            y = rbf(y * rs);
            const float x2v = bf2f(*(LAS const u16*)(smem + O_XSEL + tid * 2)), hv = bf2f(*(LAS const u16*)(smem + O_XSEL + 32 + tid * 2));
            const float bb = rbf(x2v + y);
            const int wrow = 2047 - (int)(best & 0x7ffu);
            a.out[(size_t)wrow * D_ + f2u * 16 + tid] = (u16)(pk_bf16(hv + bb, 0.f) & 0xffffu);
        }
    }
    asm volatile(".Lk_end:" ::: "memory");
}

extern "C" void kernel_launch(void* const* d_in, const int* in_sizes, int n_in,
                              void* d_out, int out_size, void* d_ws, size_t ws_size,
                              hipStream_t stream) {
    char* ws = (char*)d_ws;
    MainArgs a;
    a.epoch = (u32*)ws;             ws += 256;
    a.ybuf = ws;                    ws += L_ * D_ * 2;
    a.clr = ws;                     ws += 256 * 64;
    a.ubuf = ws;                    ws += L_ * D_ * 2;
    a.xch = ws;                     ws += 256 * XCH_STRIDE;
    a.keys = ws;                    ws += 256 * 64;
    a.cand = ws;                    ws += 256 * CAND_STRIDE;
    a.gu = ws;                      ws += 96 * 128;
    (void)hipFuncSetAttribute((const void*)k_main, hipFuncAttributeMaxDynamicSharedMemorySize, LDS_MAIN);
    a.x = (const u16*)d_in[0]; a.Are = (const u16*)d_in[1]; a.Aim = (const u16*)d_in[2]; a.Bre = (const u16*)d_in[3]; a.Bim = (const u16*)d_in[4];
    a.Cre = (const u16*)d_in[5]; a.Cim = (const u16*)d_in[6]; a.logdt = (const u16*)d_in[7]; a.Dskip = (const u16*)d_in[8];
    a.wr = (const u16*)d_in[10]; a.wg = (const u16*)d_in[11]; a.wu = (const u16*)d_in[12]; a.wd = (const u16*)d_in[13];
    a.out = (u16*)d_out;
    k_main<<<256, NT, LDS_MAIN, stream>>>(a);
}
```
